```python
import torch

def _softmax(x, axis=-1):
    return torch.softmax(x, dim=axis)
def _gelu(x):
    return torch.nn.functional.gelu(x, approximate="tanh")
def _silu(x):
    return torch.nn.functional.silu(x)
def _sigmoid(x):
    return torch.sigmoid(x)
def _layernorm(x, axis=-1, eps=1e-5):
    m = x.mean(dim=axis, keepdim=True)
    v = x.var(dim=axis, keepdim=True, unbiased=False)
    return (x - m) / torch.sqrt(v + eps)
def _scatter_add(tgt, idx, src, axis=0):
    return tgt.clone().index_add_(axis, idx, src)
def _segment_sum(src, seg, n):
    out = torch.zeros((n,) + src.shape[1:], dtype=src.dtype)
    return out.index_add_(0, seg, src)
def _pad(x, pw):
    flat = tuple(v for pair in reversed(pw) for v in pair)
    return torch.nn.functional.pad(x, flat)

kernel_name = 'parallel_sum__spectral_gfnet__mixer_token_mlp__prompt'

def setup_inputs():

    _w = (lambda a: (lambda t: t.to(torch.bfloat16) if t.is_floating_point() else t)(torch.as_tensor(a)))
    def _A_setup():
        B, S, D = 1, 2048, 576
        Df = D // 2 + 1
        return {
            "x": _w((lambda *s: torch.randn(*s, dtype=torch.bfloat16) * 0.05)(B, S, D)),
            "w_r": _w((lambda *s: torch.randn(*s, dtype=torch.bfloat16) * 3.2)(S, Df) * 0.02),
            "w_i": _w((lambda *s: torch.randn(*s, dtype=torch.bfloat16) * 3.2)(S, Df) * 0.02),
        }
    _A_in = _A_setup()

    def _B_setup():
        B, S, D, F = 1, 2048, 576, 4608
        return {
            "x": _w((lambda *s: torch.randn(*s, dtype=torch.bfloat16) * 0.05)(B, S, D)),
            "w1": _w((lambda *s: torch.randn(*s, dtype=torch.bfloat16) * 0.4)(S, F) * (S ** -0.5)),
            "b1": _w((lambda *s: torch.randn(*s, dtype=torch.bfloat16) * 3.2)(F) * 0.02),
            "w2": _w((lambda *s: torch.randn(*s, dtype=torch.bfloat16) * 0.8)(F, S) * (F ** -0.5)),
            "b2": _w((lambda *s: torch.randn(*s, dtype=torch.bfloat16) * 3.2)(S) * 0.02),
        }
    _B_in = _B_setup()

    return {
        "A_x": _A_in["x"],
        "A_w_r": _A_in["w_r"],
        "A_w_i": _A_in["w_i"],
        "B_x": _B_in["x"],
        "B_w1": _B_in["w1"],
        "B_b1": _B_in["b1"],
        "B_w2": _B_in["w2"],
        "B_b2": _B_in["b2"],
    }

def reference(A_x, A_w_r, A_w_i, B_x, B_w1, B_b1, B_w2, B_b2):
    def _A_ref(x, w_r, w_i):
        B, S, D = x.shape
        X = (lambda x, axes=(-2,-1): torch.fft.rfft2(x.float(), dim=axes))(x, axes=(-2, -1))
        W = w_r + 1j * w_i
        X = X * W
        y = (lambda x, s, axes=(-2,-1): torch.fft.irfft2(x, s=s, dim=axes).to(torch.bfloat16))(X, (S, D), axes=(-2, -1))
        return y

    def _B_ref(x, w1, b1, w2, b2):
        B, S, D = x.shape
        h = _layernorm(x, axis=-1)
        u = torch.einsum("bsd,sf->bdf", h, w1) + b1
        u = _gelu(u)
        y = torch.einsum("bdf,fs->bds", u, w2) + b2
        return (lambda x, *d: x.permute(*d))(y, 0, 2, 1)

    x = A_x
    xn = _layernorm(x, axis=-1)
    return x + _A_ref(xn, A_w_r, A_w_i) + _B_ref(xn, B_w1, B_b1, B_w2, B_b2)
```

```cpp
#include <hip/hip_runtime.h>
#include <hip/hip_bf16.h>
#include <stdint.h>

typedef unsigned short u16;
constexpr int S = 2048, D = 576, DF = 289, F = 4608;

__device__ __forceinline__ float bf2f(u16 v) { return __uint_as_float(((unsigned)v) << 16); }
__device__ __forceinline__ u16 f2bf(float f) { __hip_bfloat16 b = __float2bfloat16(f); return *reinterpret_cast<u16*>(&b); }
__device__ __forceinline__ float rbf(float f) { return bf2f(f2bf(f)); }

__device__ float block_sum(float v, float* red) {
  for (int o = 32; o > 0; o >>= 1) v += __shfl_xor(v, o);
  __syncthreads();
  if ((threadIdx.x & 63) == 0) red[threadIdx.x >> 6] = v;
  __syncthreads();
  float s = 0; for (int i = 0; i < (int)(blockDim.x >> 6); ++i) s += red[i];
  return s;
}

__global__ void ln_k(const u16* x, float* xn, u16* h) {
  __shared__ float red[16];
  int s = blockIdx.x, d = threadIdx.x;
  float v = bf2f(x[s * D + d]);
  float mean = block_sum(v, red) / D;
  float dv = v - mean;
  float var = block_sum(dv * dv, red) / D;
  float m = rbf(mean), vv = rbf(var);
  float den = rbf(sqrtf(rbf(vv + 1e-5f)));
  float a = rbf(rbf(v - m) / den);
  xn[s * D + d] = a;
  mean = block_sum(a, red) / D;
  dv = a - mean;
  var = block_sum(dv * dv, red) / D;
  m = rbf(mean); vv = rbf(var);
  den = rbf(sqrtf(rbf(vv + 1e-5f)));
  float b = rbf(rbf(a - m) / den);
  h[s * D + d] = f2bf(b);
}

__global__ void rowdft_k(const float* xn, float2* Z) {
  __shared__ float xs[D]; __shared__ float2 tw[D];
  int s = blockIdx.x;
  for (int i = threadIdx.x; i < D; i += blockDim.x) { xs[i] = xn[s * D + i]; float sn, cs; sincospif(2.0f * i / D, &sn, &cs); tw[i] = make_float2(cs, -sn); }
  __syncthreads();
  int k = threadIdx.x;
  if (k < DF) {
    float re = 0, im = 0; int idx = 0;
    for (int d = 0; d < D; ++d) { re += xs[d] * tw[idx].x; im += xs[d] * tw[idx].y; idx += k; if (idx >= D) idx -= D; }
    Z[s * DF + k] = make_float2(re, im);
  }
}

template <int SIGN, bool MULW>
__global__ void coldft_k(const float2* in, float2* out, const u16* wr, const u16* wi, float scale) {
  __shared__ float2 tw[S];
  for (int i = threadIdx.x; i < S; i += blockDim.x) { float sn, cs; sincospif(2.0f * i / S, &sn, &cs); tw[i] = make_float2(cs, SIGN * sn); }
  __syncthreads();
  int u = blockIdx.x;
  int k = threadIdx.x;
  if (k < DF) {
    float re = 0, im = 0; int idx = 0;
    for (int s = 0; s < S; ++s) { float2 z = in[s * DF + k]; float2 t = tw[idx]; re += z.x * t.x - z.y * t.y; im += z.x * t.y + z.y * t.x; idx = (idx + u) & (S - 1); }
    if (MULW) { float a = bf2f(wr[u * DF + k]), b = bf2f(wi[u * DF + k]); float r2 = re * a - im * b, i2 = re * b + im * a; re = r2; im = i2; }
    out[u * DF + k] = make_float2(re * scale, im * scale);
  }
}

__global__ void irow_k(const float2* Y, float* yA) {
  __shared__ float2 ys[DF]; __shared__ float2 tw[D];
  int s = blockIdx.x;
  for (int i = threadIdx.x; i < D; i += blockDim.x) { float sn, cs; sincospif(2.0f * i / D, &sn, &cs); tw[i] = make_float2(cs, sn); }
  for (int i = threadIdx.x; i < DF; i += blockDim.x) ys[i] = Y[s * DF + i];
  __syncthreads();
  int d = threadIdx.x;
  float acc = 0; int idx = d;
  for (int k = 1; k < DF - 1; ++k) { acc += ys[k].x * tw[idx].x - ys[k].y * tw[idx].y; idx += d; if (idx >= D) idx -= D; }
  acc = 2.0f * acc + ys[0].x + ((d & 1) ? -ys[DF - 1].x : ys[DF - 1].x);
  yA[s * D + d] = rbf(acc / D);
}

__global__ void gemm1_k(const u16* h, const u16* w1, const u16* b1, u16* g) {
  __shared__ float hs[32][16], ws[32][16];
  int f = blockIdx.x * 16 + threadIdx.x, d = blockIdx.y * 16 + threadIdx.y;
  float acc = 0;
  for (int k0 = 0; k0 < S; k0 += 32) {
    for (int r = threadIdx.y; r < 32; r += 16) { hs[r][threadIdx.x] = bf2f(h[(k0 + r) * D + blockIdx.y * 16 + threadIdx.x]); ws[r][threadIdx.x] = bf2f(w1[(size_t)(k0 + r) * F + f]); }
    __syncthreads();
    for (int r = 0; r < 32; ++r) acc += hs[r][threadIdx.y] * ws[r][threadIdx.x];
    __syncthreads();
  }
  float u = rbf(rbf(acc) + bf2f(b1[f]));
  float t = tanhf(0.7978845608028654f * (u + 0.044715f * u * u * u));
  g[(size_t)d * F + f] = f2bf(0.5f * u * (1.0f + t));
}

__global__ void gemm2_k(const u16* g, const u16* w2, const u16* b2, const u16* x, const float* yA, u16* out) {
  __shared__ float gs[16][33], ws[32][16];
  int s = blockIdx.x * 16 + threadIdx.x, d = blockIdx.y * 16 + threadIdx.y;
  float acc = 0;
  for (int k0 = 0; k0 < F; k0 += 32) {
    for (int r = threadIdx.x; r < 32; r += 16) gs[threadIdx.y][r] = bf2f(g[(size_t)d * F + k0 + r]);
    for (int r = threadIdx.y; r < 32; r += 16) ws[r][threadIdx.x] = bf2f(w2[(size_t)(k0 + r) * S + s]);
    __syncthreads();
    for (int r = 0; r < 32; ++r) acc += gs[threadIdx.y][r] * ws[r][threadIdx.x];
    __syncthreads();
  }
  float yb = rbf(rbf(acc) + bf2f(b2[s]));
  float o = rbf(bf2f(x[s * D + d]) + yA[s * D + d]);
  out[s * D + d] = f2bf(o + yb);
}

extern "C" void kernel_launch(void* const* d_in, const int* in_sizes, int n_in, void* d_out, int out_size, void* d_ws, size_t ws_size, hipStream_t stream) {
  const u16* x = (const u16*)d_in[0];
  const u16* wr = (const u16*)d_in[1];
  const u16* wi = (const u16*)d_in[2];
  const u16* w1 = (const u16*)d_in[4];
  const u16* b1 = (const u16*)d_in[5];
  const u16* w2 = (const u16*)d_in[6];
  const u16* b2 = (const u16*)d_in[7];
  char* ws = (char*)d_ws;
  float* xn = (float*)ws; ws += (size_t)S * D * 4;
  u16* h = (u16*)ws; ws += (size_t)S * D * 2;
  float2* Z = (float2*)ws; ws += (size_t)S * DF * 8;
  float2* P = (float2*)ws; ws += (size_t)S * DF * 8;
  float2* Y = (float2*)ws; ws += (size_t)S * DF * 8;
  float* yA = (float*)ws; ws += (size_t)S * D * 4;
  u16* g = (u16*)ws; ws += (size_t)D * F * 2;
  ln_k<<<S, D, 0, stream>>>(x, xn, h);
  rowdft_k<<<S, 320, 0, stream>>>(xn, Z);
  coldft_k<-1, true><<<S, 320, 0, stream>>>(Z, P, wr, wi, 1.0f);
  coldft_k<1, false><<<S, 320, 0, stream>>>(P, Y, nullptr, nullptr, 1.0f / S);
  irow_k<<<S, D, 0, stream>>>(Y, yA);
  gemm1_k<<<dim3(F / 16, D / 16), dim3(16, 16), 0, stream>>>(h, w1, b1, g);
  gemm2_k<<<dim3(S / 16, D / 16), dim3(16, 16), 0, stream>>>(g, w2, b2, x, yA, (u16*)d_out);
}
```

```cpp
#include <hip/hip_runtime.h>
#include <hip/hip_bf16.h>
#include <stdint.h>

typedef unsigned short u16;
typedef unsigned int u32;
using bf16x8 = __attribute__((ext_vector_type(8))) short;
using f32x4 = __attribute__((ext_vector_type(4))) float;
using f32x2 = __attribute__((ext_vector_type(2))) float;
using u32x4 = __attribute__((ext_vector_type(4))) unsigned;
using u32x2 = __attribute__((ext_vector_type(2))) unsigned;
#define LAS __attribute__((address_space(3)))
#define GAS __attribute__((address_space(1)))
#define SB0 __builtin_amdgcn_sched_barrier(0)

constexpr int S = 2048, D = 576, DF = 289, F = 4608;
constexpr int KX = F + D;

constexpr size_t WS_HB = 0;
constexpr size_t WS_Z = WS_HB + (size_t)S * D * 2;
constexpr size_t WS_WT = WS_Z + (size_t)DF * S * 8;
constexpr size_t WS_GB = WS_WT + (size_t)DF * S * 4;
constexpr size_t WS_YT = WS_GB + (size_t)KX * D * 2;
constexpr size_t WS_P = WS_YT + (size_t)D * S * 2;
constexpr size_t WS_P_BYTES = (size_t)256 * 4 * 18 * 1024;
constexpr size_t WS_CNT = WS_P + WS_P_BYTES;
constexpr size_t WS_END = WS_CNT + 64 * 256;

constexpr double kPi = 3.141592653589793238462643383279502884;
constexpr double c_sin_small(double x) { double t = x, s = x; for (int i = 1; i < 14; ++i) { t *= -x * x / ((2 * i) * (2 * i + 1)); s += t; } return s; }
constexpr double c_cos_small(double x) { double t = 1, s = 1; for (int i = 1; i < 14; ++i) { t *= -x * x / ((2 * i - 1) * (2 * i)); s += t; } return s; }
constexpr double c_cos(int m, int n) {
  m %= n; if (2 * m > n) m = n - m;
  if (4 * m > n) return -((8 * (n / 2 - m) > n) ? c_sin_small(2 * kPi * (m - n / 4) / n) : c_cos_small(2 * kPi * (n / 2 - m) / n));
  return (8 * m > n) ? c_sin_small(2 * kPi * (n / 4 - m) / n) : c_cos_small(2 * kPi * m / n);
}
constexpr double c_sin(int m, int n) { return c_cos((m % n) + n - n / 4, n); }
template <int N> struct TwTab { float v[2 * N]; constexpr TwTab() : v() { for (int m = 0; m < N; ++m) { v[2 * m] = (float)c_cos(m, N); v[2 * m + 1] = (float)(-c_sin(m, N)); } } };
__device__ const TwTab<576> TW576 = TwTab<576>();
__device__ const TwTab<2048> TW2048 = TwTab<2048>();

struct cf { float x, y; };
__device__ __forceinline__ cf cadd(cf a, cf b) { return {a.x + b.x, a.y + b.y}; }
__device__ __forceinline__ cf csub(cf a, cf b) { return {a.x - b.x, a.y - b.y}; }
__device__ __forceinline__ cf cmul(cf a, cf w) { return {__builtin_fmaf(a.x, w.x, -a.y * w.y), __builtin_fmaf(a.x, w.y, a.y * w.x)}; }
__device__ __forceinline__ cf cmulc(cf a, cf w) { return {__builtin_fmaf(a.x, w.x, a.y * w.y), __builtin_fmaf(a.y, w.x, -a.x * w.y)}; }
__device__ __forceinline__ cf ldtw(const float* t, int m) { f32x2 v = *(const f32x2*)(t + 2 * m); return {v.x, v.y}; }

__device__ __forceinline__ float bflo(u32 v) { return __uint_as_float(v << 16); }
__device__ __forceinline__ float bfhi(u32 v) { return __uint_as_float(v & 0xffff0000u); }
__device__ __forceinline__ u32 pack_bf16(float a, float b) {
  typedef __attribute__((ext_vector_type(2))) __bf16 bf2;
  f32x2 v = {a, b};
  return __builtin_bit_cast(u32, __builtin_convertvector(v, bf2));
}
__device__ __forceinline__ float rbf(float a) { return bflo(pack_bf16(a, a)); }

template <int CTRL> __device__ __forceinline__ float dppf(float v) {
  return __int_as_float(__builtin_amdgcn_update_dpp(0, __float_as_int(v), CTRL, 0xf, 0xf, true));
}
constexpr int DPP_X1 = 0xB1, DPP_X2 = 0x4E, DPP_X7 = 0x141, DPP_X8 = 0x128;
__device__ __forceinline__ float xor16f(float v) { return __int_as_float(__builtin_amdgcn_ds_swizzle(__float_as_int(v), 0x401F)); }
__device__ __forceinline__ float half_sum(float v) {
  v += dppf<DPP_X1>(v); v += dppf<DPP_X2>(v); v += dppf<DPP_X7>(v); v += dppf<DPP_X8>(v); v += xor16f(v); return v;
}

__device__ __forceinline__ u32x4 make_srd(const void* p, u32 bytes) {
  uint64_t a = (uint64_t)p; u32x4 r;
  r.x = __builtin_amdgcn_readfirstlane((u32)a);
  r.y = __builtin_amdgcn_readfirstlane((u32)(a >> 32)) & 0xffffu;
  r.z = bytes; r.w = 0x00020000u; return r;
}
__device__ __forceinline__ void dma16(u32 ldsaddr, u32 voff, u32x4 srd, u32 soff) {
  asm volatile("s_mov_b32 m0, %0\n\ts_nop 0\n\tbuffer_load_dwordx4 %1, %2, %3 offen lds" ::"s"(ldsaddr), "v"(voff), "s"(srd), "s"(soff) : "memory");
}
template <int OFF> __device__ __forceinline__ u32x2 trd(u32 addr) {
  u32x2 r; asm volatile("ds_read_b64_tr_b16 %0, %1 offset:%2" : "=v"(r) : "v"(addr), "n"(OFF) : "memory"); return r;
}
template <int OFF> __device__ __forceinline__ u32x4 rd128(u32 addr) {
  u32x4 r; asm volatile("ds_read_b128 %0, %1 offset:%2" : "=v"(r) : "v"(addr), "n"(OFF) : "memory"); return r;
}
__device__ __forceinline__ f32x4 mfma16(u32x4 a, u32x4 b, f32x4 c) {
  return __builtin_amdgcn_mfma_f32_16x16x32_bf16(__builtin_bit_cast(bf16x8, a), __builtin_bit_cast(bf16x8, b), c, 0, 0, 0);
}
constexpr int K1_LN = 256, K1_WT = 64, K1_TB = 81;
constexpr int K1_GRID = K1_LN + K1_WT + K1_TB;

__device__ __forceinline__ void dft3(cf& a, cf& b, cf& c) {
  const float h = 0.8660254037844386f;
  cf s = cadd(b, c), d = csub(b, c);
  cf m = {__builtin_fmaf(-0.5f, s.x, a.x), __builtin_fmaf(-0.5f, s.y, a.y)};
  a = cadd(a, s);
  b = {__builtin_fmaf(h, d.y, m.x), __builtin_fmaf(-h, d.x, m.y)};
  c = {__builtin_fmaf(-h, d.y, m.x), __builtin_fmaf(h, d.x, m.y)};
}
template <int CTRL> __device__ __forceinline__ cf bfly_dpp(cf v, float sg) {
  return {__builtin_fmaf(dppf<CTRL>(v.x), sg, v.x), __builtin_fmaf(dppf<CTRL>(v.y), sg, v.y)};
}

__global__ __launch_bounds__(256) void k1_pre(const u16* __restrict__ x, const u16* __restrict__ wr, const u16* __restrict__ wi, char* __restrict__ ws) {
  __shared__ __attribute__((aligned(16))) char smem[37120];
  const int tid = threadIdx.x, lane = tid & 63, wave = tid >> 6;
  const int bid = blockIdx.x;
  if (bid < K1_LN) {
    cf* zc = (cf*)smem;
    char* hrow = smem + 8 * 288 * 8;
    const int p = lane & 31, r = 2 * wave + (lane >> 5);
    const int l = p ^ ((p & 4) ? 3 : 0);
    const u32* xr = (const u32*)(x + (size_t)(bid * 8 + r) * D);
    u32 raw[9];
#pragma unroll
    for (int j = 0; j < 9; ++j) raw[j] = xr[l + 32 * j];
    cf tw1[9];
#pragma unroll
    for (int k2 = 1; k2 < 9; ++k2) tw1[k2] = ldtw(TW576.v, 2 * l * k2);
    cf ts4 = ldtw(TW576.v, 18 * (l & 15)), ts3 = ldtw(TW576.v, 36 * (l & 7)), ts2 = ldtw(TW576.v, 72 * (l & 3));
    float e[9], o[9];
#pragma unroll
    for (int j = 0; j < 9; ++j) { e[j] = bflo(raw[j]); o[j] = bfhi(raw[j]); }
    u32 hp[9];
    {
      float s = 0;
#pragma unroll
      for (int j = 0; j < 9; ++j) s += e[j] + o[j];
      float mean = half_sum(s) * (1.0f / D);
      float q = 0;
#pragma unroll
      for (int j = 0; j < 9; ++j) { float a = e[j] - mean, b = o[j] - mean; q = __builtin_fmaf(a, a, q); q = __builtin_fmaf(b, b, q); }
      float var = half_sum(q) * (1.0f / D);
      float m = rbf(mean);
      float inv = __builtin_amdgcn_rcpf(rbf(__builtin_sqrtf(rbf(rbf(var) + 1e-5f))));
#pragma unroll
      for (int j = 0; j < 9; ++j) {
        u32 d = pack_bf16(e[j] - m, o[j] - m);
        u32 n = pack_bf16(bflo(d) * inv, bfhi(d) * inv);
        e[j] = bflo(n); o[j] = bfhi(n);
      }
      s = 0;
#pragma unroll
      for (int j = 0; j < 9; ++j) s += e[j] + o[j];
      mean = half_sum(s) * (1.0f / D);
      q = 0;
#pragma unroll
      for (int j = 0; j < 9; ++j) { float a = e[j] - mean, b = o[j] - mean; q = __builtin_fmaf(a, a, q); q = __builtin_fmaf(b, b, q); }
      var = half_sum(q) * (1.0f / D);
      m = rbf(mean);
      inv = __builtin_amdgcn_rcpf(rbf(__builtin_sqrtf(rbf(rbf(var) + 1e-5f))));
#pragma unroll
      for (int j = 0; j < 9; ++j) {
        u32 d = pack_bf16(e[j] - m, o[j] - m);
        hp[j] = pack_bf16(bflo(d) * inv, bfhi(d) * inv);
      }
    }
#pragma unroll
    for (int j = 0; j < 9; ++j) *(u32*)(hrow + r * 1216 + (l + 32 * j) * 4) = hp[j];
    cf z[9];
#pragma unroll
    for (int j = 0; j < 9; ++j) z[j] = {e[j], o[j]};
    dft3(z[0], z[3], z[6]); dft3(z[1], z[4], z[7]); dft3(z[2], z[5], z[8]);
    {
      const cf w1 = {0.766044443118978f, -0.6427876096865393f}, w2 = {0.17364817766693041f, -0.984807753012208f}, w4 = {-0.9396926207859083f, -0.3420201433256689f};
      z[4] = cmul(z[4], w1); z[7] = cmul(z[7], w2); z[5] = cmul(z[5], w2); z[8] = cmul(z[8], w4);
    }
    dft3(z[0], z[1], z[2]); dft3(z[3], z[4], z[5]); dft3(z[6], z[7], z[8]);
    cf y[9];
#pragma unroll
    for (int c = 0; c < 3; ++c)
#pragma unroll
      for (int d = 0; d < 3; ++d) y[c + 3 * d] = z[3 * c + d];
#pragma unroll
    for (int k2 = 1; k2 < 9; ++k2) y[k2] = cmul(y[k2], tw1[k2]);
    {
      const bool u4 = l & 16, u3 = l & 8, u2 = l & 4, u1 = l & 2, u0 = l & 1;
      const float s4 = u4 ? -1.f : 1.f, s3 = u3 ? -1.f : 1.f, s2 = u2 ? -1.f : 1.f, s1 = u1 ? -1.f : 1.f, s0 = u0 ? 1.f : -1.f;
      ts4 = u4 ? cf{-ts4.x, -ts4.y} : cf{1.f, 0.f};
      ts3 = u3 ? cf{-ts3.x, -ts3.y} : cf{1.f, 0.f};
      ts2 = u2 ? cf{-ts2.x, -ts2.y} : cf{1.f, 0.f};
      cf ts1 = u1 ? (u0 ? cf{0.f, -1.f} : cf{-1.f, 0.f}) : (u0 ? cf{-1.f, 0.f} : cf{1.f, 0.f});
#pragma unroll
      for (int k = 0; k < 9; ++k) { cf t = {__builtin_fmaf(xor16f(y[k].x), s4, y[k].x), __builtin_fmaf(xor16f(y[k].y), s4, y[k].y)}; y[k] = cmul(t, ts4); }
#pragma unroll
      for (int k = 0; k < 9; ++k) y[k] = cmul(bfly_dpp<DPP_X8>(y[k], s3), ts3);
#pragma unroll
      for (int k = 0; k < 9; ++k) y[k] = cmul(bfly_dpp<DPP_X7>(y[k], s2), ts2);
#pragma unroll
      for (int k = 0; k < 9; ++k) y[k] = cmul(bfly_dpp<DPP_X2>(y[k], s1), ts1);
#pragma unroll
      for (int k = 0; k < 9; ++k) y[k] = bfly_dpp<DPP_X1>(y[k], s0);
    }
    {
      const int k1 = __builtin_bitreverse32((u32)l) >> 27;
#pragma unroll
      for (int k2 = 0; k2 < 9; ++k2) zc[r * 288 + 9 * k1 + k2] = y[k2];
    }
    __syncthreads();
    {
      const u32 hb = (u32)(size_t)hrow;
      const int g = lane >> 4, q = (lane & 15) >> 2, pp = lane & 3;
      u32x4* dst = (u32x4*)(ws + WS_HB) + (size_t)bid * D;
      for (int it = wave; it < 9; it += 4) {
        const u32 a = hb + q * 1216 + (64 * it + 16 * g + 4 * pp) * 2;
        u32x2 lo, hi;
        asm volatile("ds_read_b64_tr_b16 %0, %2\n\tds_read_b64_tr_b16 %1, %2 offset:4864\n\ts_waitcnt lgkmcnt(0)" : "=&v"(lo), "=&v"(hi) : "v"(a) : "memory");
        dst[64 * it + lane] = (u32x4){lo.x, lo.y, hi.x, hi.y};
      }
    }
    {
      f32x2* Z = (f32x2*)(ws + WS_Z);
      const int row = tid & 7;
#pragma unroll
      for (int rd = 0; rd < 5; ++rd) {
        const int k = (tid >> 3) + 32 * rd;
        if (k <= 144) {
          cf a = zc[row * 288 + k], b = zc[row * 288 + (k ? 288 - k : 0)];
          cf w = ldtw(TW576.v, k);
          cf E = {0.5f * (a.x + b.x), 0.5f * (a.y - b.y)};
          cf O = {0.5f * (a.y + b.y), 0.5f * (b.x - a.x)};
          cf T = cmul(O, w);
          Z[(size_t)k * S + bid * 8 + row] = (f32x2){E.x + T.x, E.y + T.y};
          Z[(size_t)(288 - k) * S + bid * 8 + row] = (f32x2){E.x - T.x, T.y - E.y};
        }
      }
    }
  } else if (bid < K1_LN + K1_WT) {
    const int u0 = (bid - K1_LN) * 32;
    const u32x4* sr = (const u32x4*)(wr + (size_t)u0 * DF);
    const u32x4* si = (const u32x4*)(wi + (size_t)u0 * DF);
    u32x4* lr = (u32x4*)smem;
    u32x4* li = (u32x4*)(smem + 18496);
    for (int c = tid; c < 1156; c += 256) { lr[c] = sr[c]; li[c] = si[c]; }
    if (bid == K1_LN && tid < 64) *(u32*)(ws + WS_CNT + tid * 256) = 0u;
    __syncthreads();
    const u16* pr = (const u16*)smem; const u16* pi = (const u16*)(smem + 18496);
    u32x4* dst = (u32x4*)(ws + WS_WT);
    for (int it = tid; it < DF * 8; it += 256) {
      const int k = it >> 3, q = it & 7;
      u32x4 v;
#pragma unroll
      for (int e2 = 0; e2 < 4; ++e2) { const int idx = (4 * q + e2) * DF + k; v[e2] = (u32)pr[idx] | ((u32)pi[idx] << 16); }
      dst[((size_t)k * S + u0) / 4 + q] = v;
    }
  } else {
    u32x4* dst = (u32x4*)(ws + WS_GB) + (size_t)(F / 8) * D;
#pragma unroll
    for (int rep = 0; rep < 2; ++rep) {
      const int c = (bid - K1_LN - K1_WT) * 512 + rep * 256 + tid;
      const int kc = c / D, d = c - kc * D;
      u32x4 v;
#pragma unroll
      for (int j = 0; j < 4; ++j) {
        const int k = 4 * kc + j;
        cf w = ldtw(TW576.v, (k * d) % D);
        v[j] = pack_bf16(2.f * w.x, 2.f * w.y);
      }
      if (kc == 0) v[0] = pack_bf16(1.f, (d & 1) ? -1.f : 1.f);
      dst[c] = v;
    }
  }
}
constexpr int G1_WGS = 216, FFT_WGS = 37;
constexpr int K2_GRID = G1_WGS + FFT_WGS;
constexpr int G1_NST = 5, G1_STB = 28672;
constexpr int K2_LDS = G1_NST * G1_STB;

struct Tw64 { float c[32], s[32]; constexpr Tw64() : c(), s() { for (int m = 0; m < 32; ++m) { c[m] = (float)c_cos(m, 64); s[m] = (float)c_sin(m, 64); } } };
constexpr Tw64 T64 = Tw64();
template <int DIR> __device__ __forceinline__ cf twc64(cf d, const int m) {
  if (m == 0) return d;
  if (m == 16) return DIR ? cf{-d.y, d.x} : cf{d.y, -d.x};
  const float c = T64.c[m], s = DIR ? T64.s[m] : -T64.s[m];
  return {__builtin_fmaf(d.x, c, -d.y * s), __builtin_fmaf(d.x, s, d.y * c)};
}
constexpr int brev5(int i) { return ((i & 1) << 4) | ((i & 2) << 2) | (i & 4) | ((i & 8) >> 2) | ((i & 16) >> 4); }
template <int DIR> __device__ __forceinline__ void fft32(cf (&x)[32]) {
#pragma unroll
  for (int st = 0; st < 5; ++st) {
    const int h = 16 >> st;
#pragma unroll
    for (int b = 0; b < 32; b += 2 * h)
#pragma unroll
      for (int i = 0; i < h; ++i) {
        cf a = x[b + i], c = x[b + i + h];
        x[b + i] = cadd(a, c);
        x[b + i + h] = twc64<DIR>(csub(a, c), i * (32 / h));
      }
  }
}
template <int DIR> __device__ __forceinline__ void fft2048_a(cf (&x)[32], char* ex, int lane, const float scale) {
  asm volatile("" : "+v"(lane));
  cf hi[8], lo[4];
  {
    const int n0 = lane >> 1, n1 = lane & 1;
#pragma unroll
    for (int h = 0; h < 8; ++h) hi[h] = ldtw(TW2048.v, (n0 * (4 * h + 32 * n1)) & 2047);
#pragma unroll
    for (int l = 1; l < 4; ++l) lo[l] = ldtw(TW2048.v, n0 * l);
  }
  SB0;
  fft32<DIR>(x);
  const float sg = (lane & 1) ? -1.f : 1.f, pf = (lane & 1) ? 1.f : 0.f, ss = sg * scale;
  char* wp = ex + (lane & 1) * 272 + (lane >> 1) * 8;
#pragma unroll
  for (int h = 0; h < 8; ++h) {
    SB0;
    const cf hs = {hi[h].x * ss, hi[h].y * ss};
#pragma unroll
    for (int l = 0; l < 4; ++l) {
      const int ka = 4 * h + l;
      cf v = x[brev5(ka)];
      if (ka) {
        const cf w64 = {__builtin_fmaf(pf, T64.c[ka] - 1.f, 1.f), pf * (DIR ? T64.s[ka] : -T64.s[ka])};
        v = cmul(v, w64);
      }
      v = bfly_dpp<DPP_X1>(v, sg);
      cf w = l ? cmul(hs, lo[l]) : hs;
      v = DIR ? cmulc(v, w) : cmul(v, w);
      *(f32x2*)(wp + ka * 544) = (f32x2){v.x, v.y};
    }
  }
  SB0;
  __builtin_amdgcn_fence(__ATOMIC_RELEASE, "wavefront");
  __builtin_amdgcn_wave_barrier();
  __builtin_amdgcn_fence(__ATOMIC_ACQUIRE, "wavefront");
  const char* rp = ex + lane * 272;
#pragma unroll
  for (int i = 0; i < 16; ++i) { f32x4 v = *(const f32x4*)(rp + 16 * i); x[2 * i] = {v.x, v.y}; x[2 * i + 1] = {v.z, v.w}; }
  __builtin_amdgcn_fence(__ATOMIC_RELEASE, "wavefront");
  __builtin_amdgcn_wave_barrier();
  __builtin_amdgcn_fence(__ATOMIC_ACQUIRE, "wavefront");
}
template <int DIR> __device__ __forceinline__ void fft2048_b(cf (&x)[32]) {
  fft32<DIR>(x);
  cf y[32];
#pragma unroll
  for (int j = 0; j < 32; ++j) y[j] = x[brev5(j)];
#pragma unroll
  for (int j = 0; j < 32; ++j) x[j] = y[j];
}

__device__ __forceinline__ void fft_role(char* smem, char* __restrict__ ws, const int fb) {
  const int lane = threadIdx.x & 63;
  const int wave = __builtin_amdgcn_readfirstlane(threadIdx.x >> 6);
  const int k = fb * 8 + wave;
  if (k >= DF) return;
  const int n0 = lane >> 1, n1 = lane & 1, v = n0 + 32 * n1;
  const f32x2* zp = (const f32x2*)(ws + WS_Z) + (size_t)k * S + v;
  cf x[32];
#pragma unroll
  for (int j = 0; j < 32; ++j) { f32x2 t = zp[64 * j]; x[j] = {t.x, t.y}; }
  char* ex = smem + wave * 17408;
  fft2048_a<0>(x, ex, lane, 1.0f);
  SB0;
  const u32* wp = (const u32*)(ws + WS_WT) + (size_t)k * S + v;
  u32 wv[32];
#pragma unroll
  for (int j = 0; j < 32; ++j) wv[j] = wp[64 * j];
  SB0;
  fft2048_b<0>(x);
#pragma unroll
  for (int j = 0; j < 32; ++j) x[j] = cmul(x[j], cf{bflo(wv[j]), bfhi(wv[j])});
  fft2048_a<1>(x, ex, lane, 1.0f / ((float)S * (float)D));
  fft2048_b<1>(x);
  const bool odd = n0 & 1;
  const int rowRe = (k == DF - 1) ? 1 : 2 * k;
  u32* yt = (u32*)(ws + WS_YT) + (size_t)(odd ? 2 * k + 1 : rowRe) * (S / 2) + (v >> 1);
  const bool doit = !odd || (k > 0 && k < DF - 1);
#pragma unroll
  for (int j = 0; j < 32; ++j) {
    float recv = dppf<DPP_X2>(odd ? x[j].x : x[j].y);
    u32 pk = odd ? pack_bf16(recv, x[j].y) : pack_bf16(x[j].x, recv);
    if (doit) yt[32 * j] = pk;
  }
}

__device__ __forceinline__ void g1_store(const f32x4 a, const u32x2 bb, char* gb, const int fb0, const int d) {
  const float bv[4] = {bflo(bb.x), bfhi(bb.x), bflo(bb.y), bfhi(bb.y)};
  float r[4];
#pragma unroll
  for (int e = 0; e < 4; ++e) {
    const float u = a[e] + bv[e];
    const float z = u * __builtin_fmaf(u * u, -0.044715f * 2.0f * 0.7978845608028654f * 1.4426950408889634f, -2.0f * 0.7978845608028654f * 1.4426950408889634f);
    r[e] = u * __builtin_amdgcn_rcpf(1.0f + __builtin_amdgcn_exp2f(z));
  }
  *(u32x2*)(gb + ((size_t)(fb0 >> 3) * D + d) * 16 + (fb0 & 4) * 2) = (u32x2){pack_bf16(r[0], r[1]), pack_bf16(r[2], r[3])};
}

__global__ __launch_bounds__(512, 2) void k2_gemm1(const u16* __restrict__ w1, const u16* __restrict__ b1, char* __restrict__ ws) {
  extern __shared__ __attribute__((aligned(16))) char smem[];
  constexpr int TD = 96, TF = 128, BK = 64, NST = G1_NST, STB = G1_STB;
  constexpr int WB = BK * TF * 2, NK = S / BK;
  const int bid = blockIdx.x;
  if (bid >= G1_WGS) { fft_role(smem, ws, bid - G1_WGS); return; }
  const int tid = threadIdx.x, lane = tid & 63;
  const int wave = __builtin_amdgcn_readfirstlane(tid >> 6);
  const int lin = (bid & 7) * 27 + (bid >> 3);
  const int ft = lin / 6, dt = lin - ft * 6;
  const int f0 = ft * TF, d0 = dt * TD;
  const u32 ldsb = (u32)(size_t)smem;
  const int wd = (wave >> 1) & 1, wf = wave & 1;
  const int fbase = f0 + wf * 64 + (wave >> 2) * 32 + 4 * (lane >> 4);
  const int dbase = d0 + wd * 48 + (lane & 15);
  f32x4* ex = (f32x4*)smem;

  if (wave >= 4) {
    const int lw = wave - 4;
    const u32x4 srdW = make_srd(w1, (u32)((size_t)S * F * 2));
    const u32x4 srdH = make_srd(ws + WS_HB, (u32)((size_t)S * D * 2));
    u32 vW[4], vH[3];
#pragma unroll
    for (int j = 0; j < 4; ++j) {
      const int k = 4 * (4 * lw + j) + (lane >> 4);
      const int swz = ((k & 3) << 1) | (((k >> 3) & 1) << 3);
      vW[j] = (u32)(k * (F * 2) + f0 * 2 + ((lane & 15) ^ swz) * 16);
    }
#pragma unroll
    for (int j = 0; j < 3; ++j) {
      const int p = (3 * lw + j) * 64 + lane;
      const int kc = p / 96, dd = p - kc * 96;
      vH[j] = (u32)((kc * D + d0 + dd) * 16);
    }
    const u32 mW = ldsb + 4 * lw * 1024, mH = ldsb + WB + 3 * lw * 1024;
#define G1_ISSUE(t, bo) do { \
      _Pragma("unroll") for (int j = 0; j < 4; ++j) dma16(mW + (bo) + j * 1024, vW[j], srdW, (u32)(t) * (BK * F * 2)); \
      _Pragma("unroll") for (int j = 0; j < 3; ++j) dma16(mH + (bo) + j * 1024, vH[j], srdH, (u32)(t) * (8 * D * 16)); } while (0)
#pragma unroll
    for (int t = 0; t < NST; ++t) G1_ISSUE(t, t * STB);
    asm volatile("s_waitcnt vmcnt(%0)\n\ts_barrier" ::"n"(7 * (NST - 1)) : "memory");
    u32 bo = 0;
#pragma unroll 1
    for (int t = 0; t < NK; ++t) {
      asm volatile("s_waitcnt vmcnt(%0)\n\ts_barrier" ::"n"(7 * (NST - 2)) : "memory");
      G1_ISSUE(t + NST, bo);
      bo += STB; if (bo == NST * STB) bo = 0;
    }
    const u32x2 bb0 = *(const u32x2*)(b1 + fbase), bb1 = *(const u32x2*)(b1 + fbase + 16);
    asm volatile("s_waitcnt vmcnt(0)\n\ts_barrier" ::: "memory");
    asm volatile("s_barrier" ::: "memory");
#pragma unroll
    for (int ii = 0; ii < 2; ++ii)
#pragma unroll
      for (int j = 0; j < 3; ++j) g1_store(ex[(lw * 6 + ii * 3 + j) * 64 + lane], ii ? bb1 : bb0, ws + WS_GB, fbase + 16 * ii, dbase + 16 * j);
    return;
  }
  u32 aA[4];
  {
    const int g = lane >> 4, q = (lane & 15) >> 2, p = lane & 3;
    const int swz = (q << 1) | ((g & 1) << 3);
    const int k = 8 * g + q;
#pragma unroll
    for (int i = 0; i < 4; ++i) aA[i] = ldsb + (u32)(k * 256 + (p & 1) * 8 + ((wf * 8 + 2 * i + (p >> 1)) ^ swz) * 16);
  }
  const u32 aB = ldsb + (u32)(WB + ((lane >> 4) * TD + wd * 48 + (lane & 15)) * 16);
  const u32x2 bb0 = *(const u32x2*)(b1 + fbase), bb1 = *(const u32x2*)(b1 + fbase + 16);

  f32x4 acc[4][3];
#pragma unroll
  for (int i = 0; i < 4; ++i)
#pragma unroll
    for (int j = 0; j < 3; ++j) acc[i][j] = (f32x4){0.f, 0.f, 0.f, 0.f};
  u32x4 fa[2][4], fb[2][3];
  u32x2 lo, hi;
#define G1_RDA(dst, a, H) do { lo = trd<8192 * (H)>(a); hi = trd<8192 * (H) + 1024>(a); dst = (u32x4){lo.x, lo.y, hi.x, hi.y}; } while (0)
#define G1_RDB(dst, J, a, H) dst = rd128<6144 * (H) + 256 * (J)>(a)
#define G1_M(I, J, C) do { acc[I][J] = mfma16(fa[C][I], fb[C][J], acc[I][J]); SB0; } while (0)
#define G1_HALF(C, N, bo_, H) do { \
    const u32 ab = aB + (bo_), a0 = aA[0] + (bo_), a1 = aA[1] + (bo_), a2 = aA[2] + (bo_), a3 = aA[3] + (bo_); \
    G1_M(0, 0, C); G1_RDB(fb[N][0], 0, ab, H); G1_RDA(fa[N][0], a0, H); SB0; \
    G1_M(0, 1, C); G1_RDB(fb[N][1], 1, ab, H); SB0; \
    G1_M(0, 2, C); G1_RDB(fb[N][2], 2, ab, H); SB0; \
    G1_M(1, 0, C); G1_RDA(fa[N][1], a1, H); SB0; \
    G1_M(1, 1, C); G1_RDA(fa[N][2], a2, H); SB0; \
    G1_M(1, 2, C); G1_RDA(fa[N][3], a3, H); SB0; \
    G1_M(2, 0, C); G1_M(2, 1, C); G1_M(2, 2, C); G1_M(3, 0, C); G1_M(3, 1, C); G1_M(3, 2, C); } while (0)
  asm volatile("s_barrier" ::: "memory");
  G1_RDB(fb[0][0], 0, aB, 0); G1_RDB(fb[0][1], 1, aB, 0); G1_RDB(fb[0][2], 2, aB, 0);
#pragma unroll
  for (int i = 0; i < 4; ++i) G1_RDA(fa[0][i], aA[i], 0);
  u32 bo = 0;
#pragma unroll 1
  for (int t = 0; t < NK; ++t) {
    asm volatile("s_waitcnt lgkmcnt(0)" ::: "memory");
    SB0;
    u32 nbo = bo + STB; if (nbo == NST * STB) nbo = 0;
    G1_HALF(0, 1, bo, 1);
    asm volatile("s_waitcnt lgkmcnt(0)\n\ts_barrier" ::: "memory");
    SB0;
    G1_HALF(1, 0, nbo, 0);
    bo = nbo;
  }
  asm volatile("s_waitcnt lgkmcnt(0)\n\ts_barrier" ::: "memory");
  SB0;
#pragma unroll
  for (int ii = 0; ii < 2; ++ii)
#pragma unroll
    for (int j = 0; j < 3; ++j) ex[(wave * 6 + ii * 3 + j) * 64 + lane] = acc[2 + ii][j];
  asm volatile("s_waitcnt lgkmcnt(0)\n\ts_barrier" ::: "memory");
#pragma unroll
  for (int ii = 0; ii < 2; ++ii)
#pragma unroll
    for (int j = 0; j < 3; ++j) g1_store(acc[ii][j], ii ? bb1 : bb0, ws + WS_GB, fbase + 16 * ii, dbase + 16 * j);
}
constexpr int G2_NST = 4, G2_STB = 16384 + 18432;
constexpr int K3_LDS = G2_NST * G2_STB;
typedef GAS unsigned gu32;


constexpr int g2_owner(int i, int j) { return j < 8 ? (j >> 1) : i; }
template <int Q>
__device__ __forceinline__ void g2_finish(f32x4 (&acc)[2][9], const u16* __restrict__ x, const u16* __restrict__ b2, char* __restrict__ ws, u16* __restrict__ out,
                                          const int tile, const int s0, const int d0, const int wave, const int lane) {
  const __amdgpu_buffer_rsrc_t rp = __builtin_amdgcn_make_buffer_rsrc(ws + WS_P, 0, (int)WS_P_BYTES, 0x00020000);
  const int srow = s0 + wave * 32 + (lane & 15), dcol = d0 + 4 * (lane >> 4);
  u32x2 xr[2][9]; float bs[2];
#pragma unroll
  for (int i = 0; i < 2; ++i) {
    bs[i] = bflo((u32)b2[srow + 16 * i]);
#pragma unroll
    for (int j = 0; j < 9; ++j)
      if (g2_owner(i, j) == Q) xr[i][j] = *(const u32x2*)(x + (size_t)(srow + 16 * i) * D + dcol + 16 * j);
  }
  const u32 voff = (u32)lane * 16;
  const u32 mine = (u32)(((tile * 4 + Q) * 4 + wave) * 18) * 1024;
#pragma unroll
  for (int i = 0; i < 2; ++i)
#pragma unroll
    for (int j = 0; j < 9; ++j)
      if (g2_owner(i, j) != Q) __builtin_amdgcn_raw_buffer_store_b128(__builtin_bit_cast(u32x4, acc[i][j]), rp, voff, mine + (i * 9 + j) * 1024, 16  );
  asm volatile("s_waitcnt vmcnt(0)\n\ts_barrier" ::: "memory");
  gu32* cnt = (gu32*)(ws + WS_CNT + tile * 256);
  if (wave == 0) {
    if (lane == 0) __hip_atomic_fetch_add(cnt, 1u, __ATOMIC_RELAXED, __HIP_MEMORY_SCOPE_AGENT);
    for (unsigned spins = 0; spins < (1u << 22); ++spins) {
      if (__builtin_amdgcn_readfirstlane(__hip_atomic_load(cnt, __ATOMIC_RELAXED, __HIP_MEMORY_SCOPE_AGENT)) >= 4u) break;
      __builtin_amdgcn_s_sleep(1);
    }
  }
  asm volatile("s_barrier" ::: "memory");
  u32x4 pv[3][2][9];
#pragma unroll
  for (int o = 0; o < 3; ++o) {
    const int src = o < Q ? o : o + 1;
    const u32 base = (u32)(((tile * 4 + src) * 4 + wave) * 18) * 1024;
#pragma unroll
    for (int i = 0; i < 2; ++i)
#pragma unroll
      for (int j = 0; j < 9; ++j)
        if (g2_owner(i, j) == Q) pv[o][i][j] = __builtin_amdgcn_raw_buffer_load_b128(rp, voff, base + (i * 9 + j) * 1024, 16  );
  }
#pragma unroll
  for (int i = 0; i < 2; ++i)
#pragma unroll
    for (int j = 0; j < 9; ++j)
      if (g2_owner(i, j) == Q) {
        f32x4 p[4];
#pragma unroll
        for (int q = 0; q < 4; ++q) p[q] = q == Q ? acc[i][j] : __builtin_bit_cast(f32x4, pv[q < Q ? q : q - 1][i][j]);
        const f32x4 t = (p[0] + p[1]) + (p[2] + p[3]);
        const u32x2 xv = xr[i][j];
        const u32x2 o2 = {pack_bf16(t.x + bs[i] + bflo(xv.x), t.y + bs[i] + bfhi(xv.x)), pack_bf16(t.z + bs[i] + bflo(xv.y), t.w + bs[i] + bfhi(xv.y))};
        *(u32x2*)(out + (size_t)(srow + 16 * i) * D + dcol + 16 * j) = o2;
      }
}

__global__ __launch_bounds__(512, 2) void k3_gemm2(const u16* __restrict__ w2, const u16* __restrict__ b2, const u16* __restrict__ x, char* __restrict__ ws, u16* __restrict__ out) {
  extern __shared__ __attribute__((aligned(16))) char smem[];
  constexpr int TS = 128, TD = 144, BK = 64, NST = G2_NST, STB = G2_STB;
  constexpr int WB = BK * TS * 2;
  const int bid = blockIdx.x;
  const int tid = threadIdx.x, lane = tid & 63;
  const int wave = __builtin_amdgcn_readfirstlane(tid >> 6);
  const int xg = bid & 7, idx = bid >> 3;
  const int kq = xg >> 1, st = (xg & 1) * 8 + (idx >> 2), dt = idx & 3;
  const int s0 = st * TS, d0 = dt * TD;
  const int tile = st * 4 + dt;
  const int kbeg = 20 * kq, nk = (kq == 3) ? 21 : 20;
  const u32 ldsb = (u32)(size_t)smem;

  if (wave >= 4) {
    const int lw = wave - 4;
    const u32x4 srdW = make_srd(w2, (u32)((size_t)F * S * 2));
    const u32x4 srdY = make_srd(ws + WS_YT, (u32)((size_t)D * S * 2));
    const u32x4 srdG = make_srd(ws + WS_GB, (u32)((size_t)KX * D * 2));
    u32 vW[4], vG[5];
#pragma unroll
    for (int j = 0; j < 4; ++j) {
      const int k = 4 * (4 * lw + j) + (lane >> 4);
      const int swz = ((k & 3) << 1) | (((k >> 3) & 1) << 3);
      vW[j] = (u32)(k * (S * 2) + s0 * 2 + ((lane & 15) ^ swz) * 16);
    }
    const int gp0 = lw < 2 ? 5 * lw : 10 + 4 * (lw - 2);
#pragma unroll
    for (int j = 0; j < 5; ++j) {
      const int p = (gp0 + j) * 64 + lane;
      const int kc = p / TD, dd = p - kc * TD;
      vG[j] = (u32)((kc * D + d0 + dd) * 16);
    }
    const u32 mW = ldsb + 4 * lw * 1024, mG = ldsb + WB + gp0 * 1024;
    const bool five = lw < 2;
    auto issue = [&](int tl, u32 bo) {
      const int t = kbeg + tl;
      const bool ok = tl < nk;
      const bool isY = t >= 72;
      const u32 sW = ok ? (u32)(isY ? t - 72 : t) * (BK * S * 2) : 0xf0000000u;
      const u32 sG = ok ? (u32)t * (8 * D * 16) : 0xf0000000u;
      u32x4 sa;
      sa.x = isY ? srdY.x : srdW.x; sa.y = isY ? srdY.y : srdW.y; sa.z = isY ? srdY.z : srdW.z; sa.w = srdW.w;
#pragma unroll
      for (int j = 0; j < 4; ++j) dma16(mW + bo + j * 1024, vW[j], sa, sW);
#pragma unroll
      for (int j = 0; j < 4; ++j) dma16(mG + bo + j * 1024, vG[j], srdG, sG);
      if (five) dma16(mG + bo + 4096, vG[4], srdG, sG);
    };
#define G2_WAITBAR(n) do { if (five) asm volatile("s_waitcnt vmcnt(%0)\n\ts_barrier" ::"n"(9 * (n)) : "memory"); else asm volatile("s_waitcnt vmcnt(%0)\n\ts_barrier" ::"n"(8 * (n)) : "memory"); } while (0)
#pragma unroll
    for (int t = 0; t < NST; ++t) issue(t, t * STB);
    G2_WAITBAR(NST - 1);
    u32 bo = 0;
#pragma unroll 1
    for (int t = 0; t < nk; ++t) {
      G2_WAITBAR(NST - 2);
      issue(t + NST, bo);
      bo += STB; if (bo == NST * STB) bo = 0;
    }
    return;
  }
  u32 aW[2];
  {
    const int g = lane >> 4, q = (lane & 15) >> 2, p = lane & 3;
    const int swz = (q << 1) | ((g & 1) << 3);
    const int k = 8 * g + q;
#pragma unroll
    for (int i = 0; i < 2; ++i) aW[i] = ldsb + (u32)(k * 256 + (p & 1) * 8 + ((wave * 4 + 2 * i + (p >> 1)) ^ swz) * 16);
  }
  const u32 aG = ldsb + (u32)(WB + ((lane >> 4) * TD + (lane & 15)) * 16);
  f32x4 acc[2][9];
#pragma unroll
  for (int i = 0; i < 2; ++i)
#pragma unroll
    for (int j = 0; j < 9; ++j) acc[i][j] = (f32x4){0.f, 0.f, 0.f, 0.f};
  u32x4 fw[2][2], fg[2][9];
  u32x2 lo, hi;
#define G2_RDW(dst, i, base, H) do { lo = trd<8192 * (H)>((base)); hi = trd<8192 * (H) + 1024>((base)); dst = (u32x4){lo.x, lo.y, hi.x, hi.y}; } while (0)
  asm volatile("s_barrier" ::: "memory");
  G2_RDW(fw[0][0], 0, aW[0], 0); G2_RDW(fw[0][1], 1, aW[1], 0);
#define G2_RDG(dst, J, base, H) dst = rd128<9216 * (H) + 256 * (J)>(base)
  G2_RDG(fg[0][0], 0, aG, 0); G2_RDG(fg[0][1], 1, aG, 0); G2_RDG(fg[0][2], 2, aG, 0); G2_RDG(fg[0][3], 3, aG, 0); G2_RDG(fg[0][4], 4, aG, 0);
  G2_RDG(fg[0][5], 5, aG, 0); G2_RDG(fg[0][6], 6, aG, 0); G2_RDG(fg[0][7], 7, aG, 0); G2_RDG(fg[0][8], 8, aG, 0);
  u32 bo = 0;
#define G2_MM(J, C) do { acc[0][J] = mfma16(fg[C][J], fw[C][0], acc[0][J]); SB0; acc[1][J] = mfma16(fg[C][J], fw[C][1], acc[1][J]); SB0; } while (0)
#define G2_HALF(C, N, w0, w1, g, H) do { \
    G2_MM(0, C); G2_RDW(fw[N][0], 0, w0, H); SB0; \
    G2_MM(1, C); G2_RDW(fw[N][1], 1, w1, H); SB0; \
    G2_MM(2, C); G2_RDG(fg[N][0], 0, g, H); G2_RDG(fg[N][1], 1, g, H); G2_RDG(fg[N][2], 2, g, H); SB0; \
    G2_MM(3, C); G2_RDG(fg[N][3], 3, g, H); G2_RDG(fg[N][4], 4, g, H); G2_RDG(fg[N][5], 5, g, H); SB0; \
    G2_MM(4, C); G2_RDG(fg[N][6], 6, g, H); G2_RDG(fg[N][7], 7, g, H); G2_RDG(fg[N][8], 8, g, H); SB0; \
    G2_MM(5, C); G2_MM(6, C); G2_MM(7, C); G2_MM(8, C); } while (0)
#pragma unroll 1
  for (int t = 0; t < nk; ++t) {
    asm volatile("s_waitcnt lgkmcnt(0)" ::: "memory");
    SB0;
    u32 nbo = bo + STB; if (nbo == NST * STB) nbo = 0;
    {
      const u32 w0 = aW[0] + bo, w1 = aW[1] + bo, g = aG + bo;
      G2_HALF(0, 1, w0, w1, g, 1);
    }
    asm volatile("s_waitcnt lgkmcnt(0)\n\ts_barrier" ::: "memory");
    SB0;
    {
      const u32 w0 = aW[0] + nbo, w1 = aW[1] + nbo, g = aG + nbo;
      G2_HALF(1, 0, w0, w1, g, 0);
    }
    bo = nbo;
  }
  asm volatile("s_waitcnt lgkmcnt(0)" ::: "memory");
  SB0;
  switch (kq) {
    case 0: g2_finish<0>(acc, x, b2, ws, out, tile, s0, d0, wave, lane); break;
    case 1: g2_finish<1>(acc, x, b2, ws, out, tile, s0, d0, wave, lane); break;
    case 2: g2_finish<2>(acc, x, b2, ws, out, tile, s0, d0, wave, lane); break;
    default: g2_finish<3>(acc, x, b2, ws, out, tile, s0, d0, wave, lane); break;
  }
}

extern "C" void kernel_launch(void* const* d_in, const int* in_sizes, int n_in, void* d_out, int out_size, void* d_ws, size_t ws_size, hipStream_t stream) {
  const u16* x = (const u16*)d_in[0];
  const u16* wr = (const u16*)d_in[1];
  const u16* wi = (const u16*)d_in[2];
  const u16* w1 = (const u16*)d_in[4];
  const u16* b1 = (const u16*)d_in[5];
  const u16* w2 = (const u16*)d_in[6];
  const u16* b2 = (const u16*)d_in[7];
  char* ws = (char*)d_ws;
  (void)hipFuncSetAttribute((const void*)k2_gemm1, hipFuncAttributeMaxDynamicSharedMemorySize, K2_LDS);
  (void)hipFuncSetAttribute((const void*)k3_gemm2, hipFuncAttributeMaxDynamicSharedMemorySize, K3_LDS);
  k1_pre<<<K1_GRID, 256, 0, stream>>>(x, wr, wi, ws);
  k2_gemm1<<<K2_GRID, 512, K2_LDS, stream>>>(w1, b1, ws);
  k3_gemm2<<<256, 512, K3_LDS, stream>>>(w2, b2, x, ws, (u16*)d_out);
}
```

```cpp
#include <hip/hip_runtime.h>
#include <hip/hip_bf16.h>
#include <stdint.h>

typedef unsigned short u16;
typedef unsigned int u32;
using bf16x8 = __attribute__((ext_vector_type(8))) short;
using f32x4 = __attribute__((ext_vector_type(4))) float;
using f32x16 = __attribute__((ext_vector_type(16))) float;
using f32x2 = __attribute__((ext_vector_type(2))) float;
using u32x4 = __attribute__((ext_vector_type(4))) unsigned;
using u32x2 = __attribute__((ext_vector_type(2))) unsigned;
#define LAS __attribute__((address_space(3)))
#define GAS __attribute__((address_space(1)))
#define SB0 __builtin_amdgcn_sched_barrier(0)

constexpr int S = 2048, D = 576, DF = 289, F = 4608;
constexpr int KX = F + D;

constexpr size_t WS_HB = 0;
constexpr size_t WS_Z = WS_HB + (size_t)S * D * 2;
constexpr size_t WS_WT = WS_Z + (size_t)74 * 32768;
constexpr size_t WS_GB = WS_WT + (size_t)73 * 32768;
constexpr size_t WS_YT = WS_GB + (size_t)F * D * 2;
constexpr size_t WS_P = WS_YT + (size_t)D * S * 2;
constexpr size_t WS_P_BYTES = (size_t)256 * 4 * 18 * 1024;
constexpr size_t WS_FG = WS_P + WS_P_BYTES;
constexpr size_t WS_FY = WS_FG + 1024;
constexpr size_t WS_FP = WS_FY + 256;
constexpr size_t WS_END = WS_FP + 1024;

constexpr double kPi = 3.141592653589793238462643383279502884;
constexpr double c_sin_small(double x) { double t = x, s = x; for (int i = 1; i < 14; ++i) { t *= -x * x / ((2 * i) * (2 * i + 1)); s += t; } return s; }
constexpr double c_cos_small(double x) { double t = 1, s = 1; for (int i = 1; i < 14; ++i) { t *= -x * x / ((2 * i - 1) * (2 * i)); s += t; } return s; }
constexpr double c_cos(int m, int n) {
  m %= n; if (2 * m > n) m = n - m;
  if (4 * m > n) return -((8 * (n / 2 - m) > n) ? c_sin_small(2 * kPi * (m - n / 4) / n) : c_cos_small(2 * kPi * (n / 2 - m) / n));
  return (8 * m > n) ? c_sin_small(2 * kPi * (n / 4 - m) / n) : c_cos_small(2 * kPi * m / n);
}
constexpr double c_sin(int m, int n) { return c_cos((m % n) + n - n / 4, n); }
template <int N> struct TwTab { float v[2 * N]; constexpr TwTab() : v() { for (int m = 0; m < N; ++m) { v[2 * m] = (float)c_cos(m, N); v[2 * m + 1] = (float)(-c_sin(m, N)); } } };
__device__ const TwTab<576> TW576 = TwTab<576>();
constexpr TwTab<2048> TWB = TwTab<2048>();
constexpr unsigned short c_bf16(float f) { unsigned u = __builtin_bit_cast(unsigned, f); return (unsigned short)((u + 0x7fffu + ((u >> 16) & 1u)) >> 16); }
struct FftTab {
  unsigned short t1[2][4][64][8];
  unsigned short t2[2][2][4][64][8];
  unsigned short t4[3][2][64][8];
  unsigned short pad[1024];
  float tw[2][8][64][4];
  constexpr FftTab() : t1(), t2(), t4(), pad(), tw() {
    for (int l = 0; l < 64; ++l) {
      const int r = l & 31, h = l >> 5;
      for (int j = 0; j < 8; ++j) {
        for (int m = 0; m < 4; ++m) {
          const int a = 16 * h + 4 * m + (j >> 1), e = ((a * r) & 31) * 64;
          const float cr = TWB.v[2 * e], ci = TWB.v[2 * e + 1];
          t1[0][m][l][j] = c_bf16((j & 1) ? -ci : cr);
          t1[1][m][l][j] = c_bf16((j & 1) ? cr : ci);
          for (int ub = 0; ub < 2; ++ub) {
            const int b = 16 * m + 8 * (j >> 2) + 4 * h + (j & 3), e2 = (((32 * ub + r) * b) & 63) * 32;
            t2[0][ub][m][l][j] = c_bf16(TWB.v[2 * e2]);
            t2[1][ub][m][l][j] = c_bf16(TWB.v[2 * e2 + 1]);
          }
        }
        for (int ks = 0; ks < 2; ++ks) {
          const int u1 = 16 * ks + 8 * h + j, e = ((u1 * r) & 31) * 64;
          const float sc = 1.0f / (2048.0f * 576.0f);
          t4[0][ks][l][j] = c_bf16(TWB.v[2 * e] * sc);
          t4[1][ks][l][j] = c_bf16(-TWB.v[2 * e + 1] * sc);
          t4[2][ks][l][j] = c_bf16(TWB.v[2 * e + 1] * sc);
        }
      }
      for (int rb = 0; rb < 2; ++rb)
        for (int rho = 0; rho < 16; ++rho) {
          const int b = 32 * rb + (rho & 3) + 8 * (rho >> 2) + 4 * h, e = (b * r) & 2047;
          tw[rb][rho >> 1][l][2 * (rho & 1)] = TWB.v[2 * e];
          tw[rb][rho >> 1][l][2 * (rho & 1) + 1] = TWB.v[2 * e + 1];
        }
    }
  }
};
constexpr TwTab<576> TWC = TwTab<576>();
struct IdftPart {
  unsigned short v[8][576][8];
  constexpr IdftPart(int e) : v() {
    for (int kc = 0; kc < 8; ++kc)
      for (int j = 0; j < 4; ++j) {
        const int k = 32 * e + 4 * kc + j;
        int idx = 0;
        for (int d = 0; d < 576; ++d) {
          v[kc][d][2 * j] = c_bf16(k ? 2.f * TWC.v[2 * idx] : 1.f);
          v[kc][d][2 * j + 1] = c_bf16(k ? 2.f * TWC.v[2 * idx + 1] : ((d & 1) ? -1.f : 1.f));
          idx += k; if (idx >= 576) idx -= 576;
        }
      }
  }
};
constexpr IdftPart IP0(0), IP1(1), IP2(2), IP3(3), IP4(4), IP5(5), IP6(6), IP7(7), IP8(8);
__device__ const __attribute__((aligned(16))) IdftPart IDFT[9] = {IP0, IP1, IP2, IP3, IP4, IP5, IP6, IP7, IP8};
static_assert(sizeof(FftTab) == 49152, "table is moved in whole 1 KB pieces");
__device__ const __attribute__((aligned(16))) FftTab FTAB = FftTab();

struct cf { float x, y; };
__device__ __forceinline__ cf cadd(cf a, cf b) { return {a.x + b.x, a.y + b.y}; }
__device__ __forceinline__ cf csub(cf a, cf b) { return {a.x - b.x, a.y - b.y}; }
__device__ __forceinline__ cf cmul(cf a, cf w) { return {__builtin_fmaf(a.x, w.x, -a.y * w.y), __builtin_fmaf(a.x, w.y, a.y * w.x)}; }
__device__ __forceinline__ cf cmulc(cf a, cf w) { return {__builtin_fmaf(a.x, w.x, a.y * w.y), __builtin_fmaf(a.y, w.x, -a.x * w.y)}; }
__device__ __forceinline__ cf ldtw(const float* t, int m) { f32x2 v = *(const f32x2*)(t + 2 * m); return {v.x, v.y}; }

__device__ __forceinline__ float bflo(u32 v) { return __uint_as_float(v << 16); }
__device__ __forceinline__ float bfhi(u32 v) { return __uint_as_float(v & 0xffff0000u); }
__device__ __forceinline__ u32 pack_bf16(float a, float b) {
  typedef __attribute__((ext_vector_type(2))) __bf16 bf2;
  f32x2 v = {a, b};
  return __builtin_bit_cast(u32, __builtin_convertvector(v, bf2));
}
__device__ __forceinline__ float rbf(float a) { return bflo(pack_bf16(a, a)); }

template <int CTRL> __device__ __forceinline__ float dppf(float v) {
  return __int_as_float(__builtin_amdgcn_update_dpp(0, __float_as_int(v), CTRL, 0xf, 0xf, true));
}
constexpr int DPP_X1 = 0xB1, DPP_X2 = 0x4E, DPP_X7 = 0x141, DPP_X8 = 0x128;
__device__ __forceinline__ float xor16f(float v) { return __int_as_float(__builtin_amdgcn_ds_swizzle(__float_as_int(v), 0x401F)); }
__device__ __forceinline__ float half_sum(float v) {
  v += dppf<DPP_X1>(v); v += dppf<DPP_X2>(v); v += dppf<DPP_X7>(v); v += dppf<DPP_X8>(v); v += xor16f(v); return v;
}

__device__ __forceinline__ u32x4 make_srd(const void* p, u32 bytes) {
  uint64_t a = (uint64_t)p; u32x4 r;
  r.x = __builtin_amdgcn_readfirstlane((u32)a);
  r.y = __builtin_amdgcn_readfirstlane((u32)(a >> 32)) & 0xffffu;
  r.z = bytes; r.w = 0x00020000u; return r;
}
__device__ __forceinline__ void dma16(u32 ldsaddr, u32 voff, u32x4 srd, u32 soff) {
  asm volatile("s_mov_b32 m0, %0\n\ts_nop 0\n\tbuffer_load_dwordx4 %1, %2, %3 offen lds" ::"s"(ldsaddr), "v"(voff), "s"(srd), "s"(soff) : "memory");
}
template <int OFF> __device__ __forceinline__ u32x2 trd(u32 addr) {
  u32x2 r; asm volatile("ds_read_b64_tr_b16 %0, %1 offset:%2" : "=v"(r) : "v"(addr), "n"(OFF) : "memory"); return r;
}
template <int OFF> __device__ __forceinline__ u32x4 rd128(u32 addr) {
  u32x4 r; asm volatile("ds_read_b128 %0, %1 offset:%2" : "=v"(r) : "v"(addr), "n"(OFF) : "memory"); return r;
}
__device__ __forceinline__ f32x4 mfma16(u32x4 a, u32x4 b, f32x4 c) {
  return __builtin_amdgcn_mfma_f32_16x16x32_bf16(__builtin_bit_cast(bf16x8, a), __builtin_bit_cast(bf16x8, b), c, 0, 0, 0);
}

#define PROF_STAMP_W(kern, w, slot) do { } while (0)
#define PROF_STAMP(kern, slot) do { } while (0)

__device__ __forceinline__ f32x16 mfma32(u32x4 a, u32x4 b, f32x16 c) {
  return __builtin_amdgcn_mfma_f32_32x32x16_bf16(__builtin_bit_cast(bf16x8, a), __builtin_bit_cast(bf16x8, b), c, 0, 0, 0);
}
typedef unsigned long long u64;
typedef GAS u64 gu64;
constexpr int G1_WGS = 216, FFT_WGS = 37;
constexpr int L_W = 0;
constexpr int L_X = 81920;
constexpr int L_ZC = 0, L_HROW = 18432, L_WTS = 28160, L_SYNC = 44544, L_PF1 = 44608, K1_LDS = 44864;
constexpr int L_EX = 3 * 16384;
constexpr int FFT_TAB = 0, FFT_WAVE = 49152, FFT_WAVE_B = 9216, FFT_ZS = 49152;
constexpr int L_PF = 155648;
constexpr int MK_LDS = 155904;
#define PF1 16
#define PFW2 2
#define PFW2S 5
#define PFLA 20
constexpr unsigned SPIN_CAP = 1u << 16;

#define BAR() asm volatile("s_barrier" ::: "memory")
#define WAITV(n) asm volatile("s_waitcnt vmcnt(%0)" ::"n"(n) : "memory")
#define WL(n) do { asm volatile("s_waitcnt lgkmcnt(" #n ")" ::: "memory"); SB0; } while (0)

__device__ __forceinline__ void dma16s(u32 ldsaddr, u32 voff, u32x4 srd, u32 soff) {
  asm volatile("s_mov_b32 m0, %0\n\ts_nop 0\n\tbuffer_load_dwordx4 %1, %2, %3 offen sc1 lds" ::"s"(ldsaddr), "v"(voff), "s"(srd), "s"(soff) : "memory");
}
__device__ __forceinline__ void touch(u32 ldsaddr, u32 voff, u32x4 srd, u32 soff) {
  asm volatile("s_mov_b32 m0, %0\n\ts_nop 0\n\tbuffer_load_dword %1, %2, %3 offen lds" ::"s"(ldsaddr), "v"(voff), "s"(srd), "s"(soff) : "memory");
}
typedef __amdgpu_buffer_rsrc_t rsrc_t;
__device__ __forceinline__ void st16(rsrc_t r, u32 off, u32x4 v) { __builtin_amdgcn_raw_buffer_store_b128(v, r, off, 0, 16); }
__device__ __forceinline__ void st8(rsrc_t r, u32 off, u32x2 v) { __builtin_amdgcn_raw_buffer_store_b64(v, r, off, 0, 16); }
__device__ __forceinline__ u32x4 ld16(rsrc_t r, u32 off) { return __builtin_amdgcn_raw_buffer_load_b128(r, off, 0, 16); }
__device__ __forceinline__ u32 ld4(rsrc_t r, u32 off) { return __builtin_amdgcn_raw_buffer_load_b32(r, off, 0, 16); }
typedef GAS u32 gu32;
__device__ __forceinline__ void set_flag(char* ws, size_t off) { __hip_atomic_store((gu32*)(ws + off), 1u, __ATOMIC_RELAXED, __HIP_MEMORY_SCOPE_AGENT); }
#define K1AUX 0
__device__ __forceinline__ void k1st(rsrc_t r, u32 off, u32x4 v) { __builtin_amdgcn_raw_buffer_store_b128(v, r, off, 0, K1AUX); }
__device__ __forceinline__ unsigned lds_arrive(char* smem, int ctr, int lane) {
  unsigned old = 0;
  asm volatile("s_waitcnt lgkmcnt(0)" ::: "memory");
  if (lane == 0) old = __hip_atomic_fetch_add((LAS unsigned*)(smem + L_SYNC + 4 * ctr), 1u, __ATOMIC_RELAXED, __HIP_MEMORY_SCOPE_WORKGROUP);
  return __builtin_amdgcn_readfirstlane(old);
}
__device__ __forceinline__ void lds_wait(char* smem, int ctr, unsigned n) {
  for (unsigned sp = 0; sp < (1u << 22); ++sp) {
    if (__builtin_amdgcn_readfirstlane(__hip_atomic_load((LAS unsigned*)(smem + L_SYNC + 4 * ctr), __ATOMIC_RELAXED, __HIP_MEMORY_SCOPE_WORKGROUP)) >= n) break;
    __builtin_amdgcn_s_sleep(1);
  }
  asm volatile("" ::: "memory");
}
__device__ __forceinline__ void wait_lanes(char* ws, u32 off) {
  bool ok = off == ~0u;
  for (unsigned sp = 0; sp < SPIN_CAP; ++sp) {
    if (!ok) ok = __hip_atomic_load((gu32*)(ws + off), __ATOMIC_RELAXED, __HIP_MEMORY_SCOPE_AGENT) != 0u;
    if (__all(ok)) break;
    __builtin_amdgcn_s_sleep(1);
  }
}

__device__ __forceinline__ u32 w1_line(const int xg, const int n) {
  const int t = n / 640, r = n - t * 640, k = r / 10, c = r - k * 10;
  const int col = ((xg * 27) / 6) * 256 + c * 128;
  return (t < 32 && col < F * 2) ? (u32)((8 * (t & 7) + 512 * (t >> 3) + (k >> 3) + 64 * (k & 7)) * (F * 2) + col) : 0xfffffff0u;
}
__device__ __forceinline__ void dft3(cf& a, cf& b, cf& c) {
  const float h = 0.8660254037844386f;
  cf s = cadd(b, c), d = csub(b, c);
  cf m = {__builtin_fmaf(-0.5f, s.x, a.x), __builtin_fmaf(-0.5f, s.y, a.y)};
  a = cadd(a, s);
  b = {__builtin_fmaf(h, d.y, m.x), __builtin_fmaf(-h, d.x, m.y)};
  c = {__builtin_fmaf(-h, d.y, m.x), __builtin_fmaf(h, d.x, m.y)};
}
template <int CTRL> __device__ __forceinline__ cf bfly_dpp(cf v, float sg) {
  return {__builtin_fmaf(dppf<CTRL>(v.x), sg, v.x), __builtin_fmaf(dppf<CTRL>(v.y), sg, v.y)};
}

__device__ __forceinline__ void phase1_rows(char* smem, const u16* __restrict__ x, char* ws, rsrc_t rw, const int bid, const int tid) {
  const int lane = tid & 63, wave = tid >> 6;
  cf* zc = (cf*)(smem + L_ZC);
  char* hrow = smem + L_HROW;
  const int p = lane & 31, r = 2 * wave + (lane >> 5);
  const int l = p ^ ((p & 4) ? 3 : 0);
  const u32* xr = (const u32*)(x + (size_t)((bid & 63) + 64 * (8 * (bid >> 6) + r)) * D);
  u32 raw[9];
#pragma unroll
  for (int j = 0; j < 9; ++j) raw[j] = xr[l + 32 * j];
  cf tw1[9];
#pragma unroll
  for (int k2 = 1; k2 < 9; ++k2) tw1[k2] = ldtw(TW576.v, 2 * l * k2);
  cf ts4 = ldtw(TW576.v, 18 * (l & 15)), ts3 = ldtw(TW576.v, 36 * (l & 7)), ts2 = ldtw(TW576.v, 72 * (l & 3));
  BAR();
  float e[9], o[9];
#pragma unroll
  for (int j = 0; j < 9; ++j) { e[j] = bflo(raw[j]); o[j] = bfhi(raw[j]); }
#pragma unroll
  for (int pass = 0; pass < 2; ++pass) {
    float s = 0;
#pragma unroll
    for (int j = 0; j < 9; ++j) s += e[j] + o[j];
    const float mean = half_sum(s) * (1.0f / D);
    float q = 0;
#pragma unroll
    for (int j = 0; j < 9; ++j) { const float a = e[j] - mean, b = o[j] - mean; q = __builtin_fmaf(a, a, q); q = __builtin_fmaf(b, b, q); }
    const float var = half_sum(q) * (1.0f / D);
    const float m = rbf(mean);
    const float inv = __builtin_amdgcn_rcpf(rbf(__builtin_sqrtf(rbf(rbf(var) + 1e-5f))));
#pragma unroll
    for (int j = 0; j < 9; ++j) {
      const u32 d = pack_bf16(e[j] - m, o[j] - m);
      const u32 n = pack_bf16(bflo(d) * inv, bfhi(d) * inv);
      if (pass == 0) { e[j] = bflo(n); o[j] = bfhi(n); }
      else *(u32*)(hrow + r * 1216 + (l + 32 * j) * 4) = n;
    }
  }
  PROF_STAMP(0, 1);
  lds_arrive(smem, 2, lane);
  cf z[9];
#pragma unroll
  for (int j = 0; j < 9; ++j) z[j] = {e[j], o[j]};
  dft3(z[0], z[3], z[6]); dft3(z[1], z[4], z[7]); dft3(z[2], z[5], z[8]);
  {
    const cf w1 = {0.766044443118978f, -0.6427876096865393f}, w2 = {0.17364817766693041f, -0.984807753012208f}, w4 = {-0.9396926207859083f, -0.3420201433256689f};
    z[4] = cmul(z[4], w1); z[7] = cmul(z[7], w2); z[5] = cmul(z[5], w2); z[8] = cmul(z[8], w4);
  }
  dft3(z[0], z[1], z[2]); dft3(z[3], z[4], z[5]); dft3(z[6], z[7], z[8]);
  cf y[9];
#pragma unroll
  for (int c = 0; c < 3; ++c)
#pragma unroll
    for (int d = 0; d < 3; ++d) y[c + 3 * d] = z[3 * c + d];
#pragma unroll
  for (int k2 = 1; k2 < 9; ++k2) y[k2] = cmul(y[k2], tw1[k2]);
  {
    const bool u4 = l & 16, u3 = l & 8, u2 = l & 4, u1 = l & 2, u0 = l & 1;
    const float s4 = u4 ? -1.f : 1.f, s3 = u3 ? -1.f : 1.f, s2 = u2 ? -1.f : 1.f, s1 = u1 ? -1.f : 1.f, s0 = u0 ? 1.f : -1.f;
    ts4 = u4 ? cf{-ts4.x, -ts4.y} : cf{1.f, 0.f};
    ts3 = u3 ? cf{-ts3.x, -ts3.y} : cf{1.f, 0.f};
    ts2 = u2 ? cf{-ts2.x, -ts2.y} : cf{1.f, 0.f};
    const cf ts1 = u1 ? (u0 ? cf{0.f, -1.f} : cf{-1.f, 0.f}) : (u0 ? cf{-1.f, 0.f} : cf{1.f, 0.f});
#pragma unroll
    for (int k = 0; k < 9; ++k) { cf t = {__builtin_fmaf(xor16f(y[k].x), s4, y[k].x), __builtin_fmaf(xor16f(y[k].y), s4, y[k].y)}; y[k] = cmul(t, ts4); }
#pragma unroll
    for (int k = 0; k < 9; ++k) y[k] = cmul(bfly_dpp<DPP_X8>(y[k], s3), ts3);
#pragma unroll
    for (int k = 0; k < 9; ++k) y[k] = cmul(bfly_dpp<DPP_X7>(y[k], s2), ts2);
#pragma unroll
    for (int k = 0; k < 9; ++k) y[k] = cmul(bfly_dpp<DPP_X2>(y[k], s1), ts1);
#pragma unroll
    for (int k = 0; k < 9; ++k) y[k] = bfly_dpp<DPP_X1>(y[k], s0);
  }
  {
    const int k1 = __builtin_bitreverse32((u32)l) >> 27;
#pragma unroll
    for (int k2 = 0; k2 < 9; ++k2) zc[r * 288 + 9 * k1 + k2] = y[k2];
  }
  PROF_STAMP(0, 2);
  lds_arrive(smem, 3, lane);
}
template <int NT> __device__ __forceinline__ void phase1_unpack(char* smem, rsrc_t rw, const int bid, const int t) {
  constexpr int NR = (290 + NT - 1) / NT;
  const cf* zc = (const cf*)(smem + L_ZC);
  cf wk[NR];
#pragma unroll
  for (int rd = 0; rd < NR; ++rd) wk[rd] = ldtw(TW576.v, (t + NT * rd) >> 1);
  lds_wait(smem, 3, 4);
  const u32 zo = (u32)WS_Z + ((bid & 63) * 4 + (bid >> 6)) * 128;
#pragma unroll
  for (int rd = 0; rd < NR; ++rd) {
    const int it = t + NT * rd, k = it >> 1, rq = it & 1;
    if (k <= 144) {
      u32x4 v0, v1;
#pragma unroll
      for (int e2 = 0; e2 < 4; ++e2) {
        const int row = 4 * rq + e2;
        const cf a = zc[row * 288 + k], b = zc[row * 288 + (k ? 288 - k : 0)];
        const cf E = {0.5f * (a.x + b.x), 0.5f * (a.y - b.y)};
        const cf O = {0.5f * (a.y + b.y), 0.5f * (b.x - a.x)};
        const cf T = cmul(O, wk[rd]);
        v0[e2] = pack_bf16(E.x + T.x, E.y + T.y);
        v1[e2] = pack_bf16(E.x - T.x, T.y - E.y);
      }
      k1st(rw, zo + (k >> 2) * 32768 + (k & 3) * 32 + rq * 16, v0);
      k1st(rw, zo + ((288 - k) >> 2) * 32768 + ((288 - k) & 3) * 32 + rq * 16, v1);
    }
  }
}

__device__ __forceinline__ void phase1_h(char* smem, rsrc_t rw, const int bid, const int L, const int lane) {
  const u32 hb = (u32)(size_t)(smem + L_HROW);
  const int g = lane >> 4, q = (lane & 15) >> 2, pp = lane & 3;
  const u32 a = hb + q * 1216 + (320 * L + 16 * g + 4 * pp) * 2;
  u32x2 t[10];
  asm volatile("ds_read_b64_tr_b16 %0, %10\n\tds_read_b64_tr_b16 %1, %10 offset:4864\n\tds_read_b64_tr_b16 %2, %10 offset:128\n\tds_read_b64_tr_b16 %3, %10 offset:4992\n\t"
               "ds_read_b64_tr_b16 %4, %10 offset:256\n\tds_read_b64_tr_b16 %5, %10 offset:5120\n\tds_read_b64_tr_b16 %6, %10 offset:384\n\tds_read_b64_tr_b16 %7, %10 offset:5248\n\t"
               "ds_read_b64_tr_b16 %8, %10 offset:512\n\tds_read_b64_tr_b16 %9, %10 offset:5376\n\ts_waitcnt lgkmcnt(0)"
               : "=&v"(t[0]), "=&v"(t[1]), "=&v"(t[2]), "=&v"(t[3]), "=&v"(t[4]), "=&v"(t[5]), "=&v"(t[6]), "=&v"(t[7]), "=&v"(t[8]), "=&v"(t[9]) : "v"(a) : "memory");
#pragma unroll
  for (int i = 0; i < 5; ++i)
    if (i < 4 || L == 0) k1st(rw, (u32)WS_HB + (bid * D + 320 * L + 64 * i + lane) * 16, (u32x4){t[2 * i].x, t[2 * i].y, t[2 * i + 1].x, t[2 * i + 1].y});
}
__device__ __forceinline__ void phase1_wt_load(char* stg, const u16* __restrict__ w, const int unit, const int lane) {
  const u32x4 sr = make_srd(w, S * DF * 2);
  const u32 base = (u32)(size_t)stg;
  const u32 row0 = (u32)(2 * (unit & 15) + 128 * (unit >> 4)) * (DF * 2);
#pragma unroll
  for (int e = 0; e < 4; ++e)
#pragma unroll
    for (int j = 0; j < 2; ++j)
      dma16(base + e * 2048 + j * 1024, lane * 16, sr, row0 + e * (32 * DF * 2) + j * 1024);
}
__device__ __forceinline__ void phase1_wt(char* stg, rsrc_t rw, const int unit, const int H, const int lane) {
  const u16* pr = (const u16*)stg; const u16* pi = (const u16*)(stg + 8192);
  const int i = lane & 1;
  const u32 dst = (u32)WS_WT + unit * 128 + (lane & 7) * 16 + ((lane >> 3) + 40 * H) * 32768;
#pragma unroll
  for (int rd = 0; rd < 5; ++rd) {
    const int k = 160 * H + 32 * rd + (lane >> 1);
    if (k < DF) {
      u32x4 v;
#pragma unroll
      for (int e = 0; e < 4; ++e) { const int idx = e * 1024 + i * DF + k; v[e] = (u32)pr[idx] | ((u32)pi[idx] << 16); }
      k1st(rw, dst + rd * (8 * 32768), v);
    }
  }
}

__global__ __launch_bounds__(512) void k1_rows(const u16* __restrict__ x, const u16* __restrict__ wr, const u16* __restrict__ wi, const u16* __restrict__ w1, char* ws) {
  __shared__ __attribute__((aligned(16))) char smem[K1_LDS];
  const int bid = blockIdx.x;
  const int tid = threadIdx.x, lane = tid & 63;
  const int wave = __builtin_amdgcn_readfirstlane(tid >> 6);
  const int L = wave & 1;
  PROF_STAMP(0, 0);
  const rsrc_t rw = __builtin_amdgcn_make_buffer_rsrc(ws, 0, (int)WS_END, 0x00020000);
  if (wave < 4) {
    phase1_rows(smem, x, ws, rw, bid, tid);
    phase1_unpack<256>(smem, rw, bid, tid);
  } else if (wave < 6) {
    if (bid < 9 && wave == 4) *(u32*)(ws + WS_FG + bid * 256 + lane * 4) = 0u;
    BAR();
    if (PF1) {
      const u32x4 srdW1 = make_srd(w1, (u32)((size_t)S * F * 2));
#pragma unroll 1
      for (int n = ((bid >> 3) * 2 + L) * 64; n < PF1 * 640; n += 4096) touch((u32)(size_t)smem + L_PF1, w1_line(bid & 7, n + lane), srdW1, 0);
    }
    lds_wait(smem, 2, 4);
    phase1_h(smem, rw, bid, L, lane);
  } else {
    if (wave == 7 && lane < 16) *(u32*)(smem + L_SYNC + lane * 4) = 0u;
    phase1_wt_load(smem + L_WTS + L * 8192, L ? wi : wr, bid, lane);
    asm volatile("s_waitcnt lgkmcnt(0)\n\ts_barrier" ::: "memory");
    WAITV(0);
    lds_arrive(smem, 6, lane);
    lds_wait(smem, 6, 2);
    phase1_wt(smem + L_WTS, rw, bid, L, lane);
  }
  PROF_STAMP(0, 3);
  PROF_STAMP(0, 4);
}

__device__ __forceinline__ void pack_frags(const f32x16& v, u32x4& f0, u32x4& f1) {
  f0 = (u32x4){pack_bf16(v[0], v[1]), pack_bf16(v[2], v[3]), pack_bf16(v[4], v[5]), pack_bf16(v[6], v[7])};
  f1 = (u32x4){pack_bf16(v[8], v[9]), pack_bf16(v[10], v[11]), pack_bf16(v[12], v[13]), pack_bf16(v[14], v[15])};
}
template <int INV> __device__ __forceinline__ void dft64(const char* tab, const int lane, f32x16 (&re)[2], f32x16 (&im)[2]) {
  u32x4 br[4], bi[4], bn[4];
  pack_frags(re[0], br[0], br[1]); pack_frags(re[1], br[2], br[3]);
  pack_frags(im[0], bi[0], bi[1]); pack_frags(im[1], bi[2], bi[3]);
#pragma unroll
  for (int m = 0; m < 4; ++m) bn[m] = (INV ? br[m] : bi[m]) ^ 0x80008000u;
  const u32x4* t2 = (const u32x4*)(tab + 8192) + lane;
#pragma unroll
  for (int ub = 0; ub < 2; ++ub) {
    f32x16 xr = {}, xi = {};
#pragma unroll
    for (int m = 0; m < 4; ++m) {
      const u32x4 fr = t2[(ub * 4 + m) * 64], fi = t2[(8 + ub * 4 + m) * 64];
      xr = mfma32(fr, br[m], xr);
      xi = mfma32(fr, bi[m], xi);
      xr = mfma32(fi, INV ? bi[m] : bn[m], xr);
      xi = mfma32(fi, INV ? bn[m] : br[m], xi);
    }
    re[ub] = xr; im[ub] = xi;
  }
}
template <int INV> __device__ __forceinline__ void twid(const char* tab, const int lane, f32x16 (&re)[2], f32x16 (&im)[2]) {
  const f32x4* tw = (const f32x4*)(tab + 32768) + lane;
#pragma unroll
  for (int rb = 0; rb < 2; ++rb)
#pragma unroll
    for (int p = 0; p < 8; ++p) {
      const f32x4 w = tw[(rb * 8 + p) * 64];
      cf a = {re[rb][2 * p], im[rb][2 * p]}, b = {re[rb][2 * p + 1], im[rb][2 * p + 1]};
      a = INV ? cmulc(a, cf{w.x, w.y}) : cmul(a, cf{w.x, w.y});
      b = INV ? cmulc(b, cf{w.z, w.w}) : cmul(b, cf{w.z, w.w});
      re[rb][2 * p] = a.x; im[rb][2 * p] = a.y; re[rb][2 * p + 1] = b.x; im[rb][2 * p + 1] = b.y;
    }
}
#define WAVE_LDS_FENCE() do { __builtin_amdgcn_fence(__ATOMIC_RELEASE, "wavefront"); __builtin_amdgcn_wave_barrier(); __builtin_amdgcn_fence(__ATOMIC_ACQUIRE, "wavefront"); } while (0)

__device__ __forceinline__ void fft_role(char* smem, char* ws, rsrc_t rw, const int fb, const int wave, const int lane) {
  const int kk = fb * 8 + wave, k = kk < DF ? kk : DF - 1;
  const int r = lane & 31, h = lane >> 5;
  {
    const u32x4 srdWS = make_srd(ws, (u32)WS_END), srdT = make_srd(&FTAB, 49152);
    const u32 ldsb = (u32)(size_t)smem;
#pragma unroll
    for (int j = 0; j < 8; ++j) {
      const int q = lane ^ (2 * j);
      dma16(ldsb + FFT_ZS + (wave * 8 + j) * 1024, (u32)(((q & 1) * 32 + (q >> 1)) * 16), srdWS, (u32)WS_Z + (u32)fb * 65536 + (wave * 8 + j) * 1024);
    }
#pragma unroll
    for (int j = 0; j < 6; ++j) dma16(ldsb + FFT_TAB + (wave * 6 + j) * 1024, lane * 16, srdT, (wave * 6 + j) * 1024);
  }
  WAITV(0); BAR();
  u32x4 wv[2][4];
  {
    const u32 wo = (u32)WS_WT + (k >> 2) * 32768 + (h * 16 + (r >> 1)) * 128 + (k & 3) * 32 + (r & 1) * 16;
#pragma unroll
    for (int ub = 0; ub < 2; ++ub)
#pragma unroll
      for (int g = 0; g < 4; ++g) wv[ub][g] = __builtin_amdgcn_raw_buffer_load_b128(rw, wo + (8 * ub + 2 * g) * 2048, 0, 0);
  }
  u32x4 za[2][4];
  {
    const int kl = k - fb * 8;
    const char* zs = smem + FFT_ZS + (kl >> 2) * 32768 + (r >> 1) * 1024;
#pragma unroll
    for (int rb = 0; rb < 2; ++rb)
#pragma unroll
      for (int m = 0; m < 4; ++m) {
        const int c = 2 * h + (m >> 1), slot = (kl & 3) * 2 + (m & 1);
        za[rb][m] = *(const u32x4*)(zs + rb * 16384 + ((((8 * c + slot) * 2 + (r & 1)) ^ (2 * ((r >> 1) & 7))) * 16));
      }
  }
  asm volatile("s_waitcnt lgkmcnt(0)\n\ts_barrier" : "+v"(za[0][0]), "+v"(za[0][1]), "+v"(za[0][2]), "+v"(za[0][3]), "+v"(za[1][0]), "+v"(za[1][1]), "+v"(za[1][2]), "+v"(za[1][3]) :: "memory");
  const char* tab = smem + FFT_TAB;
  char* img = smem + FFT_WAVE + wave * FFT_WAVE_B;
  f32x16 re[2], im[2];
  {
    const u32x4* t1 = (const u32x4*)tab + lane;
    re[0] = f32x16{}; re[1] = f32x16{}; im[0] = f32x16{}; im[1] = f32x16{};
#pragma unroll
    for (int m = 0; m < 4; ++m) {
      const u32x4 fr = t1[m * 64], fi = t1[(4 + m) * 64];
      re[0] = mfma32(za[0][m], fr, re[0]); im[0] = mfma32(za[0][m], fi, im[0]);
      re[1] = mfma32(za[1][m], fr, re[1]); im[1] = mfma32(za[1][m], fi, im[1]);
    }
  }
  twid<0>(tab, lane, re, im);
  dft64<0>(tab, lane, re, im);
#pragma unroll
  for (int ub = 0; ub < 2; ++ub)
#pragma unroll
    for (int rho = 0; rho < 16; ++rho) {
      const cf p = cmul(cf{re[ub][rho], im[ub][rho]}, cf{bflo(wv[ub][rho >> 2][rho & 3]), bfhi(wv[ub][rho >> 2][rho & 3])});
      re[ub][rho] = p.x; im[ub][rho] = p.y;
    }
  dft64<1>(tab, lane, re, im);
  twid<1>(tab, lane, re, im);
#pragma unroll
  for (int rb = 0; rb < 2; ++rb)
#pragma unroll
    for (int g = 0; g < 4; ++g) {
      char* p = img + r * 136 + (32 * rb + 8 * g + 4 * h) * 2;
      *(u32x2*)p = (u32x2){pack_bf16(re[rb][4 * g], re[rb][4 * g + 1]), pack_bf16(re[rb][4 * g + 2], re[rb][4 * g + 3])};
      *(u32x2*)(p + 4352) = (u32x2){pack_bf16(im[rb][4 * g], im[rb][4 * g + 1]), pack_bf16(im[rb][4 * g + 2], im[rb][4 * g + 3])};
    }
  u32x4 va[2][2][2];
  {
    const int blk = (lane >> 4) & 1, q = (lane & 15) >> 2, p = lane & 3;
    const u32 a = (u32)(size_t)img + (8 * h + q) * 136 + (16 * blk + 4 * p) * 2;
    u32x2 t[16];
    asm volatile("s_waitcnt lgkmcnt(0)\n\t"
                 "ds_read_b64_tr_b16 %0, %16\n\tds_read_b64_tr_b16 %1, %16 offset:544\n\tds_read_b64_tr_b16 %2, %16 offset:2176\n\tds_read_b64_tr_b16 %3, %16 offset:2720\n\t"
                 "ds_read_b64_tr_b16 %4, %16 offset:64\n\tds_read_b64_tr_b16 %5, %16 offset:608\n\tds_read_b64_tr_b16 %6, %16 offset:2240\n\tds_read_b64_tr_b16 %7, %16 offset:2784\n\t"
                 "ds_read_b64_tr_b16 %8, %16 offset:4352\n\tds_read_b64_tr_b16 %9, %16 offset:4896\n\tds_read_b64_tr_b16 %10, %16 offset:6528\n\tds_read_b64_tr_b16 %11, %16 offset:7072\n\t"
                 "ds_read_b64_tr_b16 %12, %16 offset:4416\n\tds_read_b64_tr_b16 %13, %16 offset:4960\n\tds_read_b64_tr_b16 %14, %16 offset:6592\n\tds_read_b64_tr_b16 %15, %16 offset:7136\n\t"
                 "s_waitcnt lgkmcnt(0)"
                 : "=&v"(t[0]), "=&v"(t[1]), "=&v"(t[2]), "=&v"(t[3]), "=&v"(t[4]), "=&v"(t[5]), "=&v"(t[6]), "=&v"(t[7]),
                   "=&v"(t[8]), "=&v"(t[9]), "=&v"(t[10]), "=&v"(t[11]), "=&v"(t[12]), "=&v"(t[13]), "=&v"(t[14]), "=&v"(t[15])
                 : "v"(a) : "memory");
#pragma unroll
    for (int i = 0; i < 8; ++i) va[i >> 2][(i >> 1) & 1][i & 1] = (u32x4){t[2 * i].x, t[2 * i].y, t[2 * i + 1].x, t[2 * i + 1].y};
  }
  {
    const u32x4* t4 = (const u32x4*)(tab + 24576) + lane;
    re[0] = f32x16{}; re[1] = f32x16{}; im[0] = f32x16{}; im[1] = f32x16{};
#pragma unroll
    for (int ks = 0; ks < 2; ++ks) {
      const u32x4 gr = t4[ks * 64], gi = t4[(2 + ks) * 64], gn = t4[(4 + ks) * 64];
#pragma unroll
      for (int rb = 0; rb < 2; ++rb) {
        re[rb] = mfma32(va[0][rb][ks], gr, re[rb]); im[rb] = mfma32(va[0][rb][ks], gi, im[rb]);
        re[rb] = mfma32(va[1][rb][ks], gn, re[rb]); im[rb] = mfma32(va[1][rb][ks], gr, im[rb]);
      }
    }
  }
#pragma unroll
  for (int rb = 0; rb < 2; ++rb)
#pragma unroll
    for (int g = 0; g < 4; ++g) {
      char* p = img + r * 144 + (32 * rb + 8 * g + 4 * h) * 2;
      *(u32x2*)p = (u32x2){pack_bf16(re[rb][4 * g], re[rb][4 * g + 1]), pack_bf16(re[rb][4 * g + 2], re[rb][4 * g + 3])};
      *(u32x2*)(p + 4608) = (u32x2){pack_bf16(im[rb][4 * g], im[rb][4 * g + 1]), pack_bf16(im[rb][4 * g + 2], im[rb][4 * g + 3])};
    }
  WAVE_LDS_FENCE();
  if (kk < DF) {
    const int rowRe = (k == DF - 1) ? 1 : 2 * k;
#pragma unroll
    for (int i = 0; i < 4; ++i) st16(rw, (u32)WS_YT + (rowRe * 256 + 64 * i + lane) * 16, *(const u32x4*)(img + (8 * i + (lane >> 3)) * 144 + (lane & 7) * 16));
    if (k > 0 && k < DF - 1) {
#pragma unroll
      for (int i = 0; i < 4; ++i) st16(rw, (u32)WS_YT + ((2 * k + 1) * 256 + 64 * i + lane) * 16, *(const u32x4*)(img + 4608 + (8 * i + (lane >> 3)) * 144 + (lane & 7) * 16));
    }
  }
}

__device__ __forceinline__ void g1_store(const f32x4 a, const u32x2 bb, rsrc_t rw, const int fb0, const int d) {
  const float bv[4] = {bflo(bb.x), bfhi(bb.x), bflo(bb.y), bfhi(bb.y)};
  float r[4];
#pragma unroll
  for (int e = 0; e < 4; ++e) {
    const float u = a[e] + bv[e];
    const float z = u * __builtin_fmaf(u * u, -0.044715f * 2.0f * 0.7978845608028654f * 1.4426950408889634f, -2.0f * 0.7978845608028654f * 1.4426950408889634f);
    r[e] = u * __builtin_amdgcn_rcpf(1.0f + __builtin_amdgcn_exp2f(z));
  }
  st8(rw, (u32)WS_GB + ((fb0 >> 3) * D + d) * 16 + (fb0 & 4) * 2, (u32x2){pack_bf16(r[0], r[1]), pack_bf16(r[2], r[3])});
}

constexpr int g2_owner(int i, int j) { return j < 8 ? (j >> 1) : i; }
template <int Q>
__device__ __forceinline__ void g2_finish(f32x4 (&acc)[2][9], const u16* __restrict__ x, const u16* __restrict__ b2, char* ws, rsrc_t rw, u16* __restrict__ out,
                                          const int tile, const int s0, const int d0, const int wave, const int lane) {
  const int srow = s0 + wave * 32 + (lane & 15), dcol = d0 + 4 * (lane >> 4);
  u32x2 xr[2][9]; float bs[2];
#pragma unroll
  for (int i = 0; i < 2; ++i) {
    bs[i] = bflo((u32)b2[srow + 16 * i]);
#pragma unroll
    for (int j = 0; j < 9; ++j)
      if (g2_owner(i, j) == Q) xr[i][j] = *(const u32x2*)(x + (size_t)(srow + 16 * i) * D + dcol + 16 * j);
  }
  const u32 mine = (u32)WS_P + (u32)(((tile * 4 + Q) * 4 + wave) * 18) * 1024 + lane * 16;
#pragma unroll
  for (int i = 0; i < 2; ++i)
#pragma unroll
    for (int j = 0; j < 9; ++j)
      if (g2_owner(i, j) != Q) st16(rw, mine + (i * 9 + j) * 1024, __builtin_bit_cast(u32x4, acc[i][j]));
  asm volatile("s_waitcnt vmcnt(0)\n\ts_barrier" ::: "memory");
  PROF_STAMP(2, 3);
  if (wave == 0) {
    if (lane == 0) set_flag(ws, WS_FP + (tile * 4 + Q) * 4);
    wait_lanes(ws, (lane < 4 && lane != Q) ? (u32)WS_FP + (tile * 4 + lane) * 4 : ~0u);
  }
  BAR();
  PROF_STAMP(2, 4);
  u32x4 pv[3][2][9];
#pragma unroll
  for (int o = 0; o < 3; ++o) {
    const int src = o < Q ? o : o + 1;
    const u32 base = (u32)WS_P + (u32)(((tile * 4 + src) * 4 + wave) * 18) * 1024 + lane * 16;
#pragma unroll
    for (int i = 0; i < 2; ++i)
#pragma unroll
      for (int j = 0; j < 9; ++j)
        if (g2_owner(i, j) == Q) pv[o][i][j] = ld16(rw, base + (i * 9 + j) * 1024);
  }
#pragma unroll
  for (int i = 0; i < 2; ++i)
#pragma unroll
    for (int j = 0; j < 9; ++j)
      if (g2_owner(i, j) == Q) {
        f32x4 p[4];
#pragma unroll
        for (int q = 0; q < 4; ++q) p[q] = q == Q ? acc[i][j] : __builtin_bit_cast(f32x4, pv[q < Q ? q : q - 1][i][j]);
        const f32x4 t = (p[0] + p[1]) + (p[2] + p[3]);
        const u32x2 xv = xr[i][j];
        const u32x2 o2 = {pack_bf16(t.x + bs[i] + bflo(xv.x), t.y + bs[i] + bfhi(xv.x)), pack_bf16(t.z + bs[i] + bflo(xv.y), t.w + bs[i] + bfhi(xv.y))};
        *(u32x2*)(out + (size_t)(srow + 16 * i) * D + dcol + 16 * j) = o2;
      }
}

struct Geo {
  int f0, d1, ft, dt;
  int kq, s0, d2, tile, dt2, nD, eb;
};

__global__ __launch_bounds__(512, 2) void k2_fused(const u16* __restrict__ x,
                                                   const u16* __restrict__ w1, const u16* __restrict__ b1, const u16* __restrict__ w2,
                                                   const u16* __restrict__ b2, char* ws, u16* __restrict__ out) {
  extern __shared__ __attribute__((aligned(16))) char smem[];
  const int bid = blockIdx.x;
  const int tid = threadIdx.x, lane = tid & 63;
  const int wave = __builtin_amdgcn_readfirstlane(tid >> 6);
  const u32 ldsb = (u32)(size_t)smem;
  PROF_STAMP(1, 0);
  const rsrc_t rw = __builtin_amdgcn_make_buffer_rsrc(ws, 0, (int)WS_END, 0x00020000);
  const bool isG1 = bid < G1_WGS, isFFT = !isG1 && bid < G1_WGS + FFT_WGS;
  Geo G;
  {
    const int xg = bid & 7, idx = bid >> 3;
    const int lin = xg * 27 + idx;
    G.ft = lin / 6; G.dt = lin - G.ft * 6; G.f0 = G.ft * 128; G.d1 = G.dt * 96;
    G.kq = xg >> 1;
    const int st = (xg & 1) * 8 + (idx >> 2);
    G.dt2 = idx & 3; G.s0 = st * 128; G.d2 = G.dt2 * 144; G.tile = st * 4 + G.dt2;
    G.nD = G.kq == 3 ? 3 : 2; G.eb = 2 * G.kq;
  }
  const int L = wave & 1;
  const int kk = lane >> 4;
  const u32 cs0 = ((lane & 15) ^ (kk << 1)) * 16, cs1 = ((lane & 15) ^ ((kk << 1) | 8)) * 16;
  const u32 mW = ldsb + L_W + L * 8192;
  const u32x4 srdW1 = make_srd(w1, (u32)((size_t)S * F * 2));
  const u32 vW0 = (u32)((64 * kk + 4 * L) * (F * 2) + G.f0 * 2) + cs0, vW1 = (u32)((64 * kk + 4 * L) * (F * 2) + G.f0 * 2) + cs1;
#define G1_W(t, sl) do { const u32 _s = (u32)(8 * ((t) & 7) + 512 * ((t) >> 3)) * (F * 2); \
    _Pragma("unroll") for (int j = 0; j < 8; ++j) dma16(mW + (sl) * 16384 + j * 1024, (j & 2) ? vW1 : vW0, srdW1, _s + ((j >> 1) + 256 * (j & 1)) * (F * 2)); } while (0)

  if (isFFT) {
    fft_role(smem, ws, rw, bid - G1_WGS, wave, lane);
    PROF_STAMP(1, 2);
    WAITV(0); BAR();
    if (wave == 4 && lane == 0) set_flag(ws, WS_FY + (bid - G1_WGS) * 4);
  }

  if (wave >= 4) {
    const int wd = (wave >> 1) & 1, wf = wave & 1;
    const int fbase = G.f0 + wf * 64 + 32 + 4 * kk, dbase = G.d1 + wd * 48 + (lane & 15);
    if (wave >= 6) {
      if (isG1) {
        const u32x2 bb0 = *(const u32x2*)(b1 + fbase), bb1 = *(const u32x2*)(b1 + fbase + 16);
        const int me = ((bid >> 3) * 2 + L) * 64;
        const u32x4 srdW2 = make_srd(w2, (u32)((size_t)F * S * 2));
        int n1 = PF1 * 640 + me, n2 = me, nxt2 = PFW2 + ((bid >> 3) % PFW2S);
#pragma unroll 1
        for (int i = 0; i < 35; ++i) {
          BAR();
          if (n1 < 32 * 640 && n1 < (i + PFLA) * 640) { touch(ldsb + L_PF, w1_line(bid & 7, n1 + lane), srdW1, 0); n1 += 3456; }
          if (i == nxt2 && n2 < 18432) {
            const int n = n2 + lane;
            touch(ldsb + L_PF, (u32)((1152 * G.kq + (n >> 4)) * (S * 2) + (bid & 1) * 2048 + (n & 15) * 128), srdW2, 0);
            n2 += 3456; nxt2 += PFW2S;
          }
        }
        const f32x4* ex = (const f32x4*)(smem + L_EX);
#pragma unroll
        for (int ii = 0; ii < 2; ++ii)
#pragma unroll
          for (int j = 0; j < 3; ++j) g1_store(ex[((wave - 4) * 6 + ii * 3 + j) * 64 + lane], ii ? bb1 : bb0, rw, fbase + 16 * ii, dbase + 16 * j);
        asm volatile("s_waitcnt vmcnt(0) lgkmcnt(0)\n\ts_barrier" ::: "memory");
      }
      return;
    }
    const u32x4 srdWS = make_srd(ws, (u32)WS_END);
    if (isG1) {
      u32 vH[6];
#pragma unroll
      for (int j = 0; j < 6; ++j) { const int p = j * 64 + lane; const int kc = p / 96, dd = p - kc * 96; vH[j] = (u32)WS_HB + (u32)(((4 * L + kc) * D + G.d1 + dd) * 16); }
      const u32 mH = ldsb + L_X + L * 6144;
#define G1_H(t, sl) do { _Pragma("unroll") for (int j = 0; j < 6; ++j) dma16(mH + (sl) * 12288 + j * 1024, vH[j], srdWS, (u32)(t) * (8 * D * 16)); } while (0)
#pragma unroll
      for (int t = 0; t < 4; ++t) { G1_W(t, t); G1_H(t, t); }
      WAITV(42); BAR();
      WAITV(28); BAR(); G1_W(4, 4); G1_H(4, 4);
      WAITV(28); BAR(); G1_W(5, 0); G1_H(5, 0);
      int sl = 1;
#pragma unroll 1
      for (int t = 2; t < 28; ++t) {
        WAITV(28); BAR();
        G1_W(t + 4, sl); G1_H(t + 4, sl);
        sl = sl == 4 ? 0 : sl + 1;
      }
      WAITV(28); BAR();
      WAITV(14); BAR();
      WAITV(0); BAR();
    }
    const u32x4 srdW2 = make_srd(w2, (u32)((size_t)F * S * 2));
    int ln2 = lane; asm volatile("" : "+v"(ln2));
    const int kk2 = ln2 >> 4;
    const u32 vA0 = (u32)((32 * L + kk2) * (S * 2) + G.s0 * 2) + ((ln2 & 15) ^ (kk2 << 1)) * 16, vA1 = (u32)((32 * L + kk2) * (S * 2) + G.s0 * 2) + ((ln2 & 15) ^ ((kk2 << 1) | 8)) * 16;
    u32 vG[9];
#pragma unroll
    for (int j = 0; j < 9; ++j) { const int p = j * 64 + ln2; const int kc = p / 144, dd = p - kc * 144; vG[j] = (u32)WS_GB + (u32)(((4 * L + kc) * D + G.d2 + dd) * 16); }
    const u32 mG = ldsb + L_X + L * 9216;
    const int nD = G.nD, nk = nD + 18;
#define G2_WY(e, sl) do { _Pragma("unroll") for (int j = 0; j < 8; ++j) dma16s(mW + (sl) * 16384 + j * 1024, (j & 2) ? vA1 : vA0, srdWS, (u32)WS_YT + (u32)(e) * (64 * S * 2) + j * (4 * S * 2)); } while (0)
#define G2_WW(t, sl) do { _Pragma("unroll") for (int j = 0; j < 8; ++j) dma16(mW + (sl) * 16384 + j * 1024, (j & 2) ? vA1 : vA0, srdW2, (u32)(t) * (64 * S * 2) + j * (4 * S * 2)); } while (0)
    const u32x4 srdI = make_srd((const char*)IDFT - WS_GB, (u32)(WS_GB + sizeof(IDFT)));
#define G2_GI(e, sl) do { _Pragma("unroll") for (int j = 0; j < 9; ++j) dma16(mG + (sl) * 18432 + j * 1024, vG[j], srdI, (u32)(e) * (8 * D * 16)); } while (0)
#define G2_G(t, sl) do { _Pragma("unroll") for (int j = 0; j < 9; ++j) dma16s(mG + (sl) * 18432 + j * 1024, vG[j], srdWS, (u32)(t) * (8 * D * 16)); } while (0)
    wait_lanes(ws, lane < 4 * nD ? (u32)WS_FY + (4 * G.eb + lane) * 4 : (lane == 63 ? (u32)WS_FY + 36 * 4 : ~0u));
    if (isG1) { BAR(); BAR(); }
    G2_WY(G.eb, 0); G2_GI(G.eb, 0);
    G2_WY(G.eb + 1, 1); G2_GI(G.eb + 1, 1);
    if (nD == 3) { G2_WY(G.eb + 2, 2); G2_GI(G.eb + 2, 2); } else G2_WW(18 * G.kq, 2);
    if (isG1) {
      const u32x2 bb0 = *(const u32x2*)(b1 + fbase), bb1 = *(const u32x2*)(b1 + fbase + 16);
      BAR();
      const f32x4* ex = (const f32x4*)(smem + L_EX);
#pragma unroll
      for (int ii = 0; ii < 2; ++ii)
#pragma unroll
        for (int j = 0; j < 3; ++j) g1_store(ex[(L * 6 + ii * 3 + j) * 64 + lane], ii ? bb1 : bb0, rw, fbase + 16 * ii, dbase + 16 * j);
      asm volatile("s_waitcnt vmcnt(0) lgkmcnt(0)\n\ts_barrier" ::: "memory");
      if (wave == 4 && lane == 0) set_flag(ws, WS_FG + (G.ft * 6 + G.dt) * 4);
    } else {
      WAITV(0); BAR();
    }
    PROF_STAMP(2, 0);
    wait_lanes(ws, lane < 18 ? (u32)WS_FG + ((9 * G.kq + (lane >> 1)) * 6 + ((G.dt2 * 3) >> 1) + (lane & 1)) * 4 : ~0u);
    PROF_STAMP(2, 1);
    const int t0 = 18 * G.kq - nD;
    if (nD == 2) {
      G2_G(t0 + 2, 2);
      G2_WW(t0 + 3, 3); G2_G(t0 + 3, 3);
      WAITV(17); BAR();
      G2_WW(t0 + 4, 0); G2_G(t0 + 4, 0);
    } else {
      G2_WW(t0 + 3, 3); G2_G(t0 + 3, 3);
      WAITV(0); BAR();
      G2_WW(t0 + 4, 0); G2_G(t0 + 4, 0);
      G2_WW(t0 + 5, 1); G2_G(t0 + 5, 1);
    }
#pragma unroll 1
    for (int sg = nD; sg < nk - 3; ++sg) {
      WAITV(17); BAR();
      const int sl = (sg + 3) & 3;
      G2_WW(t0 + sg + 3, sl); G2_G(t0 + sg + 3, sl);
    }
    WAITV(17); BAR();
    WAITV(0); BAR();
    return;
  }

  const int lg = lane >> 4, lq = (lane & 15) >> 2, lp = lane & 3;
  const u32 trbase = ldsb + L_W + (u32)((8 * lg + lq) * 256 + (lp & 1) * 8);
  const int swz = (lq << 1) | ((lg & 1) << 3);
  if (isG1) {
    const int wd = (wave >> 1) & 1, wf = wave & 1;
    u32 aA[4];
#pragma unroll
    for (int i = 0; i < 4; ++i) aA[i] = trbase + (u32)(((wf * 8 + 2 * i + (lp >> 1)) ^ swz) * 16);
    const u32 aB = ldsb + L_X + (u32)((lg * 96 + wd * 48 + (lane & 15)) * 16);
    const int fbase = G.f0 + wf * 64 + 4 * lg, dbase = G.d1 + wd * 48 + (lane & 15);
    const u32x2 bb0 = *(const u32x2*)(b1 + fbase), bb1 = *(const u32x2*)(b1 + fbase + 16);
    f32x4 acc[4][3];
#pragma unroll
    for (int i = 0; i < 4; ++i)
#pragma unroll
      for (int j = 0; j < 3; ++j) acc[i][j] = (f32x4){0.f, 0.f, 0.f, 0.f};
    u32x4 fb[2][3];
    u32x2 al[2][4], ah[2][4];
#define G1_RDB(dst, J, a, H) dst = rd128<6144 * (H) + 256 * (J)>(a)
#define P1_M(I, J, C) do { acc[I][J] = mfma16((u32x4){al[C][I].x, al[C][I].y, ah[C][I].x, ah[C][I].y}, fb[C][J], acc[I][J]); SB0; } while (0)
#define P1_HALF(C, N, sl_, H) do { \
    const u32 ab = aB + (sl_) * 12288, a0 = aA[0] + (sl_) * 16384, a1 = aA[1] + (sl_) * 16384, a2 = aA[2] + (sl_) * 16384, a3 = aA[3] + (sl_) * 16384; \
    WL(8); P1_M(0, 0, C); G1_RDB(fb[N][0], 0, ab, H); SB0; \
    WL(8); P1_M(0, 1, C); al[N][0] = trd<8192 * (H)>(a0); SB0; \
    WL(8); P1_M(0, 2, C); ah[N][0] = trd<8192 * (H) + 1024>(a0); SB0; \
    WL(7); P1_M(1, 0, C); G1_RDB(fb[N][1], 1, ab, H); SB0; \
    P1_M(1, 1, C); G1_RDB(fb[N][2], 2, ab, H); SB0; \
    P1_M(1, 2, C); al[N][1] = trd<8192 * (H)>(a1); SB0; \
    WL(8); P1_M(2, 0, C); ah[N][1] = trd<8192 * (H) + 1024>(a1); SB0; \
    P1_M(2, 1, C); al[N][2] = trd<8192 * (H)>(a2); SB0; \
    P1_M(2, 2, C); ah[N][2] = trd<8192 * (H) + 1024>(a2); SB0; \
    WL(9); P1_M(3, 0, C); al[N][3] = trd<8192 * (H)>(a3); SB0; \
    P1_M(3, 1, C); ah[N][3] = trd<8192 * (H) + 1024>(a3); SB0; \
    P1_M(3, 2, C); } while (0)
    asm volatile("s_waitcnt lgkmcnt(0)\n\ts_barrier" ::: "memory");
    PROF_STAMP(1, 1);
    G1_RDB(fb[0][0], 0, aB, 0); al[0][0] = trd<0>(aA[0]); ah[0][0] = trd<1024>(aA[0]); G1_RDB(fb[0][1], 1, aB, 0); G1_RDB(fb[0][2], 2, aB, 0);
#pragma unroll
    for (int i = 1; i < 4; ++i) { al[0][i] = trd<0>(aA[i]); ah[0][i] = trd<1024>(aA[i]); }
    u32 sl = 0;
#pragma unroll 1
    for (int t = 0; t < 32; ++t) {
      BAR();
      SB0;
      const u32 nsl = sl == 4 ? 0 : sl + 1;
      P1_HALF(0, 1, sl, 1);
      P1_HALF(1, 0, nsl, 0);
      sl = nsl;
    }
    asm volatile("s_waitcnt lgkmcnt(0)\n\ts_barrier" ::: "memory");
    SB0;
    PROF_STAMP(1, 2);
    f32x4* ex = (f32x4*)(smem + L_EX);
#pragma unroll
    for (int ii = 0; ii < 2; ++ii)
#pragma unroll
      for (int j = 0; j < 3; ++j) ex[(wave * 6 + ii * 3 + j) * 64 + lane] = acc[2 + ii][j];
    asm volatile("s_waitcnt lgkmcnt(0)\n\ts_barrier" ::: "memory");
#pragma unroll
    for (int ii = 0; ii < 2; ++ii)
#pragma unroll
      for (int j = 0; j < 3; ++j) g1_store(acc[ii][j], ii ? bb1 : bb0, rw, fbase + 16 * ii, dbase + 16 * j);
    PROF_STAMP(1, 3);
    asm volatile("s_waitcnt vmcnt(0)\n\ts_barrier" ::: "memory");
  } else {
    BAR();
  }
  PROF_STAMP(2, 0);
  {
    const u32 aW0 = trbase + (u32)(((wave * 4 + (lp >> 1)) ^ swz) * 16), aW1 = trbase + (u32)(((wave * 4 + 2 + (lp >> 1)) ^ swz) * 16);
    const u32 aG = ldsb + L_X + (u32)((lg * 144 + (lane & 15)) * 16);
    f32x4 acc[2][9];
#pragma unroll
    for (int i = 0; i < 2; ++i)
#pragma unroll
      for (int j = 0; j < 9; ++j) acc[i][j] = (f32x4){0.f, 0.f, 0.f, 0.f};
    u32x4 fg[2][9];
    u32x2 wl[2][2], wh[2][2];
#define G2_RDG(dst, J, base, H) dst = rd128<9216 * (H) + 256 * (J)>(base)
#define P2_M(I, J, C) do { acc[I][J] = mfma16(fg[C][J], (u32x4){wl[C][I].x, wl[C][I].y, wh[C][I].x, wh[C][I].y}, acc[I][J]); SB0; } while (0)
#define P2_HALF(C, N, sl_, H) do { \
    const u32 w0 = aW0 + (sl_) * 16384, w1_ = aW1 + (sl_) * 16384, g = aG + (sl_) * 18432; \
    WL(10); P2_M(0, 0, C); wl[N][0] = trd<8192 * (H)>(w0); SB0; \
    WL(9);  P2_M(1, 0, C); wh[N][0] = trd<8192 * (H) + 1024>(w0); SB0; \
    WL(9);  P2_M(0, 1, C); G2_RDG(fg[N][0], 0, g, H); SB0; \
            P2_M(1, 1, C); wl[N][1] = trd<8192 * (H)>(w1_); SB0; \
    WL(10); P2_M(0, 2, C); wh[N][1] = trd<8192 * (H) + 1024>(w1_); SB0; \
            P2_M(1, 2, C); G2_RDG(fg[N][1], 1, g, H); SB0; \
    WL(11); P2_M(0, 3, C); G2_RDG(fg[N][2], 2, g, H); SB0; \
            P2_M(1, 3, C); G2_RDG(fg[N][3], 3, g, H); SB0; \
    WL(12); P2_M(0, 4, C); G2_RDG(fg[N][4], 4, g, H); SB0; \
            P2_M(1, 4, C); G2_RDG(fg[N][5], 5, g, H); SB0; \
    WL(13); P2_M(0, 5, C); G2_RDG(fg[N][6], 6, g, H); SB0; \
            P2_M(1, 5, C); G2_RDG(fg[N][7], 7, g, H); SB0; \
    WL(14); P2_M(0, 6, C); G2_RDG(fg[N][8], 8, g, H); SB0; \
            P2_M(1, 6, C); \
    WL(14); P2_M(0, 7, C); P2_M(1, 7, C); \
    WL(13); P2_M(0, 8, C); P2_M(1, 8, C); } while (0)
    wl[0][0] = trd<0>(aW0); wh[0][0] = trd<1024>(aW0); G2_RDG(fg[0][0], 0, aG, 0); wl[0][1] = trd<0>(aW1); wh[0][1] = trd<1024>(aW1);
    G2_RDG(fg[0][1], 1, aG, 0); G2_RDG(fg[0][2], 2, aG, 0); G2_RDG(fg[0][3], 3, aG, 0); G2_RDG(fg[0][4], 4, aG, 0);
    G2_RDG(fg[0][5], 5, aG, 0); G2_RDG(fg[0][6], 6, aG, 0); G2_RDG(fg[0][7], 7, aG, 0); G2_RDG(fg[0][8], 8, aG, 0);
    const int nk = G.nD + 18;
#pragma unroll 1
    for (int sg = 0; sg < nk; ++sg) {
      if (sg >= G.nD - 1 && sg < nk - 1) BAR();
      SB0;
      const u32 sl = sg & 3, nsl = (sg + 1) & 3;
      P2_HALF(0, 1, sl, 1);
      P2_HALF(1, 0, nsl, 0);
    }
    asm volatile("s_waitcnt lgkmcnt(0)" ::: "memory");
    SB0;
    PROF_STAMP(2, 2);
    switch (G.kq) {
      case 0: g2_finish<0>(acc, x, b2, ws, rw, out, G.tile, G.s0, G.d2, wave, lane); break;
      case 1: g2_finish<1>(acc, x, b2, ws, rw, out, G.tile, G.s0, G.d2, wave, lane); break;
      case 2: g2_finish<2>(acc, x, b2, ws, rw, out, G.tile, G.s0, G.d2, wave, lane); break;
      default: g2_finish<3>(acc, x, b2, ws, rw, out, G.tile, G.s0, G.d2, wave, lane); break;
    }
    PROF_STAMP(2, 5);
  }
}

extern "C" void kernel_launch(void* const* d_in, const int* in_sizes, int n_in, void* d_out, int out_size, void* d_ws, size_t ws_size, hipStream_t stream) {
  (void)hipFuncSetAttribute((const void*)k2_fused, hipFuncAttributeMaxDynamicSharedMemorySize, MK_LDS);
  k1_rows<<<256, 512, 0, stream>>>((const u16*)d_in[0], (const u16*)d_in[1], (const u16*)d_in[2], (const u16*)d_in[4], (char*)d_ws);
  k2_fused<<<256, 512, MK_LDS, stream>>>((const u16*)d_in[0], (const u16*)d_in[4], (const u16*)d_in[5], (const u16*)d_in[6], (const u16*)d_in[7], (char*)d_ws, (u16*)d_out);
}
```

```cpp
#include <hip/hip_runtime.h>
#include <hip/hip_bf16.h>
#include <stdint.h>

typedef unsigned short u16;
typedef unsigned int u32;
using bf16x8 = __attribute__((ext_vector_type(8))) short;
using f32x4 = __attribute__((ext_vector_type(4))) float;
using f32x16 = __attribute__((ext_vector_type(16))) float;
using f32x2 = __attribute__((ext_vector_type(2))) float;
using u32x4 = __attribute__((ext_vector_type(4))) unsigned;
using u32x2 = __attribute__((ext_vector_type(2))) unsigned;
#define LAS __attribute__((address_space(3)))
#define GAS __attribute__((address_space(1)))
#define SB0 __builtin_amdgcn_sched_barrier(0)

constexpr int S = 2048, D = 576, DF = 289, F = 4608;
constexpr int KX = F + D;

constexpr size_t WS_HB = 0;
constexpr size_t WS_Z = WS_HB + (size_t)S * D * 2;
constexpr size_t WS_WT = WS_Z + (size_t)74 * 32768;
constexpr size_t WS_GB = WS_WT + (size_t)73 * 32768;
constexpr size_t WS_YT = WS_GB + (size_t)F * D * 2;
constexpr size_t WS_P = WS_YT + (size_t)D * S * 2;
constexpr size_t WS_P_BYTES = (size_t)256 * 4 * 18 * 1024;
constexpr size_t WS_FG = WS_P + WS_P_BYTES;
constexpr size_t WS_FY = WS_FG + 1024;
constexpr size_t WS_FP = WS_FY + 256;
constexpr size_t WS_END = WS_FP + 1024;

constexpr double kPi = 3.141592653589793238462643383279502884;
constexpr double c_sin_small(double x) { double t = x, s = x; for (int i = 1; i < 14; ++i) { t *= -x * x / ((2 * i) * (2 * i + 1)); s += t; } return s; }
constexpr double c_cos_small(double x) { double t = 1, s = 1; for (int i = 1; i < 14; ++i) { t *= -x * x / ((2 * i - 1) * (2 * i)); s += t; } return s; }
constexpr double c_cos(int m, int n) {
  m %= n; if (2 * m > n) m = n - m;
  if (4 * m > n) return -((8 * (n / 2 - m) > n) ? c_sin_small(2 * kPi * (m - n / 4) / n) : c_cos_small(2 * kPi * (n / 2 - m) / n));
  return (8 * m > n) ? c_sin_small(2 * kPi * (n / 4 - m) / n) : c_cos_small(2 * kPi * m / n);
}
constexpr double c_sin(int m, int n) { return c_cos((m % n) + n - n / 4, n); }
template <int N> struct TwTab { float v[2 * N]; constexpr TwTab() : v() { for (int m = 0; m < N; ++m) { v[2 * m] = (float)c_cos(m, N); v[2 * m + 1] = (float)(-c_sin(m, N)); } } };
__device__ const TwTab<576> TW576 = TwTab<576>();
#if defined(__HIP_DEVICE_COMPILE__)
constexpr TwTab<2048> TWB = TwTab<2048>();
constexpr unsigned short c_bf16(float f) { unsigned u = __builtin_bit_cast(unsigned, f); return (unsigned short)((u + 0x7fffu + ((u >> 16) & 1u)) >> 16); }
struct FftTab {
  unsigned short t1[2][4][64][8];
  unsigned short t2[2][2][4][64][8];
  unsigned short t4[3][2][64][8];
  unsigned short pad[1024];
  float tw[2][8][64][4];
  constexpr FftTab() : t1(), t2(), t4(), pad(), tw() {
    for (int l = 0; l < 64; ++l) {
      const int r = l & 31, h = l >> 5;
      for (int j = 0; j < 8; ++j) {
        for (int m = 0; m < 4; ++m) {
          const int a = 16 * h + 4 * m + (j >> 1), e = ((a * r) & 31) * 64;
          const float cr = TWB.v[2 * e], ci = TWB.v[2 * e + 1];
          t1[0][m][l][j] = c_bf16((j & 1) ? -ci : cr);
          t1[1][m][l][j] = c_bf16((j & 1) ? cr : ci);
          for (int ub = 0; ub < 2; ++ub) {
            const int b = 16 * m + 8 * (j >> 2) + 4 * h + (j & 3), e2 = (((32 * ub + r) * b) & 63) * 32;
            t2[0][ub][m][l][j] = c_bf16(TWB.v[2 * e2]);
            t2[1][ub][m][l][j] = c_bf16(TWB.v[2 * e2 + 1]);
          }
        }
        for (int ks = 0; ks < 2; ++ks) {
          const int u1 = 16 * ks + 8 * h + j, e = ((u1 * r) & 31) * 64;
          const float sc = 1.0f / (2048.0f * 576.0f);
          t4[0][ks][l][j] = c_bf16(TWB.v[2 * e] * sc);
          t4[1][ks][l][j] = c_bf16(-TWB.v[2 * e + 1] * sc);
          t4[2][ks][l][j] = c_bf16(TWB.v[2 * e + 1] * sc);
        }
      }
      for (int rb = 0; rb < 2; ++rb)
        for (int rho = 0; rho < 16; ++rho) {
          const int b = 32 * rb + (rho & 3) + 8 * (rho >> 2) + 4 * h, e = (b * r) & 2047;
          tw[rb][rho >> 1][l][2 * (rho & 1)] = TWB.v[2 * e];
          tw[rb][rho >> 1][l][2 * (rho & 1) + 1] = TWB.v[2 * e + 1];
        }
    }
  }
};
constexpr TwTab<576> TWC = TwTab<576>();
struct IdftPart {
  unsigned short v[8][576][8];
  constexpr IdftPart(int e) : v() {
    for (int kc = 0; kc < 8; ++kc)
      for (int j = 0; j < 4; ++j) {
        const int k = 32 * e + 4 * kc + j;
        int idx = 0;
        for (int d = 0; d < 576; ++d) {
          v[kc][d][2 * j] = c_bf16(k ? 2.f * TWC.v[2 * idx] : 1.f);
          v[kc][d][2 * j + 1] = c_bf16(k ? 2.f * TWC.v[2 * idx + 1] : ((d & 1) ? -1.f : 1.f));
          idx += k; if (idx >= 576) idx -= 576;
        }
      }
  }
};
constexpr IdftPart IP0(0), IP1(1), IP2(2), IP3(3), IP4(4), IP5(5), IP6(6), IP7(7), IP8(8);
__device__ const __attribute__((aligned(16))) IdftPart IDFT[9] = {IP0, IP1, IP2, IP3, IP4, IP5, IP6, IP7, IP8};
static_assert(sizeof(FftTab) == 49152, "table is moved in whole 1 KB pieces");
__device__ const __attribute__((aligned(16))) FftTab FTAB = FftTab();
#else
__device__ const __attribute__((aligned(16))) char IDFT[663552] = {};
__device__ const __attribute__((aligned(16))) char FTAB[49152] = {};
#endif

struct cf { float x, y; };
__device__ __forceinline__ cf cadd(cf a, cf b) { return {a.x + b.x, a.y + b.y}; }
__device__ __forceinline__ cf csub(cf a, cf b) { return {a.x - b.x, a.y - b.y}; }
__device__ __forceinline__ cf cmul(cf a, cf w) { return {__builtin_fmaf(a.x, w.x, -a.y * w.y), __builtin_fmaf(a.x, w.y, a.y * w.x)}; }
__device__ __forceinline__ cf cmulc(cf a, cf w) { return {__builtin_fmaf(a.x, w.x, a.y * w.y), __builtin_fmaf(a.y, w.x, -a.x * w.y)}; }
__device__ __forceinline__ cf ldtw(const float* t, int m) { f32x2 v = *(const f32x2*)(t + 2 * m); return {v.x, v.y}; }

__device__ __forceinline__ float bflo(u32 v) { return __uint_as_float(v << 16); }
__device__ __forceinline__ float bfhi(u32 v) { return __uint_as_float(v & 0xffff0000u); }
__device__ __forceinline__ u32 pack_bf16(float a, float b) {
  typedef __attribute__((ext_vector_type(2))) __bf16 bf2;
  f32x2 v = {a, b};
  return __builtin_bit_cast(u32, __builtin_convertvector(v, bf2));
}
__device__ __forceinline__ float rbf(float a) { return bflo(pack_bf16(a, a)); }

template <int CTRL> __device__ __forceinline__ float dppf(float v) {
  return __int_as_float(__builtin_amdgcn_update_dpp(0, __float_as_int(v), CTRL, 0xf, 0xf, true));
}
constexpr int DPP_X1 = 0xB1, DPP_X2 = 0x4E, DPP_X7 = 0x141, DPP_X8 = 0x128;
__device__ __forceinline__ float xor16f(float v) { return __int_as_float(__builtin_amdgcn_ds_swizzle(__float_as_int(v), 0x401F)); }
__device__ __forceinline__ float half_sum(float v) {
  v += dppf<DPP_X1>(v); v += dppf<DPP_X2>(v); v += dppf<DPP_X7>(v); v += dppf<DPP_X8>(v); v += xor16f(v); return v;
}

__device__ __forceinline__ u32x4 make_srd(const void* p, u32 bytes) {
  uint64_t a = (uint64_t)p; u32x4 r;
  r.x = __builtin_amdgcn_readfirstlane((u32)a);
  r.y = __builtin_amdgcn_readfirstlane((u32)(a >> 32)) & 0xffffu;
  r.z = bytes; r.w = 0x00020000u; return r;
}
__device__ __forceinline__ void dma16(u32 ldsaddr, u32 voff, u32x4 srd, u32 soff) {
  asm volatile("s_mov_b32 m0, %0\n\ts_nop 0\n\tbuffer_load_dwordx4 %1, %2, %3 offen lds" ::"s"(ldsaddr), "v"(voff), "s"(srd), "s"(soff) : "memory");
}
__device__ __forceinline__ void dma16n(u32 ldsaddr, u32 voff, u32x4 srd, u32 soff) {
  asm volatile("s_mov_b32 m0, %0\n\ts_nop 0\n\tbuffer_load_dwordx4 %1, %2, %3 offen nt lds" ::"s"(ldsaddr), "v"(voff), "s"(srd), "s"(soff) : "memory");
}
template <int OFF> __device__ __forceinline__ u32x2 trd(u32 addr) {
  u32x2 r; asm volatile("ds_read_b64_tr_b16 %0, %1 offset:%2" : "=v"(r) : "v"(addr), "n"(OFF) : "memory"); return r;
}
template <int OFF> __device__ __forceinline__ u32x4 rd128(u32 addr) {
  u32x4 r; asm volatile("ds_read_b128 %0, %1 offset:%2" : "=v"(r) : "v"(addr), "n"(OFF) : "memory"); return r;
}
__device__ __forceinline__ f32x4 mfma16(u32x4 a, u32x4 b, f32x4 c) {
  return __builtin_amdgcn_mfma_f32_16x16x32_bf16(__builtin_bit_cast(bf16x8, a), __builtin_bit_cast(bf16x8, b), c, 0, 0, 0);
}

#define PROF_STAMP_W(kern, w, slot) do { } while (0)
#define PROF_CLK(kern, slot) do { } while (0)
#define PROF_STAMP(kern, slot) do { } while (0)

__device__ __forceinline__ f32x16 mfma32(u32x4 a, u32x4 b, f32x16 c) {
  return __builtin_amdgcn_mfma_f32_32x32x16_bf16(__builtin_bit_cast(bf16x8, a), __builtin_bit_cast(bf16x8, b), c, 0, 0, 0);
}
typedef unsigned long long u64;
typedef GAS u64 gu64;
constexpr int G1_WGS = 216, FFT_WGS = 37;
constexpr int L_W = 0;
constexpr int L_X = 81920;
constexpr int L_ZC = 0, L_HROW = 18432, L_WTS = 28160, L_SYNC = 44544, L_PF1 = 44608, K1_LDS = 44864;
constexpr int L_EX = 3 * 16384;
constexpr int FFT_TAB = 0, FFT_WAVE = 49152, FFT_WAVE_B = 9216, FFT_ZS = 49152;
constexpr int L_PF = 155648;
constexpr int MK_LDS = 156160;
#define PFCODE 1
constexpr u32 K1_CODE = 10240, K2_CODE = 31744;
#define PFMODE 2
#define PFW2N 9
#define PF1 8
#define PFS 2
constexpr unsigned SPIN_CAP = 1u << 16;

#define BAR() asm volatile("s_barrier" ::: "memory")
#define WAITV(n) asm volatile("s_waitcnt vmcnt(%0)" ::"n"(n) : "memory")
#define WL(n) do { asm volatile("s_waitcnt lgkmcnt(" #n ")" ::: "memory"); SB0; } while (0)

__device__ __forceinline__ void dma16s(u32 ldsaddr, u32 voff, u32x4 srd, u32 soff) {
  asm volatile("s_mov_b32 m0, %0\n\ts_nop 0\n\tbuffer_load_dwordx4 %1, %2, %3 offen sc1 lds" ::"s"(ldsaddr), "v"(voff), "s"(srd), "s"(soff) : "memory");
}
__device__ __forceinline__ void touch(u32 ldsaddr, u32 voff, u32x4 srd, u32 soff) {
  asm volatile("s_mov_b32 m0, %0\n\ts_nop 0\n\tbuffer_load_dword %1, %2, %3 offen lds" ::"s"(ldsaddr), "v"(voff), "s"(srd), "s"(soff) : "memory");
}
typedef __amdgpu_buffer_rsrc_t rsrc_t;
__device__ __forceinline__ void st16(rsrc_t r, u32 off, u32x4 v) { __builtin_amdgcn_raw_buffer_store_b128(v, r, off, 0, 16); }
__device__ __forceinline__ void st8(rsrc_t r, u32 off, u32x2 v) { __builtin_amdgcn_raw_buffer_store_b64(v, r, off, 0, 16); }
__device__ __forceinline__ u32x4 ld16(rsrc_t r, u32 off) { return __builtin_amdgcn_raw_buffer_load_b128(r, off, 0, 16); }
__device__ __forceinline__ u32 ld4(rsrc_t r, u32 off) { return __builtin_amdgcn_raw_buffer_load_b32(r, off, 0, 16); }
typedef GAS u32 gu32;
__device__ __forceinline__ void set_flag(char* ws, size_t off) { __hip_atomic_store((gu32*)(ws + off), 1u, __ATOMIC_RELAXED, __HIP_MEMORY_SCOPE_AGENT); }
#define K1AUX 0
__device__ __forceinline__ void k1st(rsrc_t r, u32 off, u32x4 v) { __builtin_amdgcn_raw_buffer_store_b128(v, r, off, 0, K1AUX); }
__device__ __forceinline__ unsigned lds_arrive(char* smem, int ctr, int lane) {
  unsigned old = 0;
  asm volatile("s_waitcnt lgkmcnt(0)" ::: "memory");
  if (lane == 0) old = __hip_atomic_fetch_add((LAS unsigned*)(smem + L_SYNC + 4 * ctr), 1u, __ATOMIC_RELAXED, __HIP_MEMORY_SCOPE_WORKGROUP);
  return __builtin_amdgcn_readfirstlane(old);
}
__device__ __forceinline__ void lds_wait(char* smem, int ctr, unsigned n) {
  for (unsigned sp = 0; sp < (1u << 22); ++sp) {
    if (__builtin_amdgcn_readfirstlane(__hip_atomic_load((LAS unsigned*)(smem + L_SYNC + 4 * ctr), __ATOMIC_RELAXED, __HIP_MEMORY_SCOPE_WORKGROUP)) >= n) break;
    __builtin_amdgcn_s_sleep(1);
  }
  asm volatile("" ::: "memory");
}
__device__ __forceinline__ void wait_lanes(char* ws, u32 off) {
  bool ok = off == ~0u;
  for (unsigned sp = 0; sp < SPIN_CAP; ++sp) {
    if (!ok) ok = __hip_atomic_load((gu32*)(ws + off), __ATOMIC_RELAXED, __HIP_MEMORY_SCOPE_AGENT) != 0u;
    if (__all(ok)) break;
    __builtin_amdgcn_s_sleep(1);
  }
}

__device__ __forceinline__ u32 w1_line(const int xg, const int n) {
  const int t = n / 640, r = n - t * 640, k = r / 10, c = r - k * 10;
  const int col = ((xg * 27) / 6) * 256 + c * 128;
  return (t < 32 && col < F * 2) ? (u32)((8 * (t & 7) + 512 * (t >> 3) + (k >> 3) + 64 * (k & 7)) * (F * 2) + col) : 0xfffffff0u;
}
__device__ __forceinline__ void dft3(cf& a, cf& b, cf& c) {
  const float h = 0.8660254037844386f;
  cf s = cadd(b, c), d = csub(b, c);
  cf m = {__builtin_fmaf(-0.5f, s.x, a.x), __builtin_fmaf(-0.5f, s.y, a.y)};
  a = cadd(a, s);
  b = {__builtin_fmaf(h, d.y, m.x), __builtin_fmaf(-h, d.x, m.y)};
  c = {__builtin_fmaf(-h, d.y, m.x), __builtin_fmaf(h, d.x, m.y)};
}
template <int CTRL> __device__ __forceinline__ cf bfly_dpp(cf v, float sg) {
  return {__builtin_fmaf(dppf<CTRL>(v.x), sg, v.x), __builtin_fmaf(dppf<CTRL>(v.y), sg, v.y)};
}

__device__ __forceinline__ void phase1_rows(char* smem, const u16* __restrict__ x, char* ws, rsrc_t rw, const int bid, const int tid) {
  const int lane = tid & 63, wave = tid >> 6;
  cf* zc = (cf*)(smem + L_ZC);
  char* hrow = smem + L_HROW;
  const int p = lane & 31, r = 2 * wave + (lane >> 5);
  const int l = p ^ ((p & 4) ? 3 : 0);
  const u32* xr = (const u32*)(x + (size_t)((bid & 63) + 64 * (8 * (bid >> 6) + r)) * D);
  u32 raw[9];
#pragma unroll
  for (int j = 0; j < 9; ++j) raw[j] = __builtin_nontemporal_load(xr + l + 32 * j);
  cf tw1[9];
#pragma unroll
  for (int k2 = 1; k2 < 9; ++k2) tw1[k2] = ldtw(TW576.v, 2 * l * k2);
  cf ts4 = ldtw(TW576.v, 18 * (l & 15)), ts3 = ldtw(TW576.v, 36 * (l & 7)), ts2 = ldtw(TW576.v, 72 * (l & 3));
  BAR();
  float e[9], o[9];
#pragma unroll
  for (int j = 0; j < 9; ++j) { e[j] = bflo(raw[j]); o[j] = bfhi(raw[j]); }
#pragma unroll
  for (int pass = 0; pass < 2; ++pass) {
    float s = 0;
#pragma unroll
    for (int j = 0; j < 9; ++j) s += e[j] + o[j];
    const float mean = half_sum(s) * (1.0f / D);
    float q = 0;
#pragma unroll
    for (int j = 0; j < 9; ++j) { const float a = e[j] - mean, b = o[j] - mean; q = __builtin_fmaf(a, a, q); q = __builtin_fmaf(b, b, q); }
    const float var = half_sum(q) * (1.0f / D);
    const float m = rbf(mean);
    const float inv = __builtin_amdgcn_rcpf(rbf(__builtin_sqrtf(rbf(rbf(var) + 1e-5f))));
#pragma unroll
    for (int j = 0; j < 9; ++j) {
      const u32 d = pack_bf16(e[j] - m, o[j] - m);
      const u32 n = pack_bf16(bflo(d) * inv, bfhi(d) * inv);
      if (pass == 0) { e[j] = bflo(n); o[j] = bfhi(n); }
      else *(u32*)(hrow + r * 1216 + (l + 32 * j) * 4) = n;
    }
  }
  PROF_STAMP(0, 1); PROF_CLK(0, 1);
  lds_arrive(smem, 2, lane);
  cf z[9];
#pragma unroll
  for (int j = 0; j < 9; ++j) z[j] = {e[j], o[j]};
  dft3(z[0], z[3], z[6]); dft3(z[1], z[4], z[7]); dft3(z[2], z[5], z[8]);
  {
    const cf w1 = {0.766044443118978f, -0.6427876096865393f}, w2 = {0.17364817766693041f, -0.984807753012208f}, w4 = {-0.9396926207859083f, -0.3420201433256689f};
    z[4] = cmul(z[4], w1); z[7] = cmul(z[7], w2); z[5] = cmul(z[5], w2); z[8] = cmul(z[8], w4);
  }
  dft3(z[0], z[1], z[2]); dft3(z[3], z[4], z[5]); dft3(z[6], z[7], z[8]);
  cf y[9];
#pragma unroll
  for (int c = 0; c < 3; ++c)
#pragma unroll
    for (int d = 0; d < 3; ++d) y[c + 3 * d] = z[3 * c + d];
#pragma unroll
  for (int k2 = 1; k2 < 9; ++k2) y[k2] = cmul(y[k2], tw1[k2]);
  {
    const bool u4 = l & 16, u3 = l & 8, u2 = l & 4, u1 = l & 2, u0 = l & 1;
    const float s4 = u4 ? -1.f : 1.f, s3 = u3 ? -1.f : 1.f, s2 = u2 ? -1.f : 1.f, s1 = u1 ? -1.f : 1.f, s0 = u0 ? 1.f : -1.f;
    ts4 = u4 ? cf{-ts4.x, -ts4.y} : cf{1.f, 0.f};
    ts3 = u3 ? cf{-ts3.x, -ts3.y} : cf{1.f, 0.f};
    ts2 = u2 ? cf{-ts2.x, -ts2.y} : cf{1.f, 0.f};
    const cf ts1 = u1 ? (u0 ? cf{0.f, -1.f} : cf{-1.f, 0.f}) : (u0 ? cf{-1.f, 0.f} : cf{1.f, 0.f});
#pragma unroll
    for (int k = 0; k < 9; ++k) { cf t = {__builtin_fmaf(xor16f(y[k].x), s4, y[k].x), __builtin_fmaf(xor16f(y[k].y), s4, y[k].y)}; y[k] = cmul(t, ts4); }
#pragma unroll
    for (int k = 0; k < 9; ++k) y[k] = cmul(bfly_dpp<DPP_X8>(y[k], s3), ts3);
#pragma unroll
    for (int k = 0; k < 9; ++k) y[k] = cmul(bfly_dpp<DPP_X7>(y[k], s2), ts2);
#pragma unroll
    for (int k = 0; k < 9; ++k) y[k] = cmul(bfly_dpp<DPP_X2>(y[k], s1), ts1);
#pragma unroll
    for (int k = 0; k < 9; ++k) y[k] = bfly_dpp<DPP_X1>(y[k], s0);
  }
  {
    const int k1 = __builtin_bitreverse32((u32)l) >> 27;
#pragma unroll
    for (int k2 = 0; k2 < 9; ++k2) zc[r * 288 + 9 * k1 + k2] = y[k2];
  }
  PROF_STAMP(0, 2); PROF_CLK(0, 2);
  lds_arrive(smem, 3, lane);
}
template <int NT> __device__ __forceinline__ void phase1_unpack(char* smem, rsrc_t rw, const int bid, const int t) {
  constexpr int NR = (290 + NT - 1) / NT;
  const cf* zc = (const cf*)(smem + L_ZC);
  cf wk[NR];
#pragma unroll
  for (int rd = 0; rd < NR; ++rd) wk[rd] = ldtw(TW576.v, (t + NT * rd) >> 1);
  lds_wait(smem, 3, 4);
  const u32 zo = (u32)WS_Z + ((bid & 63) * 4 + (bid >> 6)) * 128;
#pragma unroll
  for (int rd = 0; rd < NR; ++rd) {
    const int it = t + NT * rd, k = it >> 1, rq = it & 1;
    if (k <= 144) {
      u32x4 v0, v1;
#pragma unroll
      for (int e2 = 0; e2 < 4; ++e2) {
        const int row = 4 * rq + e2;
        const cf a = zc[row * 288 + k], b = zc[row * 288 + (k ? 288 - k : 0)];
        const cf E = {0.5f * (a.x + b.x), 0.5f * (a.y - b.y)};
        const cf O = {0.5f * (a.y + b.y), 0.5f * (b.x - a.x)};
        const cf T = cmul(O, wk[rd]);
        v0[e2] = pack_bf16(E.x + T.x, E.y + T.y);
        v1[e2] = pack_bf16(E.x - T.x, T.y - E.y);
      }
      k1st(rw, zo + (k >> 2) * 32768 + (k & 3) * 32 + rq * 16, v0);
      k1st(rw, zo + ((288 - k) >> 2) * 32768 + ((288 - k) & 3) * 32 + rq * 16, v1);
    }
  }
}

__device__ __forceinline__ void phase1_h(char* smem, rsrc_t rw, const int bid, const int L, const int lane) {
  const u32 hb = (u32)(size_t)(smem + L_HROW);
  const int g = lane >> 4, q = (lane & 15) >> 2, pp = lane & 3;
  const u32 a = hb + q * 1216 + (320 * L + 16 * g + 4 * pp) * 2;
  u32x2 t[10];
  asm volatile("ds_read_b64_tr_b16 %0, %10\n\tds_read_b64_tr_b16 %1, %10 offset:4864\n\tds_read_b64_tr_b16 %2, %10 offset:128\n\tds_read_b64_tr_b16 %3, %10 offset:4992\n\t"
               "ds_read_b64_tr_b16 %4, %10 offset:256\n\tds_read_b64_tr_b16 %5, %10 offset:5120\n\tds_read_b64_tr_b16 %6, %10 offset:384\n\tds_read_b64_tr_b16 %7, %10 offset:5248\n\t"
               "ds_read_b64_tr_b16 %8, %10 offset:512\n\tds_read_b64_tr_b16 %9, %10 offset:5376\n\ts_waitcnt lgkmcnt(0)"
               : "=&v"(t[0]), "=&v"(t[1]), "=&v"(t[2]), "=&v"(t[3]), "=&v"(t[4]), "=&v"(t[5]), "=&v"(t[6]), "=&v"(t[7]), "=&v"(t[8]), "=&v"(t[9]) : "v"(a) : "memory");
#pragma unroll
  for (int i = 0; i < 5; ++i)
    if (i < 4 || L == 0) k1st(rw, (u32)WS_HB + (bid * D + 320 * L + 64 * i + lane) * 16, (u32x4){t[2 * i].x, t[2 * i].y, t[2 * i + 1].x, t[2 * i + 1].y});
}
__device__ __forceinline__ void phase1_wt_load(char* stg, const u16* __restrict__ w, const int unit, const int lane) {
  const u32x4 sr = make_srd(w, S * DF * 2);
  const u32 base = (u32)(size_t)stg;
  const u32 row0 = (u32)(2 * (unit & 15) + 128 * (unit >> 4)) * (DF * 2);
#pragma unroll
  for (int e = 0; e < 4; ++e)
#pragma unroll
    for (int j = 0; j < 2; ++j)
      dma16n(base + e * 2048 + j * 1024, lane * 16, sr, row0 + e * (32 * DF * 2) + j * 1024);
}
__device__ __forceinline__ void phase1_wt(char* stg, rsrc_t rw, const int unit, const int H, const int lane) {
  const u16* pr = (const u16*)stg; const u16* pi = (const u16*)(stg + 8192);
  const int i = lane & 1;
  const u32 dst = (u32)WS_WT + unit * 128 + (lane & 7) * 16 + ((lane >> 3) + 40 * H) * 32768;
#pragma unroll
  for (int rd = 0; rd < 5; ++rd) {
    const int k = 160 * H + 32 * rd + (lane >> 1);
    if (k < DF) {
      u32x4 v;
#pragma unroll
      for (int e = 0; e < 4; ++e) { const int idx = e * 1024 + i * DF + k; v[e] = (u32)pr[idx] | ((u32)pi[idx] << 16); }
      k1st(rw, dst + rd * (8 * 32768), v);
    }
  }
}

extern "C" __global__ __launch_bounds__(512) void k1_rows(const u16* __restrict__ x, const u16* __restrict__ wr, const u16* __restrict__ wi, const u16* __restrict__ w1, char* ws) {
  __shared__ __attribute__((aligned(16))) char smem[K1_LDS];
  const int bid = blockIdx.x;
  const int tid = threadIdx.x, lane = tid & 63;
  const int wave = __builtin_amdgcn_readfirstlane(tid >> 6);
  const int L = wave & 1;
  PROF_STAMP(0, 0);
  const rsrc_t rw = __builtin_amdgcn_make_buffer_rsrc(ws, 0, (int)WS_END, 0x00020000);
  if (wave < 4) {
    phase1_rows(smem, x, ws, rw, bid, tid);
    phase1_unpack<256>(smem, rw, bid, tid);
  } else if (wave < 6) {
    if (bid < 9 && wave == 4) *(u32*)(ws + WS_FG + bid * 256 + lane * 4) = 0u;
    {
      const int n = ((bid >> 3) * 2 + L) * 64 + lane;
      u64 c1, c2;
      asm volatile("s_getpc_b64 vcc\n\ts_add_u32 vcc_lo, vcc_lo, k1_rows@rel32@lo+4\n\ts_addc_u32 vcc_hi, vcc_hi, k1_rows@rel32@hi+12\n\ts_mov_b64 %0, vcc" : "=s"(c1) :: "vcc", "scc");
      asm volatile("s_getpc_b64 vcc\n\ts_add_u32 vcc_lo, vcc_lo, k2_fused@rel32@lo+4\n\ts_addc_u32 vcc_hi, vcc_hi, k2_fused@rel32@hi+12\n\ts_mov_b64 %0, vcc" : "=s"(c2) :: "vcc", "scc");
      const int w = (bid >> 3) * 2 + L;
      const u32 pad = (u32)(size_t)smem + L_PF1;
      if (w < 2) touch(pad, n * 128, make_srd((const void*)c1, K1_CODE), 0);
      else if (w < 8) touch(pad, (n - 128) * 128, make_srd((const void*)c2, K2_CODE), 0);
      else if (w < 14) touch(pad, (n - 512) * 128, make_srd(&FTAB, 49152), 0);
      else if (w < 40) {
        const int kq = (bid & 7) >> 1;
        touch(pad, (u32)(2 * kq * 73728 + (n - 896) * 128), make_srd(IDFT, (u32)((2 * kq + (kq == 3 ? 3 : 2)) * 73728)), 0);
      }
    }
    BAR();
    if (PF1) {
      const u32x4 srdW1 = make_srd(w1, (u32)((size_t)S * F * 2));
#pragma unroll 1
      for (int n = ((bid >> 3) * 2 + L) * 64; n < PF1 * 640; n += 4096) touch((u32)(size_t)smem + L_PF1, w1_line(bid & 7, n + lane), srdW1, 0);
    }
    lds_wait(smem, 2, 4);
    phase1_h(smem, rw, bid, L, lane);
  } else {
    if (wave == 7 && lane < 16) *(u32*)(smem + L_SYNC + lane * 4) = 0u;
    phase1_wt_load(smem + L_WTS + L * 8192, L ? wi : wr, bid, lane);
    asm volatile("s_waitcnt lgkmcnt(0)\n\ts_barrier" ::: "memory");
    WAITV(0);
    lds_arrive(smem, 6, lane);
    lds_wait(smem, 6, 2);
    phase1_wt(smem + L_WTS, rw, bid, L, lane);
  }
  PROF_STAMP(0, 3);
  PROF_STAMP(0, 4);
}

__device__ __forceinline__ void pack_frags(const f32x16& v, u32x4& f0, u32x4& f1) {
  f0 = (u32x4){pack_bf16(v[0], v[1]), pack_bf16(v[2], v[3]), pack_bf16(v[4], v[5]), pack_bf16(v[6], v[7])};
  f1 = (u32x4){pack_bf16(v[8], v[9]), pack_bf16(v[10], v[11]), pack_bf16(v[12], v[13]), pack_bf16(v[14], v[15])};
}
template <int INV> __device__ __forceinline__ void dft64(const char* tab, const int lane, f32x16 (&re)[2], f32x16 (&im)[2]) {
  u32x4 br[4], bi[4], bn[4];
  pack_frags(re[0], br[0], br[1]); pack_frags(re[1], br[2], br[3]);
  pack_frags(im[0], bi[0], bi[1]); pack_frags(im[1], bi[2], bi[3]);
#pragma unroll
  for (int m = 0; m < 4; ++m) bn[m] = (INV ? br[m] : bi[m]) ^ 0x80008000u;
  const u32x4* t2 = (const u32x4*)(tab + 8192) + lane;
#pragma unroll
  for (int ub = 0; ub < 2; ++ub) {
    f32x16 xr = {}, xi = {};
#pragma unroll
    for (int m = 0; m < 4; ++m) {
      const u32x4 fr = t2[(ub * 4 + m) * 64], fi = t2[(8 + ub * 4 + m) * 64];
      xr = mfma32(fr, br[m], xr);
      xi = mfma32(fr, bi[m], xi);
      xr = mfma32(fi, INV ? bi[m] : bn[m], xr);
      xi = mfma32(fi, INV ? bn[m] : br[m], xi);
    }
    re[ub] = xr; im[ub] = xi;
  }
}
template <int INV> __device__ __forceinline__ void twid(const char* tab, const int lane, f32x16 (&re)[2], f32x16 (&im)[2]) {
  const f32x4* tw = (const f32x4*)(tab + 32768) + lane;
#pragma unroll
  for (int rb = 0; rb < 2; ++rb)
#pragma unroll
    for (int p = 0; p < 8; ++p) {
      const f32x4 w = tw[(rb * 8 + p) * 64];
      cf a = {re[rb][2 * p], im[rb][2 * p]}, b = {re[rb][2 * p + 1], im[rb][2 * p + 1]};
      a = INV ? cmulc(a, cf{w.x, w.y}) : cmul(a, cf{w.x, w.y});
      b = INV ? cmulc(b, cf{w.z, w.w}) : cmul(b, cf{w.z, w.w});
      re[rb][2 * p] = a.x; im[rb][2 * p] = a.y; re[rb][2 * p + 1] = b.x; im[rb][2 * p + 1] = b.y;
    }
}
#define WAVE_LDS_FENCE() do { __builtin_amdgcn_fence(__ATOMIC_RELEASE, "wavefront"); __builtin_amdgcn_wave_barrier(); __builtin_amdgcn_fence(__ATOMIC_ACQUIRE, "wavefront"); } while (0)

__device__ __forceinline__ void fft_role(char* smem, char* ws, rsrc_t rw, const int fb, const int wave, const int lane, const u32x4 srdW1) {
  const int kk = fb * 8 + wave, k = kk < DF ? kk : DF - 1;
  const int r = lane & 31, h = lane >> 5;
  {
    const u32x4 srdWS = make_srd(ws, (u32)WS_END), srdT = make_srd(&FTAB, 49152);
    const u32 ldsb = (u32)(size_t)smem;
#pragma unroll
    for (int j = 0; j < 8; ++j) {
      const int q = lane ^ (2 * j);
      dma16(ldsb + FFT_ZS + (wave * 8 + j) * 1024, (u32)(((q & 1) * 32 + (q >> 1)) * 16), srdWS, (u32)WS_Z + (u32)fb * 65536 + (wave * 8 + j) * 1024);
    }
#pragma unroll
    for (int j = 0; j < 6; ++j) dma16(ldsb + FFT_TAB + (wave * 6 + j) * 1024, lane * 16, srdT, (wave * 6 + j) * 1024);
  }
  WAITV(0); BAR();
  u32x4 wv[2][4];
  {
    const u32 wo = (u32)WS_WT + (k >> 2) * 32768 + (h * 16 + (r >> 1)) * 128 + (k & 3) * 32 + (r & 1) * 16;
#pragma unroll
    for (int ub = 0; ub < 2; ++ub)
#pragma unroll
      for (int g = 0; g < 4; ++g) wv[ub][g] = __builtin_amdgcn_raw_buffer_load_b128(rw, wo + (8 * ub + 2 * g) * 2048, 0, 0);
  }
  if (PFMODE == 2) {
    const int g8 = fb & 7, ng = g8 < 5 ? 40 : 32;
#pragma unroll 1
    for (int n = PF1 * 640 + ((fb >> 3) * 8 + wave) * 64; n < 32 * 640; n += ng * 64) touch((u32)(size_t)smem + L_PF, w1_line(g8, n + lane), srdW1, 0);
  }
  u32x4 za[2][4];
  {
    const int kl = k - fb * 8;
    const char* zs = smem + FFT_ZS + (kl >> 2) * 32768 + (r >> 1) * 1024;
#pragma unroll
    for (int rb = 0; rb < 2; ++rb)
#pragma unroll
      for (int m = 0; m < 4; ++m) {
        const int c = 2 * h + (m >> 1), slot = (kl & 3) * 2 + (m & 1);
        za[rb][m] = *(const u32x4*)(zs + rb * 16384 + ((((8 * c + slot) * 2 + (r & 1)) ^ (2 * ((r >> 1) & 7))) * 16));
      }
  }
  asm volatile("s_waitcnt lgkmcnt(0)\n\ts_barrier" : "+v"(za[0][0]), "+v"(za[0][1]), "+v"(za[0][2]), "+v"(za[0][3]), "+v"(za[1][0]), "+v"(za[1][1]), "+v"(za[1][2]), "+v"(za[1][3]) :: "memory");
  const char* tab = smem + FFT_TAB;
  char* img = smem + FFT_WAVE + wave * FFT_WAVE_B;
  f32x16 re[2], im[2];
  {
    const u32x4* t1 = (const u32x4*)tab + lane;
    re[0] = f32x16{}; re[1] = f32x16{}; im[0] = f32x16{}; im[1] = f32x16{};
#pragma unroll
    for (int m = 0; m < 4; ++m) {
      const u32x4 fr = t1[m * 64], fi = t1[(4 + m) * 64];
      re[0] = mfma32(za[0][m], fr, re[0]); im[0] = mfma32(za[0][m], fi, im[0]);
      re[1] = mfma32(za[1][m], fr, re[1]); im[1] = mfma32(za[1][m], fi, im[1]);
    }
  }
  twid<0>(tab, lane, re, im);
  dft64<0>(tab, lane, re, im);
#pragma unroll
  for (int ub = 0; ub < 2; ++ub)
#pragma unroll
    for (int rho = 0; rho < 16; ++rho) {
      const cf p = cmul(cf{re[ub][rho], im[ub][rho]}, cf{bflo(wv[ub][rho >> 2][rho & 3]), bfhi(wv[ub][rho >> 2][rho & 3])});
      re[ub][rho] = p.x; im[ub][rho] = p.y;
    }
  dft64<1>(tab, lane, re, im);
  twid<1>(tab, lane, re, im);
#pragma unroll
  for (int rb = 0; rb < 2; ++rb)
#pragma unroll
    for (int g = 0; g < 4; ++g) {
      char* p = img + r * 136 + (32 * rb + 8 * g + 4 * h) * 2;
      *(u32x2*)p = (u32x2){pack_bf16(re[rb][4 * g], re[rb][4 * g + 1]), pack_bf16(re[rb][4 * g + 2], re[rb][4 * g + 3])};
      *(u32x2*)(p + 4352) = (u32x2){pack_bf16(im[rb][4 * g], im[rb][4 * g + 1]), pack_bf16(im[rb][4 * g + 2], im[rb][4 * g + 3])};
    }
  u32x4 va[2][2][2];
  {
    const int blk = (lane >> 4) & 1, q = (lane & 15) >> 2, p = lane & 3;
    const u32 a = (u32)(size_t)img + (8 * h + q) * 136 + (16 * blk + 4 * p) * 2;
    u32x2 t[16];
    asm volatile("s_waitcnt lgkmcnt(0)\n\t"
                 "ds_read_b64_tr_b16 %0, %16\n\tds_read_b64_tr_b16 %1, %16 offset:544\n\tds_read_b64_tr_b16 %2, %16 offset:2176\n\tds_read_b64_tr_b16 %3, %16 offset:2720\n\t"
                 "ds_read_b64_tr_b16 %4, %16 offset:64\n\tds_read_b64_tr_b16 %5, %16 offset:608\n\tds_read_b64_tr_b16 %6, %16 offset:2240\n\tds_read_b64_tr_b16 %7, %16 offset:2784\n\t"
                 "ds_read_b64_tr_b16 %8, %16 offset:4352\n\tds_read_b64_tr_b16 %9, %16 offset:4896\n\tds_read_b64_tr_b16 %10, %16 offset:6528\n\tds_read_b64_tr_b16 %11, %16 offset:7072\n\t"
                 "ds_read_b64_tr_b16 %12, %16 offset:4416\n\tds_read_b64_tr_b16 %13, %16 offset:4960\n\tds_read_b64_tr_b16 %14, %16 offset:6592\n\tds_read_b64_tr_b16 %15, %16 offset:7136\n\t"
                 "s_waitcnt lgkmcnt(0)"
                 : "=&v"(t[0]), "=&v"(t[1]), "=&v"(t[2]), "=&v"(t[3]), "=&v"(t[4]), "=&v"(t[5]), "=&v"(t[6]), "=&v"(t[7]),
                   "=&v"(t[8]), "=&v"(t[9]), "=&v"(t[10]), "=&v"(t[11]), "=&v"(t[12]), "=&v"(t[13]), "=&v"(t[14]), "=&v"(t[15])
                 : "v"(a) : "memory");
#pragma unroll
    for (int i = 0; i < 8; ++i) va[i >> 2][(i >> 1) & 1][i & 1] = (u32x4){t[2 * i].x, t[2 * i].y, t[2 * i + 1].x, t[2 * i + 1].y};
  }
  {
    const u32x4* t4 = (const u32x4*)(tab + 24576) + lane;
    re[0] = f32x16{}; re[1] = f32x16{}; im[0] = f32x16{}; im[1] = f32x16{};
#pragma unroll
    for (int ks = 0; ks < 2; ++ks) {
      const u32x4 gr = t4[ks * 64], gi = t4[(2 + ks) * 64], gn = t4[(4 + ks) * 64];
#pragma unroll
      for (int rb = 0; rb < 2; ++rb) {
        re[rb] = mfma32(va[0][rb][ks], gr, re[rb]); im[rb] = mfma32(va[0][rb][ks], gi, im[rb]);
        re[rb] = mfma32(va[1][rb][ks], gn, re[rb]); im[rb] = mfma32(va[1][rb][ks], gr, im[rb]);
      }
    }
  }
#pragma unroll
  for (int rb = 0; rb < 2; ++rb)
#pragma unroll
    for (int g = 0; g < 4; ++g) {
      char* p = img + r * 144 + (32 * rb + 8 * g + 4 * h) * 2;
      *(u32x2*)p = (u32x2){pack_bf16(re[rb][4 * g], re[rb][4 * g + 1]), pack_bf16(re[rb][4 * g + 2], re[rb][4 * g + 3])};
      *(u32x2*)(p + 4608) = (u32x2){pack_bf16(im[rb][4 * g], im[rb][4 * g + 1]), pack_bf16(im[rb][4 * g + 2], im[rb][4 * g + 3])};
    }
  WAVE_LDS_FENCE();
  if (kk < DF) {
    const int rowRe = (k == DF - 1) ? 1 : 2 * k;
#pragma unroll
    for (int i = 0; i < 4; ++i) st16(rw, (u32)WS_YT + (rowRe * 256 + 64 * i + lane) * 16, *(const u32x4*)(img + (8 * i + (lane >> 3)) * 144 + (lane & 7) * 16));
    if (k > 0 && k < DF - 1) {
#pragma unroll
      for (int i = 0; i < 4; ++i) st16(rw, (u32)WS_YT + ((2 * k + 1) * 256 + 64 * i + lane) * 16, *(const u32x4*)(img + 4608 + (8 * i + (lane >> 3)) * 144 + (lane & 7) * 16));
    }
  }
}

__device__ __forceinline__ void g1_store(const f32x4 a, const u32x2 bb, rsrc_t rw, const int fb0, const int d) {
  const float bv[4] = {bflo(bb.x), bfhi(bb.x), bflo(bb.y), bfhi(bb.y)};
  float r[4];
#pragma unroll
  for (int e = 0; e < 4; ++e) {
    const float u = a[e] + bv[e];
    const float z = u * __builtin_fmaf(u * u, -0.044715f * 2.0f * 0.7978845608028654f * 1.4426950408889634f, -2.0f * 0.7978845608028654f * 1.4426950408889634f);
    r[e] = u * __builtin_amdgcn_rcpf(1.0f + __builtin_amdgcn_exp2f(z));
  }
  st8(rw, (u32)WS_GB + ((fb0 >> 3) * D + d) * 16 + (fb0 & 4) * 2, (u32x2){pack_bf16(r[0], r[1]), pack_bf16(r[2], r[3])});
}

constexpr int g2_owner(int i, int j) { return j < 8 ? (j >> 1) : i; }
template <int Q>
__device__ __forceinline__ void g2_finish(f32x4 (&acc)[2][9], const u16* __restrict__ x, const u16* __restrict__ b2, char* ws, rsrc_t rw, u16* __restrict__ out,
                                          const int tile, const int s0, const int d0, const int wave, const int lane) {
  const int srow = s0 + wave * 32 + (lane & 15), dcol = d0 + 4 * (lane >> 4);
  u32x2 xr[2][9]; float bs[2];
#pragma unroll
  for (int i = 0; i < 2; ++i) {
    bs[i] = bflo((u32)b2[srow + 16 * i]);
#pragma unroll
    for (int j = 0; j < 9; ++j)
      if (g2_owner(i, j) == Q) xr[i][j] = *(const u32x2*)(x + (size_t)(srow + 16 * i) * D + dcol + 16 * j);
  }
  const u32 mine = (u32)WS_P + (u32)(((tile * 4 + Q) * 4 + wave) * 10) * 1024;
#pragma unroll
  for (int j = 0; j < 8; ++j)
    if ((j >> 1) != Q)
      st16(rw, mine + j * 1024 + lane * 16, (u32x4){pack_bf16(acc[0][j].x, acc[0][j].y), pack_bf16(acc[0][j].z, acc[0][j].w), pack_bf16(acc[1][j].x, acc[1][j].y), pack_bf16(acc[1][j].z, acc[1][j].w)});
#pragma unroll
  for (int i = 0; i < 2; ++i)
    if (i != Q) st8(rw, mine + (8 + i) * 1024 + lane * 8, (u32x2){pack_bf16(acc[i][8].x, acc[i][8].y), pack_bf16(acc[i][8].z, acc[i][8].w)});
  asm volatile("s_waitcnt vmcnt(0)\n\ts_barrier" ::: "memory");
  PROF_STAMP(2, 3);
  if (wave == 0) {
    if (lane == 0) set_flag(ws, WS_FP + (tile * 4 + Q) * 4);
    wait_lanes(ws, (lane < 4 && lane != Q) ? (u32)WS_FP + (tile * 4 + lane) * 4 : ~0u);
  }
  BAR();
  PROF_STAMP(2, 4);
  u32x4 pv[3][2]; u32x2 pe[3];
#pragma unroll
  for (int o = 0; o < 3; ++o) {
    const int src = o < Q ? o : o + 1;
    const u32 base = (u32)WS_P + (u32)(((tile * 4 + src) * 4 + wave) * 10) * 1024;
    pv[o][0] = ld16(rw, base + (2 * Q) * 1024 + lane * 16);
    pv[o][1] = ld16(rw, base + (2 * Q + 1) * 1024 + lane * 16);
    if (Q < 2) pe[o] = __builtin_amdgcn_raw_buffer_load_b64(rw, base + (8 + Q) * 1024 + lane * 8, 0, 16);
  }
#pragma unroll
  for (int i = 0; i < 2; ++i)
#pragma unroll
    for (int j = 0; j < 9; ++j)
      if (g2_owner(i, j) == Q) {
        f32x4 t = acc[i][j];
#pragma unroll
        for (int o = 0; o < 3; ++o) {
          const u32 lo = j < 8 ? pv[o][j & 1][2 * i] : pe[o].x, hi = j < 8 ? pv[o][j & 1][2 * i + 1] : pe[o].y;
          t += (f32x4){bflo(lo), bfhi(lo), bflo(hi), bfhi(hi)};
        }
        const u32x2 xv = xr[i][j];
        const u32x2 o2 = {pack_bf16(t.x + bs[i] + bflo(xv.x), t.y + bs[i] + bfhi(xv.x)), pack_bf16(t.z + bs[i] + bflo(xv.y), t.w + bs[i] + bfhi(xv.y))};
        *(u32x2*)(out + (size_t)(srow + 16 * i) * D + dcol + 16 * j) = o2;
      }
}

struct Geo {
  int f0, d1, ft, dt;
  int kq, s0, d2, tile, dt2, nD, eb;
};

extern "C" __global__ __launch_bounds__(512, 2) void k2_fused(const u16* __restrict__ x,
                                                   const u16* __restrict__ w1, const u16* __restrict__ b1, const u16* __restrict__ w2,
                                                   const u16* __restrict__ b2, char* ws, u16* __restrict__ out) {
  extern __shared__ __attribute__((aligned(16))) char smem[];
  const int bid = blockIdx.x;
  const int tid = threadIdx.x, lane = tid & 63;
  const int wave = __builtin_amdgcn_readfirstlane(tid >> 6);
  const u32 ldsb = (u32)(size_t)smem;
  PROF_STAMP(1, 0);
  const rsrc_t rw = __builtin_amdgcn_make_buffer_rsrc(ws, 0, (int)WS_END, 0x00020000);
  const bool isG1 = bid < G1_WGS, isFFT = !isG1 && bid < G1_WGS + FFT_WGS;
  Geo G;
  {
    const int xg = bid & 7, idx = bid >> 3;
    const int lin = xg * 27 + idx;
    G.ft = lin / 6; G.dt = lin - G.ft * 6; G.f0 = G.ft * 128; G.d1 = G.dt * 96;
    G.kq = xg >> 1;
    const int st = (xg & 1) * 8 + (idx >> 2);
    G.dt2 = idx & 3; G.s0 = st * 128; G.d2 = G.dt2 * 144; G.tile = st * 4 + G.dt2;
    G.nD = G.kq == 3 ? 3 : 2; G.eb = 2 * G.kq;
  }
  const int L = wave & 1;
  const int kk = lane >> 4;
  const u32 cs0 = ((lane & 15) ^ (kk << 1)) * 16, cs1 = ((lane & 15) ^ ((kk << 1) | 8)) * 16;
  const u32 mW = ldsb + L_W + L * 8192;
  const u32x4 srdW1 = make_srd(w1, (u32)((size_t)S * F * 2));
  const u32 vW0 = (u32)((64 * kk + 4 * L) * (F * 2) + G.f0 * 2) + cs0, vW1 = (u32)((64 * kk + 4 * L) * (F * 2) + G.f0 * 2) + cs1;
#define G1_W(t, sl) do { const u32 _s = (u32)(8 * ((t) & 7) + 512 * ((t) >> 3)) * (F * 2); \
    _Pragma("unroll") for (int j = 0; j < 8; ++j) dma16(mW + (sl) * 16384 + j * 1024, (j & 2) ? vW1 : vW0, srdW1, _s + ((j >> 1) + 256 * (j & 1)) * (F * 2)); } while (0)

  if (isFFT) {
    fft_role(smem, ws, rw, bid - G1_WGS, wave, lane, srdW1);
    PROF_STAMP(1, 2);
    WAITV(0); BAR();
    if (wave == 4 && lane == 0) set_flag(ws, WS_FY + (bid - G1_WGS) * 4);
  }

  if (wave >= 4) {
    const int wd = (wave >> 1) & 1, wf = wave & 1;
    const int fbase = G.f0 + wf * 64 + 32 + 4 * kk, dbase = G.d1 + wd * 48 + (lane & 15);
    const u32 fyoff = (u32)WS_FY + (lane < 4 * G.nD ? 4 * G.eb + lane : 36) * 4;
    if (wave >= 6) {
      if (isG1) {
        const u32x2 bb0 = *(const u32x2*)(b1 + fbase), bb1 = *(const u32x2*)(b1 + fbase + 16);
        if (wave == 7) {
          const int me = (bid >> 3) * 64;
          const u32x4 srdW2 = make_srd(w2, (u32)((size_t)F * S * 2));
          int n1 = PF1 * 640 + me, n2 = me, nxt = (bid >> 3) % PFS;
#pragma unroll 1
          for (int i = 0; i < 35; ++i) {
            BAR();
            if (PFMODE == 1 && i == nxt) {
              nxt += PFS;
              if (n1 < 32 * 640) { touch(ldsb + L_PF, w1_line(bid & 7, n1 + lane), srdW1, 0); n1 += 1728; }
              else if (n2 < 18432) {
                const int n = n2 + lane;
                touch(ldsb + L_PF, (u32)((1152 * G.kq + (n >> 4)) * (S * 2) + (bid & 1) * 2048 + (n & 15) * 128), srdW2, 0);
                n2 += 1728;
              }
            }
          }
        } else {
          const gu32* fy = (const gu32*)(ws + fyoff);
          if (lane == 0) *(volatile LAS u32*)(LAS char*)(smem + L_PF + 256) = 0u;
          u32 seen = 0;
#pragma unroll 1
          for (int i = 0; i < 35; ++i) {
            BAR();
            if (i == 25 || i == 28 || i == 31) {
              if (i != 25 && __all(seen != 0u) && lane == 0) *(volatile LAS u32*)(LAS char*)(smem + L_PF + 256) = 1u;
              asm volatile("s_waitcnt lgkmcnt(0)" ::: "memory");
              if (i != 31) seen = __hip_atomic_load(fy, __ATOMIC_RELAXED, __HIP_MEMORY_SCOPE_AGENT);
            }
          }
        }
        const f32x4* ex = (const f32x4*)(smem + L_EX);
#pragma unroll
        for (int ii = 0; ii < 2; ++ii)
#pragma unroll
          for (int j = 0; j < 3; ++j) g1_store(ex[((wave - 4) * 6 + ii * 3 + j) * 64 + lane], ii ? bb1 : bb0, rw, fbase + 16 * ii, dbase + 16 * j);
        asm volatile("s_waitcnt vmcnt(0) lgkmcnt(0)\n\ts_barrier" ::: "memory");
      }
      return;
    }
    const u32x4 srdWS = make_srd(ws, (u32)WS_END);
    if (isG1) {
      u32 vH[6];
#pragma unroll
      for (int j = 0; j < 6; ++j) { const int p = j * 64 + lane; const int kc = p / 96, dd = p - kc * 96; vH[j] = (u32)WS_HB + (u32)(((4 * L + kc) * D + G.d1 + dd) * 16); }
      const u32 mH = ldsb + L_X + L * 6144;
#define G1_H(t, sl) do { _Pragma("unroll") for (int j = 0; j < 6; ++j) dma16(mH + (sl) * 12288 + j * 1024, vH[j], srdWS, (u32)(t) * (8 * D * 16)); } while (0)
#pragma unroll
      for (int t = 0; t < 4; ++t) { G1_W(t, t); G1_H(t, t); }
      WAITV(42); BAR();
      WAITV(28); BAR(); G1_W(4, 4); G1_H(4, 4);
      WAITV(28); BAR(); G1_W(5, 0); G1_H(5, 0);
      int sl = 1;
#pragma unroll 1
      for (int t = 2; t < 28; ++t) {
        WAITV(28); BAR();
        G1_W(t + 4, sl); G1_H(t + 4, sl);
        sl = sl == 4 ? 0 : sl + 1;
      }
      WAITV(28); BAR();
      WAITV(14); BAR();
      WAITV(0); BAR();
    }
    const u32x4 srdW2 = make_srd(w2, (u32)((size_t)F * S * 2));
    int ln2 = lane; asm volatile("" : "+v"(ln2));
    const int kk2 = ln2 >> 4;
    const u32 vA0 = (u32)((32 * L + kk2) * (S * 2) + G.s0 * 2) + ((ln2 & 15) ^ (kk2 << 1)) * 16, vA1 = (u32)((32 * L + kk2) * (S * 2) + G.s0 * 2) + ((ln2 & 15) ^ ((kk2 << 1) | 8)) * 16;
    u32 vG[9];
#pragma unroll
    for (int j = 0; j < 9; ++j) { const int p = j * 64 + ln2; const int kc = p / 144, dd = p - kc * 144; vG[j] = (u32)WS_GB + (u32)(((4 * L + kc) * D + G.d2 + dd) * 16); }
    const u32 mG = ldsb + L_X + L * 9216;
    const int nD = G.nD, nk = nD + 18;
#define G2_WY(e, sl) do { _Pragma("unroll") for (int j = 0; j < 8; ++j) dma16s(mW + (sl) * 16384 + j * 1024, (j & 2) ? vA1 : vA0, srdWS, (u32)WS_YT + (u32)(e) * (64 * S * 2) + j * (4 * S * 2)); } while (0)
#define G2_WW(t, sl) do { _Pragma("unroll") for (int j = 0; j < 8; ++j) dma16(mW + (sl) * 16384 + j * 1024, (j & 2) ? vA1 : vA0, srdW2, (u32)(t) * (64 * S * 2) + j * (4 * S * 2)); } while (0)
    const u32x4 srdI = make_srd((const char*)IDFT - WS_GB, (u32)(WS_GB + sizeof(IDFT)));
#define G2_GI(e, sl) do { _Pragma("unroll") for (int j = 0; j < 9; ++j) dma16(mG + (sl) * 18432 + j * 1024, vG[j], srdI, (u32)(e) * (8 * D * 16)); } while (0)
#define G2_G(t, sl) do { _Pragma("unroll") for (int j = 0; j < 9; ++j) dma16s(mG + (sl) * 18432 + j * 1024, vG[j], srdWS, (u32)(t) * (8 * D * 16)); } while (0)
    bool early = false;
    if (isG1) {
      const u32x2 bb0 = *(const u32x2*)(b1 + fbase), bb1 = *(const u32x2*)(b1 + fbase + 16);
      BAR(); BAR();
      early = __builtin_amdgcn_readfirstlane(*(volatile LAS u32*)(LAS char*)(smem + L_PF + 256)) != 0;
      if (early) { G2_WY(G.eb, 0); G2_WY(G.eb + 1, 1); if (nD == 3) G2_WY(G.eb + 2, 2); }
      if (nD == 2) G2_WW(18 * G.kq, 2);
      G2_GI(G.eb, 0); G2_GI(G.eb + 1, 1);
      if (nD == 3) G2_GI(G.eb + 2, 2);
      BAR();
      const f32x4* ex = (const f32x4*)(smem + L_EX);
#pragma unroll
      for (int ii = 0; ii < 2; ++ii)
#pragma unroll
        for (int j = 0; j < 3; ++j) g1_store(ex[(L * 6 + ii * 3 + j) * 64 + lane], ii ? bb1 : bb0, rw, fbase + 16 * ii, dbase + 16 * j);
      asm volatile("s_waitcnt vmcnt(0) lgkmcnt(0)\n\ts_barrier" ::: "memory");
      if (wave == 4 && lane == 0) set_flag(ws, WS_FG + (G.ft * 6 + G.dt) * 4);
    } else {
      if (nD == 2) G2_WW(18 * G.kq, 2);
      G2_GI(G.eb, 0); G2_GI(G.eb + 1, 1);
      if (nD == 3) G2_GI(G.eb + 2, 2);
    }
    if (!early) {
      wait_lanes(ws, fyoff);
      G2_WY(G.eb, 0); G2_WY(G.eb + 1, 1);
      if (nD == 3) G2_WY(G.eb + 2, 2);
    }
    WAITV(0); BAR();
    PROF_STAMP(2, 0);
    wait_lanes(ws, lane < 18 ? (u32)WS_FG + ((9 * G.kq + (lane >> 1)) * 6 + ((G.dt2 * 3) >> 1) + (lane & 1)) * 4 : ~0u);
    PROF_STAMP(2, 1);
    const int t0 = 18 * G.kq - nD;
    if (nD == 2) {
      G2_G(t0 + 2, 2);
      G2_WW(t0 + 3, 3); G2_G(t0 + 3, 3);
      WAITV(17); BAR();
      G2_WW(t0 + 4, 0); G2_G(t0 + 4, 0);
    } else {
      G2_WW(t0 + 3, 3); G2_G(t0 + 3, 3);
      WAITV(0); BAR();
      G2_WW(t0 + 4, 0); G2_G(t0 + 4, 0);
      G2_WW(t0 + 5, 1); G2_G(t0 + 5, 1);
    }
#pragma unroll 1
    for (int sg = nD; sg < nk - 3; ++sg) {
      WAITV(17); BAR();
      const int sl = (sg + 3) & 3;
      G2_WW(t0 + sg + 3, sl); G2_G(t0 + sg + 3, sl);
    }
    WAITV(17); BAR();
    WAITV(0); BAR();
    return;
  }

  const int lg = lane >> 4, lq = (lane & 15) >> 2, lp = lane & 3;
  const u32 trbase = ldsb + L_W + (u32)((8 * lg + lq) * 256 + (lp & 1) * 8);
  const int swz = (lq << 1) | ((lg & 1) << 3);
  if (isG1) {
    const int wd = (wave >> 1) & 1, wf = wave & 1;
    u32 aA[4];
#pragma unroll
    for (int i = 0; i < 4; ++i) aA[i] = trbase + (u32)(((wf * 8 + 2 * i + (lp >> 1)) ^ swz) * 16);
    const u32 aB = ldsb + L_X + (u32)((lg * 96 + wd * 48 + (lane & 15)) * 16);
    const int fbase = G.f0 + wf * 64 + 4 * lg, dbase = G.d1 + wd * 48 + (lane & 15);
    const u32x2 bb0 = *(const u32x2*)(b1 + fbase), bb1 = *(const u32x2*)(b1 + fbase + 16);
    f32x4 acc[4][3];
#pragma unroll
    for (int i = 0; i < 4; ++i)
#pragma unroll
      for (int j = 0; j < 3; ++j) acc[i][j] = (f32x4){0.f, 0.f, 0.f, 0.f};
    u32x4 fb[2][3];
    u32x2 al[2][4], ah[2][4];
#define G1_RDB(dst, J, a, H) dst = rd128<6144 * (H) + 256 * (J)>(a)
#define P1_M(I, J, C) do { acc[I][J] = mfma16((u32x4){al[C][I].x, al[C][I].y, ah[C][I].x, ah[C][I].y}, fb[C][J], acc[I][J]); SB0; } while (0)
#define P1_HALF(C, N, sl_, H) do { \
    const u32 ab = aB + (sl_) * 12288, a0 = aA[0] + (sl_) * 16384, a1 = aA[1] + (sl_) * 16384, a2 = aA[2] + (sl_) * 16384, a3 = aA[3] + (sl_) * 16384; \
    WL(8); P1_M(0, 0, C); G1_RDB(fb[N][0], 0, ab, H); SB0; \
    WL(8); P1_M(0, 1, C); al[N][0] = trd<8192 * (H)>(a0); SB0; \
    WL(8); P1_M(0, 2, C); ah[N][0] = trd<8192 * (H) + 1024>(a0); SB0; \
    WL(7); P1_M(1, 0, C); G1_RDB(fb[N][1], 1, ab, H); SB0; \
    P1_M(1, 1, C); G1_RDB(fb[N][2], 2, ab, H); SB0; \
    P1_M(1, 2, C); al[N][1] = trd<8192 * (H)>(a1); SB0; \
    WL(8); P1_M(2, 0, C); ah[N][1] = trd<8192 * (H) + 1024>(a1); SB0; \
    P1_M(2, 1, C); al[N][2] = trd<8192 * (H)>(a2); SB0; \
    P1_M(2, 2, C); ah[N][2] = trd<8192 * (H) + 1024>(a2); SB0; \
    WL(9); P1_M(3, 0, C); al[N][3] = trd<8192 * (H)>(a3); SB0; \
    P1_M(3, 1, C); ah[N][3] = trd<8192 * (H) + 1024>(a3); SB0; \
    P1_M(3, 2, C); } while (0)
    asm volatile("s_waitcnt lgkmcnt(0)\n\ts_barrier" ::: "memory");
    PROF_STAMP(1, 1); PROF_CLK(1, 1);
    G1_RDB(fb[0][0], 0, aB, 0); al[0][0] = trd<0>(aA[0]); ah[0][0] = trd<1024>(aA[0]); G1_RDB(fb[0][1], 1, aB, 0); G1_RDB(fb[0][2], 2, aB, 0);
#pragma unroll
    for (int i = 1; i < 4; ++i) { al[0][i] = trd<0>(aA[i]); ah[0][i] = trd<1024>(aA[i]); }
    u32 sl = 0;
#pragma unroll 1
    for (int t = 0; t < 32; ++t) {
      BAR();
      SB0;
      const u32 nsl = sl == 4 ? 0 : sl + 1;
      P1_HALF(0, 1, sl, 1);
      P1_HALF(1, 0, nsl, 0);
      sl = nsl;
    }
    asm volatile("s_waitcnt lgkmcnt(0)\n\ts_barrier" ::: "memory");
    SB0;
    PROF_STAMP(1, 2); PROF_CLK(1, 2);
    f32x4* ex = (f32x4*)(smem + L_EX);
#pragma unroll
    for (int ii = 0; ii < 2; ++ii)
#pragma unroll
      for (int j = 0; j < 3; ++j) ex[(wave * 6 + ii * 3 + j) * 64 + lane] = acc[2 + ii][j];
    asm volatile("s_waitcnt lgkmcnt(0)\n\ts_barrier" ::: "memory");
#pragma unroll
    for (int ii = 0; ii < 2; ++ii)
#pragma unroll
      for (int j = 0; j < 3; ++j) g1_store(acc[ii][j], ii ? bb1 : bb0, rw, fbase + 16 * ii, dbase + 16 * j);
    PROF_STAMP(1, 3);
    asm volatile("s_waitcnt vmcnt(0)\n\ts_barrier" ::: "memory");
    if (PFMODE == 2) {
      const u32x4 srdW2 = make_srd(w2, (u32)((size_t)F * S * 2));
#pragma unroll 1
      for (int n = 3 * 1024 + ((bid >> 3) * 4 + wave) * 64 + lane; n < PFW2N * 1024; n += 27 * 256)
        touch(ldsb + L_PF, (u32)((1152 * G.kq + (n >> 4)) * (S * 2) + (bid & 1) * 2048 + (n & 15) * 128), srdW2, 0);
    }
  }
  BAR();
  PROF_STAMP(2, 0); PROF_CLK(2, 0);
  {
    const u32 aW0 = trbase + (u32)(((wave * 4 + (lp >> 1)) ^ swz) * 16), aW1 = trbase + (u32)(((wave * 4 + 2 + (lp >> 1)) ^ swz) * 16);
    const u32 aG = ldsb + L_X + (u32)((lg * 144 + (lane & 15)) * 16);
    f32x4 acc[2][9];
#pragma unroll
    for (int i = 0; i < 2; ++i)
#pragma unroll
      for (int j = 0; j < 9; ++j) acc[i][j] = (f32x4){0.f, 0.f, 0.f, 0.f};
    u32x4 fg[2][9];
    u32x2 wl[2][2], wh[2][2];
#define G2_RDG(dst, J, base, H) dst = rd128<9216 * (H) + 256 * (J)>(base)
#define P2_M(I, J, C) do { acc[I][J] = mfma16(fg[C][J], (u32x4){wl[C][I].x, wl[C][I].y, wh[C][I].x, wh[C][I].y}, acc[I][J]); SB0; } while (0)
#define P2_HALF(C, N, sl_, H) do { \
    const u32 w0 = aW0 + (sl_) * 16384, w1_ = aW1 + (sl_) * 16384, g = aG + (sl_) * 18432; \
    WL(10); P2_M(0, 0, C); wl[N][0] = trd<8192 * (H)>(w0); SB0; \
    WL(9);  P2_M(1, 0, C); wh[N][0] = trd<8192 * (H) + 1024>(w0); SB0; \
    WL(9);  P2_M(0, 1, C); G2_RDG(fg[N][0], 0, g, H); SB0; \
            P2_M(1, 1, C); wl[N][1] = trd<8192 * (H)>(w1_); SB0; \
    WL(10); P2_M(0, 2, C); wh[N][1] = trd<8192 * (H) + 1024>(w1_); SB0; \
            P2_M(1, 2, C); G2_RDG(fg[N][1], 1, g, H); SB0; \
    WL(11); P2_M(0, 3, C); G2_RDG(fg[N][2], 2, g, H); SB0; \
            P2_M(1, 3, C); G2_RDG(fg[N][3], 3, g, H); SB0; \
    WL(12); P2_M(0, 4, C); G2_RDG(fg[N][4], 4, g, H); SB0; \
            P2_M(1, 4, C); G2_RDG(fg[N][5], 5, g, H); SB0; \
    WL(13); P2_M(0, 5, C); G2_RDG(fg[N][6], 6, g, H); SB0; \
            P2_M(1, 5, C); G2_RDG(fg[N][7], 7, g, H); SB0; \
    WL(14); P2_M(0, 6, C); G2_RDG(fg[N][8], 8, g, H); SB0; \
            P2_M(1, 6, C); \
    WL(14); P2_M(0, 7, C); P2_M(1, 7, C); \
    WL(13); P2_M(0, 8, C); P2_M(1, 8, C); } while (0)
    wl[0][0] = trd<0>(aW0); wh[0][0] = trd<1024>(aW0); G2_RDG(fg[0][0], 0, aG, 0); wl[0][1] = trd<0>(aW1); wh[0][1] = trd<1024>(aW1);
    G2_RDG(fg[0][1], 1, aG, 0); G2_RDG(fg[0][2], 2, aG, 0); G2_RDG(fg[0][3], 3, aG, 0); G2_RDG(fg[0][4], 4, aG, 0);
    G2_RDG(fg[0][5], 5, aG, 0); G2_RDG(fg[0][6], 6, aG, 0); G2_RDG(fg[0][7], 7, aG, 0); G2_RDG(fg[0][8], 8, aG, 0);
    const int nk = G.nD + 18;
#pragma unroll 1
    for (int sg = 0; sg < nk; ++sg) {
      if (sg >= G.nD - 1 && sg < nk - 1) BAR();
      SB0;
      const u32 sl = sg & 3, nsl = (sg + 1) & 3;
      P2_HALF(0, 1, sl, 1);
      P2_HALF(1, 0, nsl, 0);
    }
    asm volatile("s_waitcnt lgkmcnt(0)" ::: "memory");
    SB0;
    PROF_STAMP(2, 2); PROF_CLK(2, 2);
    switch (G.kq) {
      case 0: g2_finish<0>(acc, x, b2, ws, rw, out, G.tile, G.s0, G.d2, wave, lane); break;
      case 1: g2_finish<1>(acc, x, b2, ws, rw, out, G.tile, G.s0, G.d2, wave, lane); break;
      case 2: g2_finish<2>(acc, x, b2, ws, rw, out, G.tile, G.s0, G.d2, wave, lane); break;
      default: g2_finish<3>(acc, x, b2, ws, rw, out, G.tile, G.s0, G.d2, wave, lane); break;
    }
    PROF_STAMP(2, 5);
  }
}

extern "C" void kernel_launch(void* const* d_in, const int* in_sizes, int n_in, void* d_out, int out_size, void* d_ws, size_t ws_size, hipStream_t stream) {
  (void)hipFuncSetAttribute((const void*)k2_fused, hipFuncAttributeMaxDynamicSharedMemorySize, MK_LDS);
  k1_rows<<<256, 512, 0, stream>>>((const u16*)d_in[0], (const u16*)d_in[1], (const u16*)d_in[2], (const u16*)d_in[4], (char*)d_ws);
  k2_fused<<<256, 512, MK_LDS, stream>>>((const u16*)d_in[0], (const u16*)d_in[4], (const u16*)d_in[5], (const u16*)d_in[6], (const u16*)d_in[7], (char*)d_ws, (u16*)d_out);
}
```

```cpp
#include <hip/hip_runtime.h>
#include <hip/hip_bf16.h>
#include <stdint.h>

typedef unsigned short u16;
typedef unsigned int u32;
using bf16x8 = __attribute__((ext_vector_type(8))) short;
using f32x4 = __attribute__((ext_vector_type(4))) float;
using f32x16 = __attribute__((ext_vector_type(16))) float;
using f32x2 = __attribute__((ext_vector_type(2))) float;
using u32x4 = __attribute__((ext_vector_type(4))) unsigned;
using u32x2 = __attribute__((ext_vector_type(2))) unsigned;
#define LAS __attribute__((address_space(3)))
#define GAS __attribute__((address_space(1)))
#define SB0 __builtin_amdgcn_sched_barrier(0)

constexpr int S = 2048, D = 576, DF = 289, F = 4608;
constexpr int KX = F + D;

constexpr size_t WS_HB = 0;
constexpr size_t WS_Z = WS_HB + (size_t)S * D * 2;
constexpr size_t WS_WT = WS_Z + (size_t)74 * 32768;
constexpr size_t WS_GB = WS_WT + (size_t)73 * 32768;
constexpr size_t WS_YT = WS_GB + (size_t)F * D * 2;
constexpr size_t WS_P = WS_YT + (size_t)D * S * 2;
constexpr size_t WS_P_BYTES = (size_t)256 * 4 * 18 * 1024;
constexpr size_t WS_FG = WS_P + WS_P_BYTES;
constexpr size_t WS_FY = WS_FG + 1024;
constexpr size_t WS_FP = WS_FY + 256;
constexpr size_t WS_END = WS_FP + 4096;

constexpr double kPi = 3.141592653589793238462643383279502884;
constexpr double c_sin_small(double x) { double t = x, s = x; for (int i = 1; i < 14; ++i) { t *= -x * x / ((2 * i) * (2 * i + 1)); s += t; } return s; }
constexpr double c_cos_small(double x) { double t = 1, s = 1; for (int i = 1; i < 14; ++i) { t *= -x * x / ((2 * i - 1) * (2 * i)); s += t; } return s; }
constexpr double c_cos(int m, int n) {
  m %= n; if (2 * m > n) m = n - m;
  if (4 * m > n) return -((8 * (n / 2 - m) > n) ? c_sin_small(2 * kPi * (m - n / 4) / n) : c_cos_small(2 * kPi * (n / 2 - m) / n));
  return (8 * m > n) ? c_sin_small(2 * kPi * (n / 4 - m) / n) : c_cos_small(2 * kPi * m / n);
}
constexpr double c_sin(int m, int n) { return c_cos((m % n) + n - n / 4, n); }
template <int N> struct TwTab { float v[2 * N]; constexpr TwTab() : v() { for (int m = 0; m < N; ++m) { v[2 * m] = (float)c_cos(m, N); v[2 * m + 1] = (float)(-c_sin(m, N)); } } };
__device__ const TwTab<576> TW576 = TwTab<576>();
#if defined(__HIP_DEVICE_COMPILE__)
constexpr TwTab<2048> TWB = TwTab<2048>();
constexpr unsigned short c_bf16(float f) { unsigned u = __builtin_bit_cast(unsigned, f); return (unsigned short)((u + 0x7fffu + ((u >> 16) & 1u)) >> 16); }
struct FftTab {
  unsigned short t1[2][4][64][8];
  unsigned short t2[2][2][4][64][8];
  unsigned short t4[3][2][64][8];
  unsigned short pad[1024];
  float tw[2][8][64][4];
  constexpr FftTab() : t1(), t2(), t4(), pad(), tw() {
    for (int l = 0; l < 64; ++l) {
      const int r = l & 31, h = l >> 5;
      for (int j = 0; j < 8; ++j) {
        for (int m = 0; m < 4; ++m) {
          const int a = 16 * h + 4 * m + (j >> 1), e = ((a * r) & 31) * 64;
          const float cr = TWB.v[2 * e], ci = TWB.v[2 * e + 1];
          t1[0][m][l][j] = c_bf16((j & 1) ? -ci : cr);
          t1[1][m][l][j] = c_bf16((j & 1) ? cr : ci);
          for (int ub = 0; ub < 2; ++ub) {
            const int b = 16 * m + 8 * (j >> 2) + 4 * h + (j & 3), e2 = (((32 * ub + r) * b) & 63) * 32;
            t2[0][ub][m][l][j] = c_bf16(TWB.v[2 * e2]);
            t2[1][ub][m][l][j] = c_bf16(TWB.v[2 * e2 + 1]);
          }
        }
        for (int ks = 0; ks < 2; ++ks) {
          const int u1 = 16 * ks + 8 * h + j, e = ((u1 * r) & 31) * 64;
          const float sc = 1.0f / (2048.0f * 576.0f);
          t4[0][ks][l][j] = c_bf16(TWB.v[2 * e] * sc);
          t4[1][ks][l][j] = c_bf16(-TWB.v[2 * e + 1] * sc);
          t4[2][ks][l][j] = c_bf16(TWB.v[2 * e + 1] * sc);
        }
      }
      for (int rb = 0; rb < 2; ++rb)
        for (int rho = 0; rho < 16; ++rho) {
          const int b = 32 * rb + (rho & 3) + 8 * (rho >> 2) + 4 * h, e = (b * r) & 2047;
          tw[rb][rho >> 1][l][2 * (rho & 1)] = TWB.v[2 * e];
          tw[rb][rho >> 1][l][2 * (rho & 1) + 1] = TWB.v[2 * e + 1];
        }
    }
  }
};
constexpr TwTab<576> TWC = TwTab<576>();
struct IdftPart {
  unsigned short v[8][576][8];
  constexpr IdftPart(int e) : v() {
    for (int kc = 0; kc < 8; ++kc)
      for (int j = 0; j < 4; ++j) {
        const int k = 32 * e + 4 * kc + j;
        int idx = 0;
        for (int d = 0; d < 576; ++d) {
          v[kc][d][2 * j] = c_bf16(k ? 2.f * TWC.v[2 * idx] : 1.f);
          v[kc][d][2 * j + 1] = c_bf16(k ? 2.f * TWC.v[2 * idx + 1] : ((d & 1) ? -1.f : 1.f));
          idx += k; if (idx >= 576) idx -= 576;
        }
      }
  }
};
constexpr IdftPart IP0(0), IP1(1), IP2(2), IP3(3), IP4(4), IP5(5), IP6(6), IP7(7), IP8(8);
__device__ const __attribute__((aligned(16))) IdftPart IDFT[9] = {IP0, IP1, IP2, IP3, IP4, IP5, IP6, IP7, IP8};
static_assert(sizeof(FftTab) == 49152, "table is moved in whole 1 KB pieces");
__device__ const __attribute__((aligned(16))) FftTab FTAB = FftTab();
#else
__device__ const __attribute__((aligned(16))) char IDFT[663552] = {};
__device__ const __attribute__((aligned(16))) char FTAB[49152] = {};
#endif

struct cf { float x, y; };
__device__ __forceinline__ cf cadd(cf a, cf b) { return {a.x + b.x, a.y + b.y}; }
__device__ __forceinline__ cf csub(cf a, cf b) { return {a.x - b.x, a.y - b.y}; }
__device__ __forceinline__ cf cmul(cf a, cf w) { return {__builtin_fmaf(a.x, w.x, -a.y * w.y), __builtin_fmaf(a.x, w.y, a.y * w.x)}; }
__device__ __forceinline__ cf cmulc(cf a, cf w) { return {__builtin_fmaf(a.x, w.x, a.y * w.y), __builtin_fmaf(a.y, w.x, -a.x * w.y)}; }
__device__ __forceinline__ cf ldtw(const float* t, int m) { f32x2 v = *(const f32x2*)(t + 2 * m); return {v.x, v.y}; }

__device__ __forceinline__ float bflo(u32 v) { return __uint_as_float(v << 16); }
__device__ __forceinline__ float bfhi(u32 v) { return __uint_as_float(v & 0xffff0000u); }
__device__ __forceinline__ u32 pack_bf16(float a, float b) {
  typedef __attribute__((ext_vector_type(2))) __bf16 bf2;
  f32x2 v = {a, b};
  return __builtin_bit_cast(u32, __builtin_convertvector(v, bf2));
}
__device__ __forceinline__ float rbf(float a) { return bflo(pack_bf16(a, a)); }

template <int CTRL> __device__ __forceinline__ float dppf(float v) {
  return __int_as_float(__builtin_amdgcn_update_dpp(0, __float_as_int(v), CTRL, 0xf, 0xf, true));
}
constexpr int DPP_X1 = 0xB1, DPP_X2 = 0x4E, DPP_X7 = 0x141, DPP_X8 = 0x128;
__device__ __forceinline__ float xor16f(float v) { return __int_as_float(__builtin_amdgcn_ds_swizzle(__float_as_int(v), 0x401F)); }
__device__ __forceinline__ float half_sum(float v) {
  v += dppf<DPP_X1>(v); v += dppf<DPP_X2>(v); v += dppf<DPP_X7>(v); v += dppf<DPP_X8>(v); v += xor16f(v); return v;
}

__device__ __forceinline__ u32x4 make_srd(const void* p, u32 bytes) {
  uint64_t a = (uint64_t)p; u32x4 r;
  r.x = __builtin_amdgcn_readfirstlane((u32)a);
  r.y = __builtin_amdgcn_readfirstlane((u32)(a >> 32)) & 0xffffu;
  r.z = bytes; r.w = 0x00020000u; return r;
}
__device__ __forceinline__ void dma16(u32 ldsaddr, u32 voff, u32x4 srd, u32 soff) {
  asm volatile("s_mov_b32 m0, %0\n\ts_nop 0\n\tbuffer_load_dwordx4 %1, %2, %3 offen lds" ::"s"(ldsaddr), "v"(voff), "s"(srd), "s"(soff) : "memory");
}
__device__ __forceinline__ void dma16n(u32 ldsaddr, u32 voff, u32x4 srd, u32 soff) {
  asm volatile("s_mov_b32 m0, %0\n\ts_nop 0\n\tbuffer_load_dwordx4 %1, %2, %3 offen nt lds" ::"s"(ldsaddr), "v"(voff), "s"(srd), "s"(soff) : "memory");
}
template <int OFF> __device__ __forceinline__ u32x2 trd(u32 addr) {
  u32x2 r; asm volatile("ds_read_b64_tr_b16 %0, %1 offset:%2" : "=v"(r) : "v"(addr), "n"(OFF) : "memory"); return r;
}
template <int OFF> __device__ __forceinline__ u32x4 rd128(u32 addr) {
  u32x4 r; asm volatile("ds_read_b128 %0, %1 offset:%2" : "=v"(r) : "v"(addr), "n"(OFF) : "memory"); return r;
}
__device__ __forceinline__ f32x4 mfma16(u32x4 a, u32x4 b, f32x4 c) {
  return __builtin_amdgcn_mfma_f32_16x16x32_bf16(__builtin_bit_cast(bf16x8, a), __builtin_bit_cast(bf16x8, b), c, 0, 0, 0);
}

#define PROF_STAMP_W(kern, w, slot) do { } while (0)
#define PROF_CLK(kern, slot) do { } while (0)
#define PROF_STAMP(kern, slot) do { } while (0)

__device__ __forceinline__ f32x16 mfma32(u32x4 a, u32x4 b, f32x16 c) {
  return __builtin_amdgcn_mfma_f32_32x32x16_bf16(__builtin_bit_cast(bf16x8, a), __builtin_bit_cast(bf16x8, b), c, 0, 0, 0);
}
typedef unsigned long long u64;
typedef GAS u64 gu64;
constexpr int G1_WGS = 216, FFT_WGS = 37;
constexpr int L_W = 0;
constexpr int L_X = 81920;
constexpr int L_ZC = 0, L_HROW = 18432, L_WTS = 28160, L_SYNC = 44544, L_PF1 = 44608, K1_LDS = 44864;
constexpr int L_EX = 3 * 16384;
constexpr int FFT_TAB = 0, FFT_WAVE = 49152, FFT_WAVE_B = 9216, FFT_ZS = 49152;
constexpr int L_PF = 155648;
constexpr int MK_LDS = 156160;
#define PFCODE 1
constexpr u32 K1_CODE = 10240, K2_CODE = 31744;
#define PFMODE 2
#define PFW2N 9
#define PF2 32
#define PF1 8
#define PFS 2
constexpr unsigned SPIN_CAP = 1u << 16;

#define BAR() asm volatile("s_barrier" ::: "memory")
#define WAITV(n) asm volatile("s_waitcnt vmcnt(%0)" ::"n"(n) : "memory")
#define WL(n) do { asm volatile("s_waitcnt lgkmcnt(" #n ")" ::: "memory"); SB0; } while (0)

__device__ __forceinline__ void dma16s(u32 ldsaddr, u32 voff, u32x4 srd, u32 soff) {
  asm volatile("s_mov_b32 m0, %0\n\ts_nop 0\n\tbuffer_load_dwordx4 %1, %2, %3 offen sc1 lds" ::"s"(ldsaddr), "v"(voff), "s"(srd), "s"(soff) : "memory");
}
__device__ __forceinline__ void touch(u32 ldsaddr, u32 voff, u32x4 srd, u32 soff) {
  asm volatile("s_mov_b32 m0, %0\n\ts_nop 0\n\tbuffer_load_dword %1, %2, %3 offen lds" ::"s"(ldsaddr), "v"(voff), "s"(srd), "s"(soff) : "memory");
}
typedef __amdgpu_buffer_rsrc_t rsrc_t;
__device__ __forceinline__ void st16(rsrc_t r, u32 off, u32x4 v) { __builtin_amdgcn_raw_buffer_store_b128(v, r, off, 0, 16); }
__device__ __forceinline__ void st8(rsrc_t r, u32 off, u32x2 v) { __builtin_amdgcn_raw_buffer_store_b64(v, r, off, 0, 16); }
__device__ __forceinline__ u32x4 ld16(rsrc_t r, u32 off) { return __builtin_amdgcn_raw_buffer_load_b128(r, off, 0, 16); }
__device__ __forceinline__ u32 ld4(rsrc_t r, u32 off) { return __builtin_amdgcn_raw_buffer_load_b32(r, off, 0, 16); }
typedef GAS u32 gu32;
__device__ __forceinline__ void set_flag(char* ws, size_t off) { __hip_atomic_store((gu32*)(ws + off), 1u, __ATOMIC_RELAXED, __HIP_MEMORY_SCOPE_AGENT); }
#define K1AUX 0
#define K1E st16
__device__ __forceinline__ void k1st(rsrc_t r, u32 off, u32x4 v) { __builtin_amdgcn_raw_buffer_store_b128(v, r, off, 0, K1AUX); }
__device__ __forceinline__ unsigned lds_arrive(char* smem, int ctr, int lane) {
  unsigned old = 0;
  asm volatile("s_waitcnt lgkmcnt(0)" ::: "memory");
  if (lane == 0) old = __hip_atomic_fetch_add((LAS unsigned*)(smem + L_SYNC + 4 * ctr), 1u, __ATOMIC_RELAXED, __HIP_MEMORY_SCOPE_WORKGROUP);
  return __builtin_amdgcn_readfirstlane(old);
}
__device__ __forceinline__ void lds_wait(char* smem, int ctr, unsigned n) {
  for (unsigned sp = 0; sp < (1u << 22); ++sp) {
    if (__builtin_amdgcn_readfirstlane(__hip_atomic_load((LAS unsigned*)(smem + L_SYNC + 4 * ctr), __ATOMIC_RELAXED, __HIP_MEMORY_SCOPE_WORKGROUP)) >= n) break;
    __builtin_amdgcn_s_sleep(1);
  }
  asm volatile("" ::: "memory");
}
__device__ __forceinline__ void wait_lanes(char* ws, u32 off, u32 want) {
  bool ok = off == ~0u;
  for (unsigned sp = 0; sp < SPIN_CAP; ++sp) {
    if (!ok) ok = __hip_atomic_load((gu32*)(ws + off), __ATOMIC_RELAXED, __HIP_MEMORY_SCOPE_AGENT) >= want;
    if (__all(ok)) break;
    __builtin_amdgcn_s_sleep(1);
  }
}
__device__ __forceinline__ bool look_lanes(char* ws, u32 off, u32 want) {
  bool ok = off == ~0u;
  if (!ok) ok = __hip_atomic_load((gu32*)(ws + off), __ATOMIC_RELAXED, __HIP_MEMORY_SCOPE_AGENT) >= want;
  return __all(ok);
}

__device__ __forceinline__ u32 w1_line(const int xg, const int n) {
  const int t = n / 640, r = n - t * 640, k = r / 10, c = r - k * 10;
  const int col = ((xg * 27) / 6) * 256 + c * 128;
  return (t < 32 && col < F * 2) ? (u32)((8 * (t & 7) + 512 * (t >> 3) + (k >> 3) + 64 * (k & 7)) * (F * 2) + col) : 0xfffffff0u;
}
__device__ __forceinline__ void dft3(cf& a, cf& b, cf& c) {
  const float h = 0.8660254037844386f;
  cf s = cadd(b, c), d = csub(b, c);
  cf m = {__builtin_fmaf(-0.5f, s.x, a.x), __builtin_fmaf(-0.5f, s.y, a.y)};
  a = cadd(a, s);
  b = {__builtin_fmaf(h, d.y, m.x), __builtin_fmaf(-h, d.x, m.y)};
  c = {__builtin_fmaf(-h, d.y, m.x), __builtin_fmaf(h, d.x, m.y)};
}
template <int CTRL> __device__ __forceinline__ cf bfly_dpp(cf v, float sg) {
  return {__builtin_fmaf(dppf<CTRL>(v.x), sg, v.x), __builtin_fmaf(dppf<CTRL>(v.y), sg, v.y)};
}

__device__ __forceinline__ void phase1_rows(char* smem, const u16* __restrict__ x, char* ws, rsrc_t rw, const int bid, const int tid) {
  const int lane = tid & 63, wave = tid >> 6;
  cf* zc = (cf*)(smem + L_ZC);
  char* hrow = smem + L_HROW;
  const int p = lane & 31, r = 2 * wave + (lane >> 5);
  const int l = p ^ ((p & 4) ? 3 : 0);
  const u32* xr = (const u32*)(x + (size_t)((bid & 63) + 64 * (8 * (bid >> 6) + r)) * D);
  u32 raw[9];
#pragma unroll
  for (int j = 0; j < 9; ++j) raw[j] = __builtin_nontemporal_load(xr + l + 32 * j);
  cf tw1[9];
#pragma unroll
  for (int k2 = 1; k2 < 9; ++k2) tw1[k2] = ldtw(TW576.v, 2 * l * k2);
  cf ts4 = ldtw(TW576.v, 18 * (l & 15)), ts3 = ldtw(TW576.v, 36 * (l & 7)), ts2 = ldtw(TW576.v, 72 * (l & 3));
  BAR();
  float e[9], o[9];
#pragma unroll
  for (int j = 0; j < 9; ++j) { e[j] = bflo(raw[j]); o[j] = bfhi(raw[j]); }
#pragma unroll
  for (int pass = 0; pass < 2; ++pass) {
    float s = 0;
#pragma unroll
    for (int j = 0; j < 9; ++j) s += e[j] + o[j];
    const float mean = half_sum(s) * (1.0f / D);
    float q = 0;
#pragma unroll
    for (int j = 0; j < 9; ++j) { const float a = e[j] - mean, b = o[j] - mean; q = __builtin_fmaf(a, a, q); q = __builtin_fmaf(b, b, q); }
    const float var = half_sum(q) * (1.0f / D);
    const float m = rbf(mean);
    const float inv = __builtin_amdgcn_rcpf(rbf(__builtin_sqrtf(rbf(rbf(var) + 1e-5f))));
#pragma unroll
    for (int j = 0; j < 9; ++j) {
      const u32 d = pack_bf16(e[j] - m, o[j] - m);
      const u32 n = pack_bf16(bflo(d) * inv, bfhi(d) * inv);
      if (pass == 0) { e[j] = bflo(n); o[j] = bfhi(n); }
      else *(u32*)(hrow + r * 1216 + (l + 32 * j) * 4) = n;
    }
  }
  PROF_STAMP(0, 1); PROF_CLK(0, 1);
  lds_arrive(smem, 2, lane);
  cf z[9];
#pragma unroll
  for (int j = 0; j < 9; ++j) z[j] = {e[j], o[j]};
  dft3(z[0], z[3], z[6]); dft3(z[1], z[4], z[7]); dft3(z[2], z[5], z[8]);
  {
    const cf w1 = {0.766044443118978f, -0.6427876096865393f}, w2 = {0.17364817766693041f, -0.984807753012208f}, w4 = {-0.9396926207859083f, -0.3420201433256689f};
    z[4] = cmul(z[4], w1); z[7] = cmul(z[7], w2); z[5] = cmul(z[5], w2); z[8] = cmul(z[8], w4);
  }
  dft3(z[0], z[1], z[2]); dft3(z[3], z[4], z[5]); dft3(z[6], z[7], z[8]);
  cf y[9];
#pragma unroll
  for (int c = 0; c < 3; ++c)
#pragma unroll
    for (int d = 0; d < 3; ++d) y[c + 3 * d] = z[3 * c + d];
#pragma unroll
  for (int k2 = 1; k2 < 9; ++k2) y[k2] = cmul(y[k2], tw1[k2]);
  {
    const bool u4 = l & 16, u3 = l & 8, u2 = l & 4, u1 = l & 2, u0 = l & 1;
    const float s4 = u4 ? -1.f : 1.f, s3 = u3 ? -1.f : 1.f, s2 = u2 ? -1.f : 1.f, s1 = u1 ? -1.f : 1.f, s0 = u0 ? 1.f : -1.f;
    ts4 = u4 ? cf{-ts4.x, -ts4.y} : cf{1.f, 0.f};
    ts3 = u3 ? cf{-ts3.x, -ts3.y} : cf{1.f, 0.f};
    ts2 = u2 ? cf{-ts2.x, -ts2.y} : cf{1.f, 0.f};
    const cf ts1 = u1 ? (u0 ? cf{0.f, -1.f} : cf{-1.f, 0.f}) : (u0 ? cf{-1.f, 0.f} : cf{1.f, 0.f});
#pragma unroll
    for (int k = 0; k < 9; ++k) { cf t = {__builtin_fmaf(xor16f(y[k].x), s4, y[k].x), __builtin_fmaf(xor16f(y[k].y), s4, y[k].y)}; y[k] = cmul(t, ts4); }
#pragma unroll
    for (int k = 0; k < 9; ++k) y[k] = cmul(bfly_dpp<DPP_X8>(y[k], s3), ts3);
#pragma unroll
    for (int k = 0; k < 9; ++k) y[k] = cmul(bfly_dpp<DPP_X7>(y[k], s2), ts2);
#pragma unroll
    for (int k = 0; k < 9; ++k) y[k] = cmul(bfly_dpp<DPP_X2>(y[k], s1), ts1);
#pragma unroll
    for (int k = 0; k < 9; ++k) y[k] = bfly_dpp<DPP_X1>(y[k], s0);
  }
  {
    const int k1 = __builtin_bitreverse32((u32)l) >> 27;
#pragma unroll
    for (int k2 = 0; k2 < 9; ++k2) zc[r * 288 + 9 * k1 + k2] = y[k2];
  }
  PROF_STAMP(0, 2); PROF_CLK(0, 2);
  lds_arrive(smem, 3, lane);
}
template <int NT> __device__ __forceinline__ void phase1_unpack(char* smem, rsrc_t rw, const int bid, const int t) {
  constexpr int NR = (290 + NT - 1) / NT;
  const cf* zc = (const cf*)(smem + L_ZC);
  cf wk[NR];
#pragma unroll
  for (int rd = 0; rd < NR; ++rd) wk[rd] = ldtw(TW576.v, (t + NT * rd) >> 1);
  lds_wait(smem, 3, 4);
  const u32 zo = (u32)WS_Z + ((bid & 63) * 4 + (bid >> 6)) * 128;
#pragma unroll
  for (int rd = 0; rd < NR; ++rd) {
    const int it = t + NT * rd, k = it >> 1, rq = it & 1;
    if (k <= 144) {
      u32x4 v0, v1;
#pragma unroll
      for (int e2 = 0; e2 < 4; ++e2) {
        const int row = 4 * rq + e2;
        const cf a = zc[row * 288 + k], b = zc[row * 288 + (k ? 288 - k : 0)];
        const cf E = {0.5f * (a.x + b.x), 0.5f * (a.y - b.y)};
        const cf O = {0.5f * (a.y + b.y), 0.5f * (b.x - a.x)};
        const cf T = cmul(O, wk[rd]);
        v0[e2] = pack_bf16(E.x + T.x, E.y + T.y);
        v1[e2] = pack_bf16(E.x - T.x, T.y - E.y);
      }
      k1st(rw, zo + (k >> 2) * 32768 + (k & 3) * 32 + rq * 16, v0);
      k1st(rw, zo + ((288 - k) >> 2) * 32768 + ((288 - k) & 3) * 32 + rq * 16, v1);
    }
  }
}

__device__ __forceinline__ void phase1_h(char* smem, rsrc_t rw, const int bid, const int L, const int lane) {
  const u32 hb = (u32)(size_t)(smem + L_HROW);
  const int g = lane >> 4, q = (lane & 15) >> 2, pp = lane & 3;
  const u32 a = hb + q * 1216 + (320 * L + 16 * g + 4 * pp) * 2;
  u32x2 t[10];
  asm volatile("ds_read_b64_tr_b16 %0, %10\n\tds_read_b64_tr_b16 %1, %10 offset:4864\n\tds_read_b64_tr_b16 %2, %10 offset:128\n\tds_read_b64_tr_b16 %3, %10 offset:4992\n\t"
               "ds_read_b64_tr_b16 %4, %10 offset:256\n\tds_read_b64_tr_b16 %5, %10 offset:5120\n\tds_read_b64_tr_b16 %6, %10 offset:384\n\tds_read_b64_tr_b16 %7, %10 offset:5248\n\t"
               "ds_read_b64_tr_b16 %8, %10 offset:512\n\tds_read_b64_tr_b16 %9, %10 offset:5376\n\ts_waitcnt lgkmcnt(0)"
               : "=&v"(t[0]), "=&v"(t[1]), "=&v"(t[2]), "=&v"(t[3]), "=&v"(t[4]), "=&v"(t[5]), "=&v"(t[6]), "=&v"(t[7]), "=&v"(t[8]), "=&v"(t[9]) : "v"(a) : "memory");
#pragma unroll
  for (int i = 0; i < 5; ++i)
    if (i < 4 || L == 0) K1E(rw, (u32)WS_HB + (bid * D + 320 * L + 64 * i + lane) * 16, (u32x4){t[2 * i].x, t[2 * i].y, t[2 * i + 1].x, t[2 * i + 1].y});
}
__device__ __forceinline__ void phase1_wt_load(char* stg, const u16* __restrict__ w, const int unit, const int lane) {
  const u32x4 sr = make_srd(w, S * DF * 2);
  const u32 base = (u32)(size_t)stg;
  const u32 row0 = (u32)(2 * (unit & 15) + 128 * (unit >> 4)) * (DF * 2);
#pragma unroll
  for (int e = 0; e < 4; ++e)
#pragma unroll
    for (int j = 0; j < 2; ++j)
      dma16n(base + e * 2048 + j * 1024, lane * 16, sr, row0 + e * (32 * DF * 2) + j * 1024);
}
__device__ __forceinline__ void phase1_wt(char* stg, rsrc_t rw, const int unit, const int H, const int lane) {
  const u16* pr = (const u16*)stg; const u16* pi = (const u16*)(stg + 8192);
  const int i = lane & 1;
  const u32 dst = (u32)WS_WT + unit * 128 + (lane & 7) * 16 + ((lane >> 3) + 40 * H) * 32768;
#pragma unroll
  for (int rd = 0; rd < 5; ++rd) {
    const int k = 160 * H + 32 * rd + (lane >> 1);
    if (k < DF) {
      u32x4 v;
#pragma unroll
      for (int e = 0; e < 4; ++e) { const int idx = e * 1024 + i * DF + k; v[e] = (u32)pr[idx] | ((u32)pi[idx] << 16); }
      K1E(rw, dst + rd * (8 * 32768), v);
    }
  }
}

extern "C" __global__ __launch_bounds__(512) void k1_rows(const u16* __restrict__ x, const u16* __restrict__ wr, const u16* __restrict__ wi, const u16* __restrict__ w1, char* ws) {
  __shared__ __attribute__((aligned(16))) char smem[K1_LDS];
  const int bid = blockIdx.x;
  const int tid = threadIdx.x, lane = tid & 63;
  const int wave = __builtin_amdgcn_readfirstlane(tid >> 6);
  const int L = wave & 1;
  PROF_STAMP(0, 0);
  const rsrc_t rw = __builtin_amdgcn_make_buffer_rsrc(ws, 0, (int)WS_END, 0x00020000);
  if (wave < 4) {
    phase1_rows(smem, x, ws, rw, bid, tid);
  } else if (wave < 6) {
    if (bid < 21 && wave == 4) *(u32*)(ws + WS_FG + bid * 256 + lane * 4) = 0u;
    {
      const int n = ((bid >> 3) * 2 + L) * 64 + lane;
      u64 c1, c2;
      asm volatile("s_getpc_b64 vcc\n\ts_add_u32 vcc_lo, vcc_lo, k1_rows@rel32@lo+4\n\ts_addc_u32 vcc_hi, vcc_hi, k1_rows@rel32@hi+12\n\ts_mov_b64 %0, vcc" : "=s"(c1) :: "vcc", "scc");
      asm volatile("s_getpc_b64 vcc\n\ts_add_u32 vcc_lo, vcc_lo, k2_fused@rel32@lo+4\n\ts_addc_u32 vcc_hi, vcc_hi, k2_fused@rel32@hi+12\n\ts_mov_b64 %0, vcc" : "=s"(c2) :: "vcc", "scc");
      const int w = (bid >> 3) * 2 + L;
      const u32 pad = (u32)(size_t)smem + L_PF1;
      if (w < 2) touch(pad, n * 128, make_srd((const void*)c1, K1_CODE), 0);
      else if (w < 8) touch(pad, (n - 128) * 128, make_srd((const void*)c2, K2_CODE), 0);
      else if (w < 14) touch(pad, (n - 512) * 128, make_srd(&FTAB, 49152), 0);
      else if (w < 40) {
        const int kq = (bid & 7) >> 1;
        touch(pad, (u32)(2 * kq * 73728 + (n - 896) * 128), make_srd(IDFT, (u32)((2 * kq + (kq == 3 ? 3 : 2)) * 73728)), 0);
      }
    }
    BAR();
    if (PF1) {
      const u32x4 srdW1 = make_srd(w1, (u32)((size_t)S * F * 2));
#pragma unroll 1
      for (int n = ((bid >> 3) * 2 + L) * 64; n < PF1 * 640; n += 4096) touch((u32)(size_t)smem + L_PF1, w1_line(bid & 7, n + lane), srdW1, 0);
    }
    lds_wait(smem, 2, 4);
    phase1_h(smem, rw, bid, L, lane);
  } else {
    if (wave == 7 && lane < 16) *(u32*)(smem + L_SYNC + lane * 4) = 0u;
    phase1_wt_load(smem + L_WTS + L * 8192, L ? wi : wr, bid, lane);
    asm volatile("s_waitcnt lgkmcnt(0)\n\ts_barrier" ::: "memory");
    WAITV(0);
    lds_arrive(smem, 6, lane);
    lds_wait(smem, 6, 2);
    phase1_wt(smem + L_WTS, rw, bid, L, lane);
  }
  if (tid < 320) phase1_unpack<320>(smem, rw, bid, tid);
  PROF_STAMP(0, 3);
  PROF_STAMP(0, 4);
}

__device__ __forceinline__ void pack_frags(const f32x16& v, u32x4& f0, u32x4& f1) {
  f0 = (u32x4){pack_bf16(v[0], v[1]), pack_bf16(v[2], v[3]), pack_bf16(v[4], v[5]), pack_bf16(v[6], v[7])};
  f1 = (u32x4){pack_bf16(v[8], v[9]), pack_bf16(v[10], v[11]), pack_bf16(v[12], v[13]), pack_bf16(v[14], v[15])};
}
template <int INV> __device__ __forceinline__ void dft64(const char* tab, const int lane, f32x16 (&re)[2], f32x16 (&im)[2]) {
  u32x4 br[4], bi[4], bn[4];
  pack_frags(re[0], br[0], br[1]); pack_frags(re[1], br[2], br[3]);
  pack_frags(im[0], bi[0], bi[1]); pack_frags(im[1], bi[2], bi[3]);
#pragma unroll
  for (int m = 0; m < 4; ++m) bn[m] = (INV ? br[m] : bi[m]) ^ 0x80008000u;
  const u32x4* t2 = (const u32x4*)(tab + 8192) + lane;
#pragma unroll
  for (int ub = 0; ub < 2; ++ub) {
    f32x16 xr = {}, xi = {};
#pragma unroll
    for (int m = 0; m < 4; ++m) {
      const u32x4 fr = t2[(ub * 4 + m) * 64], fi = t2[(8 + ub * 4 + m) * 64];
      xr = mfma32(fr, br[m], xr);
      xi = mfma32(fr, bi[m], xi);
      xr = mfma32(fi, INV ? bi[m] : bn[m], xr);
      xi = mfma32(fi, INV ? bn[m] : br[m], xi);
    }
    re[ub] = xr; im[ub] = xi;
  }
}
template <int INV> __device__ __forceinline__ void twid(const char* tab, const int lane, f32x16 (&re)[2], f32x16 (&im)[2]) {
  const f32x4* tw = (const f32x4*)(tab + 32768) + lane;
#pragma unroll
  for (int rb = 0; rb < 2; ++rb)
#pragma unroll
    for (int p = 0; p < 8; ++p) {
      const f32x4 w = tw[(rb * 8 + p) * 64];
      cf a = {re[rb][2 * p], im[rb][2 * p]}, b = {re[rb][2 * p + 1], im[rb][2 * p + 1]};
      a = INV ? cmulc(a, cf{w.x, w.y}) : cmul(a, cf{w.x, w.y});
      b = INV ? cmulc(b, cf{w.z, w.w}) : cmul(b, cf{w.z, w.w});
      re[rb][2 * p] = a.x; im[rb][2 * p] = a.y; re[rb][2 * p + 1] = b.x; im[rb][2 * p + 1] = b.y;
    }
}
#define WAVE_LDS_FENCE() do { __builtin_amdgcn_fence(__ATOMIC_RELEASE, "wavefront"); __builtin_amdgcn_wave_barrier(); __builtin_amdgcn_fence(__ATOMIC_ACQUIRE, "wavefront"); } while (0)

__device__ __forceinline__ void fft_role(char* smem, char* ws, rsrc_t rw, const int fb, const int wave, const int lane, const u32x4 srdW1) {
  const int kk = fb * 8 + wave, k = kk < DF ? kk : DF - 1;
  const int r = lane & 31, h = lane >> 5;
  {
    const u32x4 srdWS = make_srd(ws, (u32)WS_END), srdT = make_srd(&FTAB, 49152);
    const u32 ldsb = (u32)(size_t)smem;
#pragma unroll
    for (int j = 0; j < 8; ++j) {
      const int q = lane ^ (2 * j);
      dma16(ldsb + FFT_ZS + (wave * 8 + j) * 1024, (u32)(((q & 1) * 32 + (q >> 1)) * 16), srdWS, (u32)WS_Z + (u32)fb * 65536 + (wave * 8 + j) * 1024);
    }
#pragma unroll
    for (int j = 0; j < 6; ++j) dma16(ldsb + FFT_TAB + (wave * 6 + j) * 1024, lane * 16, srdT, (wave * 6 + j) * 1024);
  }
  WAITV(0); BAR();
  u32x4 wv[2][4];
  {
    const u32 wo = (u32)WS_WT + (k >> 2) * 32768 + (h * 16 + (r >> 1)) * 128 + (k & 3) * 32 + (r & 1) * 16;
#pragma unroll
    for (int ub = 0; ub < 2; ++ub)
#pragma unroll
      for (int g = 0; g < 4; ++g) wv[ub][g] = __builtin_amdgcn_raw_buffer_load_b128(rw, wo + (8 * ub + 2 * g) * 2048, 0, 0);
  }
  if (PFMODE >= 2) {
    const int g8 = fb & 7, ng = g8 < 5 ? 40 : 32;
#pragma unroll 1
    for (int n = PF1 * 640 + ((fb >> 3) * 8 + wave) * 64; n < PF2 * 640; n += ng * 64) touch((u32)(size_t)smem + L_PF, w1_line(g8, n + lane), srdW1, 0);
  }
  u32x4 za[2][4];
  {
    const int kl = k - fb * 8;
    const char* zs = smem + FFT_ZS + (kl >> 2) * 32768 + (r >> 1) * 1024;
#pragma unroll
    for (int rb = 0; rb < 2; ++rb)
#pragma unroll
      for (int m = 0; m < 4; ++m) {
        const int c = 2 * h + (m >> 1), slot = (kl & 3) * 2 + (m & 1);
        za[rb][m] = *(const u32x4*)(zs + rb * 16384 + ((((8 * c + slot) * 2 + (r & 1)) ^ (2 * ((r >> 1) & 7))) * 16));
      }
  }
  asm volatile("s_waitcnt lgkmcnt(0)\n\ts_barrier" : "+v"(za[0][0]), "+v"(za[0][1]), "+v"(za[0][2]), "+v"(za[0][3]), "+v"(za[1][0]), "+v"(za[1][1]), "+v"(za[1][2]), "+v"(za[1][3]) :: "memory");
  const char* tab = smem + FFT_TAB;
  char* img = smem + FFT_WAVE + wave * FFT_WAVE_B;
  f32x16 re[2], im[2];
  {
    const u32x4* t1 = (const u32x4*)tab + lane;
    re[0] = f32x16{}; re[1] = f32x16{}; im[0] = f32x16{}; im[1] = f32x16{};
#pragma unroll
    for (int m = 0; m < 4; ++m) {
      const u32x4 fr = t1[m * 64], fi = t1[(4 + m) * 64];
      re[0] = mfma32(za[0][m], fr, re[0]); im[0] = mfma32(za[0][m], fi, im[0]);
      re[1] = mfma32(za[1][m], fr, re[1]); im[1] = mfma32(za[1][m], fi, im[1]);
    }
  }
  twid<0>(tab, lane, re, im);
  dft64<0>(tab, lane, re, im);
#pragma unroll
  for (int ub = 0; ub < 2; ++ub)
#pragma unroll
    for (int rho = 0; rho < 16; ++rho) {
      const cf p = cmul(cf{re[ub][rho], im[ub][rho]}, cf{bflo(wv[ub][rho >> 2][rho & 3]), bfhi(wv[ub][rho >> 2][rho & 3])});
      re[ub][rho] = p.x; im[ub][rho] = p.y;
    }
  dft64<1>(tab, lane, re, im);
  twid<1>(tab, lane, re, im);
#pragma unroll
  for (int rb = 0; rb < 2; ++rb)
#pragma unroll
    for (int g = 0; g < 4; ++g) {
      char* p = img + r * 136 + (32 * rb + 8 * g + 4 * h) * 2;
      *(u32x2*)p = (u32x2){pack_bf16(re[rb][4 * g], re[rb][4 * g + 1]), pack_bf16(re[rb][4 * g + 2], re[rb][4 * g + 3])};
      *(u32x2*)(p + 4352) = (u32x2){pack_bf16(im[rb][4 * g], im[rb][4 * g + 1]), pack_bf16(im[rb][4 * g + 2], im[rb][4 * g + 3])};
    }
  u32x4 va[2][2][2];
  {
    const int blk = (lane >> 4) & 1, q = (lane & 15) >> 2, p = lane & 3;
    const u32 a = (u32)(size_t)img + (8 * h + q) * 136 + (16 * blk + 4 * p) * 2;
    u32x2 t[16];
    asm volatile("s_waitcnt lgkmcnt(0)\n\t"
                 "ds_read_b64_tr_b16 %0, %16\n\tds_read_b64_tr_b16 %1, %16 offset:544\n\tds_read_b64_tr_b16 %2, %16 offset:2176\n\tds_read_b64_tr_b16 %3, %16 offset:2720\n\t"
                 "ds_read_b64_tr_b16 %4, %16 offset:64\n\tds_read_b64_tr_b16 %5, %16 offset:608\n\tds_read_b64_tr_b16 %6, %16 offset:2240\n\tds_read_b64_tr_b16 %7, %16 offset:2784\n\t"
                 "ds_read_b64_tr_b16 %8, %16 offset:4352\n\tds_read_b64_tr_b16 %9, %16 offset:4896\n\tds_read_b64_tr_b16 %10, %16 offset:6528\n\tds_read_b64_tr_b16 %11, %16 offset:7072\n\t"
                 "ds_read_b64_tr_b16 %12, %16 offset:4416\n\tds_read_b64_tr_b16 %13, %16 offset:4960\n\tds_read_b64_tr_b16 %14, %16 offset:6592\n\tds_read_b64_tr_b16 %15, %16 offset:7136\n\t"
                 "s_waitcnt lgkmcnt(0)"
                 : "=&v"(t[0]), "=&v"(t[1]), "=&v"(t[2]), "=&v"(t[3]), "=&v"(t[4]), "=&v"(t[5]), "=&v"(t[6]), "=&v"(t[7]),
                   "=&v"(t[8]), "=&v"(t[9]), "=&v"(t[10]), "=&v"(t[11]), "=&v"(t[12]), "=&v"(t[13]), "=&v"(t[14]), "=&v"(t[15])
                 : "v"(a) : "memory");
#pragma unroll
    for (int i = 0; i < 8; ++i) va[i >> 2][(i >> 1) & 1][i & 1] = (u32x4){t[2 * i].x, t[2 * i].y, t[2 * i + 1].x, t[2 * i + 1].y};
  }
  {
    const u32x4* t4 = (const u32x4*)(tab + 24576) + lane;
    re[0] = f32x16{}; re[1] = f32x16{}; im[0] = f32x16{}; im[1] = f32x16{};
#pragma unroll
    for (int ks = 0; ks < 2; ++ks) {
      const u32x4 gr = t4[ks * 64], gi = t4[(2 + ks) * 64], gn = t4[(4 + ks) * 64];
#pragma unroll
      for (int rb = 0; rb < 2; ++rb) {
        re[rb] = mfma32(va[0][rb][ks], gr, re[rb]); im[rb] = mfma32(va[0][rb][ks], gi, im[rb]);
        re[rb] = mfma32(va[1][rb][ks], gn, re[rb]); im[rb] = mfma32(va[1][rb][ks], gr, im[rb]);
      }
    }
  }
#pragma unroll
  for (int rb = 0; rb < 2; ++rb)
#pragma unroll
    for (int g = 0; g < 4; ++g) {
      char* p = img + r * 144 + (32 * rb + 8 * g + 4 * h) * 2;
      *(u32x2*)p = (u32x2){pack_bf16(re[rb][4 * g], re[rb][4 * g + 1]), pack_bf16(re[rb][4 * g + 2], re[rb][4 * g + 3])};
      *(u32x2*)(p + 4608) = (u32x2){pack_bf16(im[rb][4 * g], im[rb][4 * g + 1]), pack_bf16(im[rb][4 * g + 2], im[rb][4 * g + 3])};
    }
  WAVE_LDS_FENCE();
  if (kk < DF) {
    const int rowRe = (k == DF - 1) ? 1 : 2 * k;
#pragma unroll
    for (int i = 0; i < 4; ++i) st16(rw, (u32)WS_YT + (rowRe * 256 + 64 * i + lane) * 16, *(const u32x4*)(img + (8 * i + (lane >> 3)) * 144 + (lane & 7) * 16));
    if (k > 0 && k < DF - 1) {
#pragma unroll
      for (int i = 0; i < 4; ++i) st16(rw, (u32)WS_YT + ((2 * k + 1) * 256 + 64 * i + lane) * 16, *(const u32x4*)(img + 4608 + (8 * i + (lane >> 3)) * 144 + (lane & 7) * 16));
    }
  }
}

#define PRIO_C 0
#define PRIO_L 0
__device__ __forceinline__ u32x2 gelu4(const f32x4 a, const u32x2 bb) {
  const float bv[4] = {bflo(bb.x), bfhi(bb.x), bflo(bb.y), bfhi(bb.y)};
  float r[4];
#pragma unroll
  for (int e = 0; e < 4; ++e) {
    const float u = a[e] + bv[e];
    const float z = u * __builtin_fmaf(u * u, -0.044715f * 2.0f * 0.7978845608028654f * 1.4426950408889634f, -2.0f * 0.7978845608028654f * 1.4426950408889634f);
    r[e] = u * __builtin_amdgcn_rcpf(1.0f + __builtin_amdgcn_exp2f(z));
  }
  return (u32x2){pack_bf16(r[0], r[1]), pack_bf16(r[2], r[3])};
}
__device__ __forceinline__ void g1_store2(const f32x4 a0, const f32x4 a1, const u32x2 bb0, const u32x2 bb1, rsrc_t rw, const u32 off) {
  const u32x2 x = gelu4(a0, bb0), y = gelu4(a1, bb1);
  const auto s0 = __builtin_amdgcn_permlane16_swap(x.x, y.x, false, false);
  const auto s1 = __builtin_amdgcn_permlane16_swap(x.y, y.y, false, false);
  st16(rw, off, (u32x4){s0[0], s1[0], s0[1], s1[1]});
}
__device__ __forceinline__ void count_in(char* ws, size_t off, int lane) {
  if (lane == 0) __hip_atomic_fetch_add((gu32*)(ws + off), 1u, __ATOMIC_RELAXED, __HIP_MEMORY_SCOPE_AGENT);
}

constexpr int g2_owner(int i, int j) { return j < 8 ? (j >> 1) : i; }
template <int Q>
__device__ __forceinline__ void g2_finish(f32x4 (&acc)[2][9], const u16* __restrict__ x, const u16* __restrict__ b2, char* ws, rsrc_t rw, u16* __restrict__ out,
                                          const int tile, const int s0, const int d0, const int wave, const int lane) {
  const int srow = s0 + wave * 32 + (lane & 15), dcol = d0 + 4 * (lane >> 4);
  u32x2 xr[2][9]; float bs[2];
#pragma unroll
  for (int i = 0; i < 2; ++i) {
    bs[i] = bflo((u32)b2[srow + 16 * i]);
#pragma unroll
    for (int j = 0; j < 9; ++j)
      if (g2_owner(i, j) == Q) xr[i][j] = *(const u32x2*)(x + (size_t)(srow + 16 * i) * D + dcol + 16 * j);
  }
  const u32 mine = (u32)WS_P + (u32)(((tile * 4 + Q) * 4 + wave) * 10) * 1024;
#pragma unroll
  for (int j = 0; j < 8; ++j)
    if ((j >> 1) != Q)
      st16(rw, mine + j * 1024 + lane * 16, (u32x4){pack_bf16(acc[0][j].x, acc[0][j].y), pack_bf16(acc[0][j].z, acc[0][j].w), pack_bf16(acc[1][j].x, acc[1][j].y), pack_bf16(acc[1][j].z, acc[1][j].w)});
#pragma unroll
  for (int i = 0; i < 2; ++i)
    if (i != Q) st8(rw, mine + (8 + i) * 1024 + lane * 8, (u32x2){pack_bf16(acc[i][8].x, acc[i][8].y), pack_bf16(acc[i][8].z, acc[i][8].w)});
  WAITV(0);
  PROF_STAMP(2, 3);
  if (lane == 0) set_flag(ws, WS_FP + ((tile * 4 + wave) * 4 + Q) * 4);
  wait_lanes(ws, (lane < 4 && lane != Q) ? (u32)WS_FP + ((tile * 4 + wave) * 4 + lane) * 4 : ~0u, 1);
  PROF_STAMP(2, 4);
  u32x4 pv[3][2]; u32x2 pe[3];
#pragma unroll
  for (int o = 0; o < 3; ++o) {
    const int src = o < Q ? o : o + 1;
    const u32 base = (u32)WS_P + (u32)(((tile * 4 + src) * 4 + wave) * 10) * 1024;
    pv[o][0] = ld16(rw, base + (2 * Q) * 1024 + lane * 16);
    pv[o][1] = ld16(rw, base + (2 * Q + 1) * 1024 + lane * 16);
    if (Q < 2) pe[o] = __builtin_amdgcn_raw_buffer_load_b64(rw, base + (8 + Q) * 1024 + lane * 8, 0, 16);
  }
#pragma unroll
  for (int i = 0; i < 2; ++i)
#pragma unroll
    for (int j = 0; j < 9; ++j)
      if (g2_owner(i, j) == Q) {
        f32x4 t = acc[i][j];
#pragma unroll
        for (int o = 0; o < 3; ++o) {
          const u32 lo = j < 8 ? pv[o][j & 1][2 * i] : pe[o].x, hi = j < 8 ? pv[o][j & 1][2 * i + 1] : pe[o].y;
          t += (f32x4){bflo(lo), bfhi(lo), bflo(hi), bfhi(hi)};
        }
        const u32x2 xv = xr[i][j];
        const u32x2 o2 = {pack_bf16(t.x + bs[i] + bflo(xv.x), t.y + bs[i] + bfhi(xv.x)), pack_bf16(t.z + bs[i] + bflo(xv.y), t.w + bs[i] + bfhi(xv.y))};
        *(u32x2*)(out + (size_t)(srow + 16 * i) * D + dcol + 16 * j) = o2;
      }
}

struct Geo {
  int f0, d1, ft, dt;
  int kq, s0, d2, tile, dt2, nD, eb;
};

extern "C" __global__ __launch_bounds__(512, 2) void k2_fused(const u16* __restrict__ x,
                                                   const u16* __restrict__ w1, const u16* __restrict__ b1, const u16* __restrict__ w2,
                                                   const u16* __restrict__ b2, char* ws, u16* __restrict__ out) {
  extern __shared__ __attribute__((aligned(16))) char smem[];
  const int bid = blockIdx.x;
  const int tid = threadIdx.x, lane = tid & 63;
  const int wave = __builtin_amdgcn_readfirstlane(tid >> 6);
  const u32 ldsb = (u32)(size_t)smem;
  PROF_STAMP(1, 0);
  const rsrc_t rw = __builtin_amdgcn_make_buffer_rsrc(ws, 0, (int)WS_END, 0x00020000);
  const bool isG1 = bid < G1_WGS, isFFT = !isG1 && bid < G1_WGS + FFT_WGS;
  Geo G;
  {
    const int xg = bid & 7, idx = bid >> 3;
    const int lin = xg * 27 + idx;
    G.ft = lin / 6; G.dt = lin - G.ft * 6; G.f0 = G.ft * 128; G.d1 = G.dt * 96;
    G.kq = xg >> 1;
    const int st = (xg & 1) * 8 + (idx >> 2);
    G.dt2 = idx & 3; G.s0 = st * 128; G.d2 = G.dt2 * 144; G.tile = st * 4 + G.dt2;
    G.nD = G.kq == 3 ? 3 : 2; G.eb = 2 * G.kq;
  }
  const int L = wave & 1;
  const int kk = lane >> 4;
  const u32 cs0 = ((lane & 15) ^ (kk << 1)) * 16, cs1 = ((lane & 15) ^ ((kk << 1) | 8)) * 16;
  const u32 mW = ldsb + L_W + L * 8192;
  const u32x4 srdW1 = make_srd(w1, (u32)((size_t)S * F * 2));
  const u32 vW0 = (u32)((64 * kk + 4 * L) * (F * 2) + G.f0 * 2) + cs0, vW1 = (u32)((64 * kk + 4 * L) * (F * 2) + G.f0 * 2) + cs1;
#define G1_W(t, sl) do { const u32 _s = (u32)(8 * ((t) & 7) + 512 * ((t) >> 3)) * (F * 2); \
    _Pragma("unroll") for (int j = 0; j < 8; ++j) dma16(mW + (sl) * 16384 + j * 1024, (j & 2) ? vW1 : vW0, srdW1, _s + ((j >> 1) + 256 * (j & 1)) * (F * 2)); } while (0)

  if (isFFT) {
    fft_role(smem, ws, rw, bid - G1_WGS, wave, lane, srdW1);
    PROF_STAMP(1, 2);
    WAITV(0); BAR();
    if (wave == 4 && lane == 0) set_flag(ws, WS_FY + (bid - G1_WGS) * 4);
  }

  if (wave >= 4) {
    const int wd = (wave >> 1) & 1, wf = wave & 1;
    const int fbase = G.f0 + wf * 64 + 32 + 4 * kk;
    const u32 goff = (u32)WS_GB + (u32)((((G.f0 + wf * 64 + 32) >> 3) + (kk & 1) * 2 + (kk >> 1)) * D + G.d1 + wd * 48 + (lane & 15)) * 16;
    const u32 fyoff = (u32)WS_FY + (lane < 4 * G.nD ? 4 * G.eb + lane : 36) * 4;
    if (wave >= 6) {
      if (isG1) {
        const u32x2 bb0 = *(const u32x2*)(b1 + fbase), bb1 = *(const u32x2*)(b1 + fbase + 16);
        if (wave == 7) {
          const int me = (bid >> 3) * 64;
          const u32x4 srdW2 = make_srd(w2, (u32)((size_t)F * S * 2));
          int n1 = PF1 * 640 + me, n2 = me, nxt = (bid >> 3) % PFS;
#pragma unroll 1
          for (int i = 0; i < 35; ++i) {
            BAR();
            if (PFMODE == 1 && i == nxt) {
              nxt += PFS;
              if (n1 < 32 * 640) { touch(ldsb + L_PF, w1_line(bid & 7, n1 + lane), srdW1, 0); n1 += 1728; }
              else if (n2 < 18432) {
                const int n = n2 + lane;
                touch(ldsb + L_PF, (u32)((1152 * G.kq + (n >> 4)) * (S * 2) + (bid & 1) * 2048 + (n & 15) * 128), srdW2, 0);
                n2 += 1728;
              }
            }
          }
        } else {
          const gu32* fy = (const gu32*)(ws + fyoff);
          if (lane == 0) *(volatile LAS u32*)(LAS char*)(smem + L_PF + 256) = 0u;
          u32 seen = 0;
#pragma unroll 1
          for (int i = 0; i < 35; ++i) {
            BAR();
            if (i == 25 || i == 28 || i == 31) {
              if (i != 25 && __all(seen != 0u) && lane == 0) *(volatile LAS u32*)(LAS char*)(smem + L_PF + 256) = 1u;
              asm volatile("s_waitcnt lgkmcnt(0)" ::: "memory");
              if (i != 31) seen = __hip_atomic_load(fy, __ATOMIC_RELAXED, __HIP_MEMORY_SCOPE_AGENT);
            }
          }
        }
        const f32x4* ex = (const f32x4*)(smem + L_EX) + (wave - 4) * 384 + lane;
#pragma unroll
        for (int j = 0; j < 3; ++j) g1_store2(ex[j * 64], ex[(3 + j) * 64], bb0, bb1, rw, goff + j * 256);
        WAITV(0);
        count_in(ws, WS_FG + (G.ft * 6 + G.dt) * 4, lane);
        asm volatile("s_waitcnt lgkmcnt(0)\n\ts_barrier" ::: "memory");
      }
      return;
    }
    __builtin_amdgcn_s_setprio(PRIO_L);
    const u32x4 srdWS = make_srd(ws, (u32)WS_END);
    if (isG1) {
      u32 vH[6];
#pragma unroll
      for (int j = 0; j < 6; ++j) { const int p = j * 64 + lane; const int kc = p / 96, dd = p - kc * 96; vH[j] = (u32)WS_HB + (u32)(((4 * L + kc) * D + G.d1 + dd) * 16); }
      const u32 mH = ldsb + L_X + L * 6144;
#define G1_H(t, sl) do { _Pragma("unroll") for (int j = 0; j < 6; ++j) dma16(mH + (sl) * 12288 + j * 1024, vH[j], srdWS, (u32)(t) * (8 * D * 16)); } while (0)
#pragma unroll
      for (int t = 0; t < 4; ++t) { G1_W(t, t); G1_H(t, t); }
      WAITV(42); BAR();
      WAITV(28); BAR(); G1_W(4, 4); G1_H(4, 4);
      WAITV(28); BAR(); G1_W(5, 0); G1_H(5, 0);
      int sl = 1;
#pragma unroll 1
      for (int t = 2; t < 28; ++t) {
        WAITV(28); BAR();
        G1_W(t + 4, sl); G1_H(t + 4, sl);
        sl = sl == 4 ? 0 : sl + 1;
      }
      WAITV(28); BAR();
      WAITV(14); BAR();
      WAITV(0); BAR();
    }
    const u32x4 srdW2 = make_srd(w2, (u32)((size_t)F * S * 2));
    int ln2 = lane; asm volatile("" : "+v"(ln2));
    const int kk2 = ln2 >> 4;
    const u32 vA0 = (u32)((32 * L + kk2) * (S * 2) + G.s0 * 2) + ((ln2 & 15) ^ (kk2 << 1)) * 16, vA1 = (u32)((32 * L + kk2) * (S * 2) + G.s0 * 2) + ((ln2 & 15) ^ ((kk2 << 1) | 8)) * 16;
    u32 vG[9];
#pragma unroll
    for (int j = 0; j < 9; ++j) { const int p = j * 64 + ln2; const int kc = p / 144, dd = p - kc * 144; vG[j] = (u32)WS_GB + (u32)(((4 * L + kc) * D + G.d2 + dd) * 16); }
    const u32 mG = ldsb + L_X + L * 9216;
    const int nD = G.nD, nk = nD + 18;
#define G2_WY(e, sl) do { _Pragma("unroll") for (int j = 0; j < 8; ++j) dma16s(mW + (sl) * 16384 + j * 1024, (j & 2) ? vA1 : vA0, srdWS, (u32)WS_YT + (u32)(e) * (64 * S * 2) + j * (4 * S * 2)); } while (0)
#define G2_WW(t, sl) do { _Pragma("unroll") for (int j = 0; j < 8; ++j) dma16(mW + (sl) * 16384 + j * 1024, (j & 2) ? vA1 : vA0, srdW2, (u32)(t) * (64 * S * 2) + j * (4 * S * 2)); } while (0)
    const u32x4 srdI = make_srd((const char*)IDFT - WS_GB, (u32)(WS_GB + sizeof(IDFT)));
#define G2_GI(e, sl) do { _Pragma("unroll") for (int j = 0; j < 9; ++j) dma16(mG + (sl) * 18432 + j * 1024, vG[j], srdI, (u32)(e) * (8 * D * 16)); } while (0)
#define G2_G(t, sl) do { _Pragma("unroll") for (int j = 0; j < 9; ++j) dma16s(mG + (sl) * 18432 + j * 1024, vG[j], srdWS, (u32)(t) * (8 * D * 16)); } while (0)
    bool early = false;
    if (isG1) {
      const u32x2 bb0 = *(const u32x2*)(b1 + fbase), bb1 = *(const u32x2*)(b1 + fbase + 16);
      BAR(); BAR();
      early = __builtin_amdgcn_readfirstlane(*(volatile LAS u32*)(LAS char*)(smem + L_PF + 256)) != 0;
      if (early) { G2_WY(G.eb, 0); G2_WY(G.eb + 1, 1); if (nD == 3) G2_WY(G.eb + 2, 2); }
      G2_GI(G.eb, 0); G2_GI(G.eb + 1, 1);
      if (nD == 3) G2_GI(G.eb + 2, 2);
      BAR();
      const f32x4* ex = (const f32x4*)(smem + L_EX) + L * 384 + lane;
#pragma unroll
      for (int j = 0; j < 3; ++j) g1_store2(ex[j * 64], ex[(3 + j) * 64], bb0, bb1, rw, goff + j * 256);
      WAITV(0);
      count_in(ws, WS_FG + (G.ft * 6 + G.dt) * 4, lane);
      asm volatile("s_waitcnt lgkmcnt(0)\n\ts_barrier" ::: "memory");
    } else {
      G2_GI(G.eb, 0); G2_GI(G.eb + 1, 1);
      if (nD == 3) G2_GI(G.eb + 2, 2);
    }
    const u32 fgoff = lane < 18 ? (u32)WS_FG + ((9 * G.kq + (lane >> 1)) * 6 + ((G.dt2 * 3) >> 1) + (lane & 1)) * 4 : ~0u;
    bool gseen = false;
    if (!early) {
      wait_lanes(ws, fyoff, 1);
      G2_WY(G.eb, 0); G2_WY(G.eb + 1, 1);
      if (nD == 3) G2_WY(G.eb + 2, 2);
      gseen = look_lanes(ws, fgoff, 8);
    }
    WAITV(0); BAR();
    PROF_STAMP(2, 0);
    if (!gseen) wait_lanes(ws, fgoff, 8);
    PROF_STAMP(2, 1);
    const int t0 = 18 * G.kq - nD;
    if (nD == 2) {
      G2_WW(t0 + 2, 2); G2_G(t0 + 2, 2);
      G2_WW(t0 + 3, 3); G2_G(t0 + 3, 3);
      WAITV(17); BAR();
      G2_WW(t0 + 4, 0); G2_G(t0 + 4, 0);
    } else {
      G2_WW(t0 + 3, 3); G2_G(t0 + 3, 3);
      WAITV(0); BAR();
      G2_WW(t0 + 4, 0); G2_G(t0 + 4, 0);
      G2_WW(t0 + 5, 1); G2_G(t0 + 5, 1);
    }
#pragma unroll 1
    for (int sg = nD; sg < nk - 3; ++sg) {
      WAITV(17); BAR();
      const int sl = (sg + 3) & 3;
      G2_WW(t0 + sg + 3, sl); G2_G(t0 + sg + 3, sl);
    }
    WAITV(17); BAR();
    WAITV(0); BAR();
    return;
  }

  __builtin_amdgcn_s_setprio(PRIO_C);
  const int lg = lane >> 4, lq = (lane & 15) >> 2, lp = lane & 3;
  const u32 trbase = ldsb + L_W + (u32)((8 * lg + lq) * 256 + (lp & 1) * 8);
  const int swz = (lq << 1) | ((lg & 1) << 3);
  if (isG1) {
    const int wd = (wave >> 1) & 1, wf = wave & 1;
    u32 aA[4];
#pragma unroll
    for (int i = 0; i < 4; ++i) aA[i] = trbase + (u32)(((wf * 8 + 2 * i + (lp >> 1)) ^ swz) * 16);
    const u32 aB = ldsb + L_X + (u32)((lg * 96 + wd * 48 + (lane & 15)) * 16);
    const int fbase = G.f0 + wf * 64 + 4 * lg;
    const u32 goff = (u32)WS_GB + (u32)((((G.f0 + wf * 64) >> 3) + (lg & 1) * 2 + (lg >> 1)) * D + G.d1 + wd * 48 + (lane & 15)) * 16;
    const u32x2 bb0 = *(const u32x2*)(b1 + fbase), bb1 = *(const u32x2*)(b1 + fbase + 16);
    f32x4 acc[4][3];
#pragma unroll
    for (int i = 0; i < 4; ++i)
#pragma unroll
      for (int j = 0; j < 3; ++j) acc[i][j] = (f32x4){0.f, 0.f, 0.f, 0.f};
    u32x4 fb[2][3];
    u32x2 al[2][4], ah[2][4];
    u32 aA3[4];
#pragma unroll
    for (int i = 0; i < 4; ++i) aA3[i] = aA[i] + 3 * 16384;
#define G1_RDB(dst, J, SL, H) dst = rd128<12288 * (SL) + 6144 * (H) + 256 * (J)>(aB)
#define G1_RDA(I, SL, H, T) trd<16384 * ((SL) % 3) + 8192 * (H) + 1024 * (T)>((SL) < 3 ? aA[I] : aA3[I])
#define P1_M(I, J, C) do { acc[I][J] = mfma16((u32x4){al[C][I].x, al[C][I].y, ah[C][I].x, ah[C][I].y}, fb[C][J], acc[I][J]); SB0; } while (0)
#define P1_HALF(C, N, SL, H) do { \
    WL(8); P1_M(0, 0, C); G1_RDB(fb[N][0], 0, SL, H); SB0; \
    WL(8); P1_M(0, 1, C); al[N][0] = G1_RDA(0, SL, H, 0); SB0; \
    WL(8); P1_M(0, 2, C); ah[N][0] = G1_RDA(0, SL, H, 1); SB0; \
    WL(7); P1_M(1, 0, C); G1_RDB(fb[N][1], 1, SL, H); SB0; \
    P1_M(1, 1, C); G1_RDB(fb[N][2], 2, SL, H); SB0; \
    P1_M(1, 2, C); al[N][1] = G1_RDA(1, SL, H, 0); SB0; \
    WL(8); P1_M(2, 0, C); ah[N][1] = G1_RDA(1, SL, H, 1); SB0; \
    P1_M(2, 1, C); al[N][2] = G1_RDA(2, SL, H, 0); SB0; \
    P1_M(2, 2, C); ah[N][2] = G1_RDA(2, SL, H, 1); SB0; \
    WL(9); P1_M(3, 0, C); al[N][3] = G1_RDA(3, SL, H, 0); SB0; \
    P1_M(3, 1, C); ah[N][3] = G1_RDA(3, SL, H, 1); SB0; \
    P1_M(3, 2, C); } while (0)
#define P1_STEP(SL) do { BAR(); SB0; P1_HALF(0, 1, SL, 1); P1_HALF(1, 0, ((SL) + 1) % 5, 0); } while (0)
    asm volatile("s_waitcnt lgkmcnt(0)\n\ts_barrier" ::: "memory");
    PROF_STAMP(1, 1); PROF_CLK(1, 1);
    G1_RDB(fb[0][0], 0, 0, 0); al[0][0] = G1_RDA(0, 0, 0, 0); ah[0][0] = G1_RDA(0, 0, 0, 1); G1_RDB(fb[0][1], 1, 0, 0); G1_RDB(fb[0][2], 2, 0, 0);
    al[0][1] = G1_RDA(1, 0, 0, 0); ah[0][1] = G1_RDA(1, 0, 0, 1); al[0][2] = G1_RDA(2, 0, 0, 0); ah[0][2] = G1_RDA(2, 0, 0, 1);
    al[0][3] = G1_RDA(3, 0, 0, 0); ah[0][3] = G1_RDA(3, 0, 0, 1);
    P1_STEP(0); P1_STEP(1);
#pragma unroll 1
    for (int t = 0; t < 6; ++t) { P1_STEP(2); P1_STEP(3); P1_STEP(4); P1_STEP(0); P1_STEP(1); }
    asm volatile("s_waitcnt lgkmcnt(0)\n\ts_barrier" ::: "memory");
    SB0;
    PROF_STAMP(1, 2); PROF_CLK(1, 2);
    f32x4* ex = (f32x4*)(smem + L_EX);
#pragma unroll
    for (int ii = 0; ii < 2; ++ii)
#pragma unroll
      for (int j = 0; j < 3; ++j) ex[(wave * 6 + ii * 3 + j) * 64 + lane] = acc[2 + ii][j];
    asm volatile("s_waitcnt lgkmcnt(0)\n\ts_barrier" ::: "memory");
#pragma unroll
    for (int j = 0; j < 3; ++j) g1_store2(acc[0][j], acc[1][j], bb0, bb1, rw, goff + j * 256);
    PROF_STAMP(1, 3);
    WAITV(0);
    count_in(ws, WS_FG + (G.ft * 6 + G.dt) * 4, lane);
    BAR();
    if (PFMODE == 2) {
      const u32x4 srdW2 = make_srd(w2, (u32)((size_t)F * S * 2));
#pragma unroll 1
      for (int n = ((bid >> 3) * 4 + wave) * 64 + lane; n < PFW2N * 1024; n += 27 * 256)
        touch(ldsb + L_PF, (u32)((1152 * G.kq + (n >> 4)) * (S * 2) + (bid & 1) * 2048 + (n & 15) * 128), srdW2, 0);
    }
  }
  BAR();
  PROF_STAMP(2, 0); PROF_CLK(2, 0);
  {
    const u32 aW0 = trbase + (u32)(((wave * 4 + (lp >> 1)) ^ swz) * 16), aW1 = trbase + (u32)(((wave * 4 + 2 + (lp >> 1)) ^ swz) * 16);
    const u32 aG = ldsb + L_X + (u32)((lg * 144 + (lane & 15)) * 16);
    f32x4 acc[2][9];
#pragma unroll
    for (int i = 0; i < 2; ++i)
#pragma unroll
      for (int j = 0; j < 9; ++j) acc[i][j] = (f32x4){0.f, 0.f, 0.f, 0.f};
    u32x4 fg[2][9];
    u32x2 wl[2][2], wh[2][2];
    const u32 aG2 = aG + 2 * 18432;
#define G2_RDG(dst, J, SL, H) dst = rd128<18432 * ((SL) & 1) + 9216 * (H) + 256 * (J)>((SL) < 2 ? aG : aG2)
#define G2_RDW(a, SL, H, T) trd<16384 * (SL) + 8192 * (H) + 1024 * (T)>(a)
#define P2_M(I, J, C) do { acc[I][J] = mfma16(fg[C][J], (u32x4){wl[C][I].x, wl[C][I].y, wh[C][I].x, wh[C][I].y}, acc[I][J]); SB0; } while (0)
#define P2_HALF(C, N, SL, H) do { \
    WL(10); P2_M(0, 0, C); wl[N][0] = G2_RDW(aW0, SL, H, 0); SB0; \
    WL(9);  P2_M(1, 0, C); wh[N][0] = G2_RDW(aW0, SL, H, 1); SB0; \
    WL(9);  P2_M(0, 1, C); G2_RDG(fg[N][0], 0, SL, H); SB0; \
            P2_M(1, 1, C); wl[N][1] = G2_RDW(aW1, SL, H, 0); SB0; \
    WL(10); P2_M(0, 2, C); wh[N][1] = G2_RDW(aW1, SL, H, 1); SB0; \
            P2_M(1, 2, C); G2_RDG(fg[N][1], 1, SL, H); SB0; \
    WL(11); P2_M(0, 3, C); G2_RDG(fg[N][2], 2, SL, H); SB0; \
            P2_M(1, 3, C); G2_RDG(fg[N][3], 3, SL, H); SB0; \
    WL(12); P2_M(0, 4, C); G2_RDG(fg[N][4], 4, SL, H); SB0; \
            P2_M(1, 4, C); G2_RDG(fg[N][5], 5, SL, H); SB0; \
    WL(13); P2_M(0, 5, C); G2_RDG(fg[N][6], 6, SL, H); SB0; \
            P2_M(1, 5, C); G2_RDG(fg[N][7], 7, SL, H); SB0; \
    WL(14); P2_M(0, 6, C); G2_RDG(fg[N][8], 8, SL, H); SB0; \
            P2_M(1, 6, C); \
    WL(14); P2_M(0, 7, C); P2_M(1, 7, C); \
    WL(13); P2_M(0, 8, C); P2_M(1, 8, C); } while (0)
#define P2_BODY(SL) do { SB0; P2_HALF(0, 1, SL, 1); P2_HALF(1, 0, ((SL) + 1) & 3, 0); } while (0)
    wl[0][0] = G2_RDW(aW0, 0, 0, 0); wh[0][0] = G2_RDW(aW0, 0, 0, 1); G2_RDG(fg[0][0], 0, 0, 0); wl[0][1] = G2_RDW(aW1, 0, 0, 0); wh[0][1] = G2_RDW(aW1, 0, 0, 1);
    G2_RDG(fg[0][1], 1, 0, 0); G2_RDG(fg[0][2], 2, 0, 0); G2_RDG(fg[0][3], 3, 0, 0); G2_RDG(fg[0][4], 4, 0, 0);
    G2_RDG(fg[0][5], 5, 0, 0); G2_RDG(fg[0][6], 6, 0, 0); G2_RDG(fg[0][7], 7, 0, 0); G2_RDG(fg[0][8], 8, 0, 0);
    P2_BODY(0);
    if (G.nD == 2) BAR();
    P2_BODY(1);
#pragma unroll 1
    for (int it = 0; it < 4; ++it) { BAR(); P2_BODY(2); BAR(); P2_BODY(3); BAR(); P2_BODY(0); BAR(); P2_BODY(1); }
    BAR(); P2_BODY(2);
    if (G.nD == 3) BAR();
    P2_BODY(3);
    if (G.nD == 3) P2_BODY(0);
    asm volatile("s_waitcnt lgkmcnt(0)" ::: "memory");
    SB0;
    PROF_STAMP(2, 2); PROF_CLK(2, 2);
    switch (G.kq) {
      case 0: g2_finish<0>(acc, x, b2, ws, rw, out, G.tile, G.s0, G.d2, wave, lane); break;
      case 1: g2_finish<1>(acc, x, b2, ws, rw, out, G.tile, G.s0, G.d2, wave, lane); break;
      case 2: g2_finish<2>(acc, x, b2, ws, rw, out, G.tile, G.s0, G.d2, wave, lane); break;
      default: g2_finish<3>(acc, x, b2, ws, rw, out, G.tile, G.s0, G.d2, wave, lane); break;
    }
    PROF_STAMP(2, 5);
  }
}

extern "C" void kernel_launch(void* const* d_in, const int* in_sizes, int n_in, void* d_out, int out_size, void* d_ws, size_t ws_size, hipStream_t stream) {
  (void)hipFuncSetAttribute((const void*)k2_fused, hipFuncAttributeMaxDynamicSharedMemorySize, MK_LDS);
  k1_rows<<<256, 512, 0, stream>>>((const u16*)d_in[0], (const u16*)d_in[1], (const u16*)d_in[2], (const u16*)d_in[4], (char*)d_ws);
  k2_fused<<<256, 512, MK_LDS, stream>>>((const u16*)d_in[0], (const u16*)d_in[4], (const u16*)d_in[5], (const u16*)d_in[6], (const u16*)d_in[7], (char*)d_ws, (u16*)d_out);
}
```

```cpp
#include <hip/hip_runtime.h>
#include <hip/hip_bf16.h>
#include <stdint.h>

typedef unsigned short u16;
typedef unsigned int u32;
using bf16x8 = __attribute__((ext_vector_type(8))) short;
using f32x4 = __attribute__((ext_vector_type(4))) float;
using f32x16 = __attribute__((ext_vector_type(16))) float;
using f32x2 = __attribute__((ext_vector_type(2))) float;
using u32x4 = __attribute__((ext_vector_type(4))) unsigned;
using u32x2 = __attribute__((ext_vector_type(2))) unsigned;
#define LAS __attribute__((address_space(3)))
#define GAS __attribute__((address_space(1)))
#define SB0 __builtin_amdgcn_sched_barrier(0)

constexpr int S = 2048, D = 576, DF = 289, F = 4608;
constexpr int KX = F + D;

constexpr size_t WS_HB = 0;
constexpr size_t WS_Z = WS_HB + (size_t)S * D * 2;
constexpr size_t WS_WT = WS_Z + (size_t)74 * 32768;
constexpr size_t WS_GB = WS_WT + (size_t)73 * 32768;
constexpr size_t WS_YT = WS_GB + (size_t)F * D * 2;
constexpr size_t WS_P = WS_YT + (size_t)D * S * 2;
constexpr size_t WS_P_BYTES = (size_t)256 * 4 * 18 * 1024;
constexpr size_t WS_FG = WS_P + WS_P_BYTES;
constexpr size_t WS_FY = WS_FG + 1024;
constexpr size_t WS_FP = WS_FY + 256;
constexpr size_t WS_END = WS_FP + 4096;

constexpr double kPi = 3.141592653589793238462643383279502884;
constexpr double c_sin_small(double x) { double t = x, s = x; for (int i = 1; i < 14; ++i) { t *= -x * x / ((2 * i) * (2 * i + 1)); s += t; } return s; }
constexpr double c_cos_small(double x) { double t = 1, s = 1; for (int i = 1; i < 14; ++i) { t *= -x * x / ((2 * i - 1) * (2 * i)); s += t; } return s; }
constexpr double c_cos(int m, int n) {
  m %= n; if (2 * m > n) m = n - m;
  if (4 * m > n) return -((8 * (n / 2 - m) > n) ? c_sin_small(2 * kPi * (m - n / 4) / n) : c_cos_small(2 * kPi * (n / 2 - m) / n));
  return (8 * m > n) ? c_sin_small(2 * kPi * (n / 4 - m) / n) : c_cos_small(2 * kPi * m / n);
}
constexpr double c_sin(int m, int n) { return c_cos((m % n) + n - n / 4, n); }
template <int N> struct TwTab { float v[2 * N]; constexpr TwTab() : v() { for (int m = 0; m < N; ++m) { v[2 * m] = (float)c_cos(m, N); v[2 * m + 1] = (float)(-c_sin(m, N)); } } };
__device__ const TwTab<576> TW576 = TwTab<576>();
#if defined(__HIP_DEVICE_COMPILE__)
constexpr TwTab<2048> TWB = TwTab<2048>();
constexpr unsigned short c_bf16(float f) { unsigned u = __builtin_bit_cast(unsigned, f); return (unsigned short)((u + 0x7fffu + ((u >> 16) & 1u)) >> 16); }
struct FftTab {
  unsigned short t1[2][4][64][8];
  unsigned short t2[2][2][4][64][8];
  unsigned short t4[3][2][64][8];
  unsigned short pad[1024];
  float tw[2][8][64][4];
  constexpr FftTab() : t1(), t2(), t4(), pad(), tw() {
    for (int l = 0; l < 64; ++l) {
      const int r = l & 31, h = l >> 5;
      for (int j = 0; j < 8; ++j) {
        for (int m = 0; m < 4; ++m) {
          const int a = 16 * h + 4 * m + (j >> 1), e = ((a * r) & 31) * 64;
          const float cr = TWB.v[2 * e], ci = TWB.v[2 * e + 1];
          t1[0][m][l][j] = c_bf16((j & 1) ? -ci : cr);
          t1[1][m][l][j] = c_bf16((j & 1) ? cr : ci);
          for (int ub = 0; ub < 2; ++ub) {
            const int b = 16 * m + 8 * (j >> 2) + 4 * h + (j & 3), e2 = (((32 * ub + r) * b) & 63) * 32;
            t2[0][ub][m][l][j] = c_bf16(TWB.v[2 * e2]);
            t2[1][ub][m][l][j] = c_bf16(TWB.v[2 * e2 + 1]);
          }
        }
        for (int ks = 0; ks < 2; ++ks) {
          const int u1 = 16 * ks + 8 * h + j, e = ((u1 * r) & 31) * 64;
          const float sc = 1.0f / (2048.0f * 576.0f);
          t4[0][ks][l][j] = c_bf16(TWB.v[2 * e] * sc);
          t4[1][ks][l][j] = c_bf16(-TWB.v[2 * e + 1] * sc);
          t4[2][ks][l][j] = c_bf16(TWB.v[2 * e + 1] * sc);
        }
      }
      for (int rb = 0; rb < 2; ++rb)
        for (int rho = 0; rho < 16; ++rho) {
          const int b = 32 * rb + (rho & 3) + 8 * (rho >> 2) + 4 * h, e = (b * r) & 2047;
          tw[rb][rho >> 1][l][2 * (rho & 1)] = TWB.v[2 * e];
          tw[rb][rho >> 1][l][2 * (rho & 1) + 1] = TWB.v[2 * e + 1];
        }
    }
  }
};
constexpr TwTab<576> TWC = TwTab<576>();
struct IdftPart {
  unsigned short v[8][576][8];
  constexpr IdftPart(int e) : v() {
    for (int kc = 0; kc < 8; ++kc)
      for (int j = 0; j < 4; ++j) {
        const int k = 32 * e + 4 * kc + j;
        int idx = 0;
        for (int d = 0; d < 576; ++d) {
          v[kc][d][2 * j] = c_bf16(k ? 2.f * TWC.v[2 * idx] : 1.f);
          v[kc][d][2 * j + 1] = c_bf16(k ? 2.f * TWC.v[2 * idx + 1] : ((d & 1) ? -1.f : 1.f));
          idx += k; if (idx >= 576) idx -= 576;
        }
      }
  }
};
constexpr IdftPart IP0(0), IP1(1), IP2(2), IP3(3), IP4(4), IP5(5), IP6(6), IP7(7), IP8(8);
__device__ const __attribute__((aligned(16))) IdftPart IDFT[9] = {IP0, IP1, IP2, IP3, IP4, IP5, IP6, IP7, IP8};
static_assert(sizeof(FftTab) == 49152, "table is moved in whole 1 KB pieces");
__device__ const __attribute__((aligned(16))) FftTab FTAB = FftTab();
#else
__device__ const __attribute__((aligned(16))) char IDFT[663552] = {};
__device__ const __attribute__((aligned(16))) char FTAB[49152] = {};
#endif

struct cf { float x, y; };
__device__ __forceinline__ cf cadd(cf a, cf b) { return {a.x + b.x, a.y + b.y}; }
__device__ __forceinline__ cf csub(cf a, cf b) { return {a.x - b.x, a.y - b.y}; }
__device__ __forceinline__ cf cmul(cf a, cf w) { return {__builtin_fmaf(a.x, w.x, -a.y * w.y), __builtin_fmaf(a.x, w.y, a.y * w.x)}; }
__device__ __forceinline__ cf cmulc(cf a, cf w) { return {__builtin_fmaf(a.x, w.x, a.y * w.y), __builtin_fmaf(a.y, w.x, -a.x * w.y)}; }
__device__ __forceinline__ cf ldtw(const float* t, int m) { f32x2 v = *(const f32x2*)(t + 2 * m); return {v.x, v.y}; }

__device__ __forceinline__ float bflo(u32 v) { return __uint_as_float(v << 16); }
__device__ __forceinline__ float bfhi(u32 v) { return __uint_as_float(v & 0xffff0000u); }
__device__ __forceinline__ u32 pack_bf16(float a, float b) {
  typedef __attribute__((ext_vector_type(2))) __bf16 bf2;
  f32x2 v = {a, b};
  return __builtin_bit_cast(u32, __builtin_convertvector(v, bf2));
}
__device__ __forceinline__ float rbf(float a) { return bflo(pack_bf16(a, a)); }

template <int CTRL> __device__ __forceinline__ float dppf(float v) {
  return __int_as_float(__builtin_amdgcn_update_dpp(0, __float_as_int(v), CTRL, 0xf, 0xf, true));
}
constexpr int DPP_X1 = 0xB1, DPP_X2 = 0x4E, DPP_X7 = 0x141, DPP_X8 = 0x128;
__device__ __forceinline__ float xor16f(float v) { return __int_as_float(__builtin_amdgcn_ds_swizzle(__float_as_int(v), 0x401F)); }
__device__ __forceinline__ float half_sum(float v) {
  v += dppf<DPP_X1>(v); v += dppf<DPP_X2>(v); v += dppf<DPP_X7>(v); v += dppf<DPP_X8>(v); v += xor16f(v); return v;
}

__device__ __forceinline__ u32x4 make_srd(const void* p, u32 bytes) {
  uint64_t a = (uint64_t)p; u32x4 r;
  r.x = __builtin_amdgcn_readfirstlane((u32)a);
  r.y = __builtin_amdgcn_readfirstlane((u32)(a >> 32)) & 0xffffu;
  r.z = bytes; r.w = 0x00020000u; return r;
}
__device__ __forceinline__ void dma16(u32 ldsaddr, u32 voff, u32x4 srd, u32 soff) {
  asm volatile("s_mov_b32 m0, %0\n\ts_nop 0\n\tbuffer_load_dwordx4 %1, %2, %3 offen lds" ::"s"(ldsaddr), "v"(voff), "s"(srd), "s"(soff) : "memory");
}
__device__ __forceinline__ void dma16n(u32 ldsaddr, u32 voff, u32x4 srd, u32 soff) {
  asm volatile("s_mov_b32 m0, %0\n\ts_nop 0\n\tbuffer_load_dwordx4 %1, %2, %3 offen nt lds" ::"s"(ldsaddr), "v"(voff), "s"(srd), "s"(soff) : "memory");
}
template <int OFF> __device__ __forceinline__ u32x2 trd(u32 addr) {
  u32x2 r; asm volatile("ds_read_b64_tr_b16 %0, %1 offset:%2" : "=v"(r) : "v"(addr), "n"(OFF) : "memory"); return r;
}
template <int OFF> __device__ __forceinline__ u32x4 rd128(u32 addr) {
  u32x4 r; asm volatile("ds_read_b128 %0, %1 offset:%2" : "=v"(r) : "v"(addr), "n"(OFF) : "memory"); return r;
}
__device__ __forceinline__ f32x4 mfma16(u32x4 a, u32x4 b, f32x4 c) {
  return __builtin_amdgcn_mfma_f32_16x16x32_bf16(__builtin_bit_cast(bf16x8, a), __builtin_bit_cast(bf16x8, b), c, 0, 0, 0);
}

__device__ __forceinline__ f32x16 mfma32(u32x4 a, u32x4 b, f32x16 c) {
  return __builtin_amdgcn_mfma_f32_32x32x16_bf16(__builtin_bit_cast(bf16x8, a), __builtin_bit_cast(bf16x8, b), c, 0, 0, 0);
}
typedef unsigned long long u64;
typedef GAS u64 gu64;
constexpr int G1_WGS = 216, FFT_WGS = 37;
constexpr int L_W = 0;
constexpr int L_X = 81920;
constexpr int L_ZC = 0, L_HROW = 18432, L_WTS = 28160, L_SYNC = 44544, L_PF1 = 44608, K1_LDS = 44864;
constexpr int L_EX = 3 * 16384;
constexpr int FFT_TAB = 0, FFT_WAVE = 49152, FFT_WAVE_B = 9216, FFT_ZS = 49152;
constexpr int L_PF = 155648;
constexpr int MK_LDS = 156160;
constexpr int PF1 = 8, PFW2N = 9;
constexpr u32 K1_CODE = 9728, K2_CODE = 38912;
constexpr unsigned SPIN_CAP = 1u << 16;

#define BAR() asm volatile("s_barrier" ::: "memory")
#define WAITV(n) asm volatile("s_waitcnt vmcnt(%0)" ::"n"(n) : "memory")
#define WL(n) do { asm volatile("s_waitcnt lgkmcnt(" #n ")" ::: "memory"); SB0; } while (0)

__device__ __forceinline__ void dma16s(u32 ldsaddr, u32 voff, u32x4 srd, u32 soff) {
  asm volatile("s_mov_b32 m0, %0\n\ts_nop 0\n\tbuffer_load_dwordx4 %1, %2, %3 offen sc1 lds" ::"s"(ldsaddr), "v"(voff), "s"(srd), "s"(soff) : "memory");
}
__device__ __forceinline__ void touch(u32 ldsaddr, u32 voff, u32x4 srd, u32 soff) {
  asm volatile("s_mov_b32 m0, %0\n\ts_nop 0\n\tbuffer_load_dword %1, %2, %3 offen lds" ::"s"(ldsaddr), "v"(voff), "s"(srd), "s"(soff) : "memory");
}
typedef __amdgpu_buffer_rsrc_t rsrc_t;
__device__ __forceinline__ void st16(rsrc_t r, u32 off, u32x4 v) { __builtin_amdgcn_raw_buffer_store_b128(v, r, off, 0, 16); }
__device__ __forceinline__ void st8(rsrc_t r, u32 off, u32x2 v) { __builtin_amdgcn_raw_buffer_store_b64(v, r, off, 0, 16); }
__device__ __forceinline__ u32x4 ld16(rsrc_t r, u32 off) { return __builtin_amdgcn_raw_buffer_load_b128(r, off, 0, 16); }
__device__ __forceinline__ u32 ld4(rsrc_t r, u32 off) { return __builtin_amdgcn_raw_buffer_load_b32(r, off, 0, 16); }
typedef GAS u32 gu32;
__device__ __forceinline__ void set_flag(char* ws, size_t off) { __hip_atomic_store((gu32*)(ws + off), 1u, __ATOMIC_RELAXED, __HIP_MEMORY_SCOPE_AGENT); }
__device__ __forceinline__ void k1st(rsrc_t r, u32 off, u32x4 v) { __builtin_amdgcn_raw_buffer_store_b128(v, r, off, 0, 0); }
__device__ __forceinline__ unsigned lds_arrive(char* smem, int ctr, int lane) {
  unsigned old = 0;
  asm volatile("s_waitcnt lgkmcnt(0)" ::: "memory");
  if (lane == 0) old = __hip_atomic_fetch_add((LAS unsigned*)(smem + L_SYNC + 4 * ctr), 1u, __ATOMIC_RELAXED, __HIP_MEMORY_SCOPE_WORKGROUP);
  return __builtin_amdgcn_readfirstlane(old);
}
__device__ __forceinline__ void lds_wait(char* smem, int ctr, unsigned n) {
  for (unsigned sp = 0; sp < (1u << 22); ++sp) {
    if (__builtin_amdgcn_readfirstlane(__hip_atomic_load((LAS unsigned*)(smem + L_SYNC + 4 * ctr), __ATOMIC_RELAXED, __HIP_MEMORY_SCOPE_WORKGROUP)) >= n) break;
    __builtin_amdgcn_s_sleep(1);
  }
  asm volatile("" ::: "memory");
}
__device__ __forceinline__ void wait_lanes(char* ws, u32 off, u32 want) {
  bool ok = off == ~0u;
  for (unsigned sp = 0; sp < SPIN_CAP; ++sp) {
    if (!ok) ok = __hip_atomic_load((gu32*)(ws + off), __ATOMIC_RELAXED, __HIP_MEMORY_SCOPE_AGENT) >= want;
    if (__all(ok)) break;
    __builtin_amdgcn_s_sleep(1);
  }
}
__device__ __forceinline__ bool look_lanes(char* ws, u32 off, u32 want) {
  bool ok = off == ~0u;
  if (!ok) ok = __hip_atomic_load((gu32*)(ws + off), __ATOMIC_RELAXED, __HIP_MEMORY_SCOPE_AGENT) >= want;
  return __all(ok);
}

__device__ __forceinline__ u32 w1_line(const int xg, const int n) {
  const int t = n / 640, r = n - t * 640, k = r / 10, c = r - k * 10;
  const int col = ((xg * 27) / 6) * 256 + c * 128;
  return (t < 32 && col < F * 2) ? (u32)((8 * (t & 7) + 512 * (t >> 3) + (k >> 3) + 64 * (k & 7)) * (F * 2) + col) : 0xfffffff0u;
}
__device__ __forceinline__ void dft3(cf& a, cf& b, cf& c) {
  const float h = 0.8660254037844386f;
  cf s = cadd(b, c), d = csub(b, c);
  cf m = {__builtin_fmaf(-0.5f, s.x, a.x), __builtin_fmaf(-0.5f, s.y, a.y)};
  a = cadd(a, s);
  b = {__builtin_fmaf(h, d.y, m.x), __builtin_fmaf(-h, d.x, m.y)};
  c = {__builtin_fmaf(-h, d.y, m.x), __builtin_fmaf(h, d.x, m.y)};
}
template <int CTRL> __device__ __forceinline__ cf bfly_dpp(cf v, float sg) {
  return {__builtin_fmaf(dppf<CTRL>(v.x), sg, v.x), __builtin_fmaf(dppf<CTRL>(v.y), sg, v.y)};
}

__device__ __forceinline__ void phase1_rows(char* smem, const u16* __restrict__ x, char* ws, rsrc_t rw, const int bid, const int tid) {
  const int lane = tid & 63, wave = tid >> 6;
  cf* zc = (cf*)(smem + L_ZC);
  char* hrow = smem + L_HROW;
  const int p = lane & 31, r = 2 * wave + (lane >> 5);
  const int l = p ^ ((p & 4) ? 3 : 0);
  const u32* xr = (const u32*)(x + (size_t)((bid & 63) + 64 * (8 * (bid >> 6) + r)) * D);
  u32 raw[9];
#pragma unroll
  for (int j = 0; j < 9; ++j) raw[j] = __builtin_nontemporal_load(xr + l + 32 * j);
  cf tw1[9];
#pragma unroll
  for (int k2 = 1; k2 < 9; ++k2) tw1[k2] = ldtw(TW576.v, 2 * l * k2);
  cf ts4 = ldtw(TW576.v, 18 * (l & 15)), ts3 = ldtw(TW576.v, 36 * (l & 7)), ts2 = ldtw(TW576.v, 72 * (l & 3));
  BAR();
  float e[9], o[9];
#pragma unroll
  for (int j = 0; j < 9; ++j) { e[j] = bflo(raw[j]); o[j] = bfhi(raw[j]); }
#pragma unroll
  for (int pass = 0; pass < 2; ++pass) {
    float s = 0;
#pragma unroll
    for (int j = 0; j < 9; ++j) s += e[j] + o[j];
    const float mean = half_sum(s) * (1.0f / D);
    float q = 0;
#pragma unroll
    for (int j = 0; j < 9; ++j) { const float a = e[j] - mean, b = o[j] - mean; q = __builtin_fmaf(a, a, q); q = __builtin_fmaf(b, b, q); }
    const float var = half_sum(q) * (1.0f / D);
    const float m = rbf(mean);
    const float inv = __builtin_amdgcn_rcpf(rbf(__builtin_sqrtf(rbf(rbf(var) + 1e-5f))));
#pragma unroll
    for (int j = 0; j < 9; ++j) {
      const u32 d = pack_bf16(e[j] - m, o[j] - m);
      const u32 n = pack_bf16(bflo(d) * inv, bfhi(d) * inv);
      if (pass == 0) { e[j] = bflo(n); o[j] = bfhi(n); }
      else *(u32*)(hrow + r * 1216 + (l + 32 * j) * 4) = n;
    }
  }
  lds_arrive(smem, 2, lane);
  cf z[9];
#pragma unroll
  for (int j = 0; j < 9; ++j) z[j] = {e[j], o[j]};
  dft3(z[0], z[3], z[6]); dft3(z[1], z[4], z[7]); dft3(z[2], z[5], z[8]);
  {
    const cf w1 = {0.766044443118978f, -0.6427876096865393f}, w2 = {0.17364817766693041f, -0.984807753012208f}, w4 = {-0.9396926207859083f, -0.3420201433256689f};
    z[4] = cmul(z[4], w1); z[7] = cmul(z[7], w2); z[5] = cmul(z[5], w2); z[8] = cmul(z[8], w4);
  }
  dft3(z[0], z[1], z[2]); dft3(z[3], z[4], z[5]); dft3(z[6], z[7], z[8]);
  cf y[9];
#pragma unroll
  for (int c = 0; c < 3; ++c)
#pragma unroll
    for (int d = 0; d < 3; ++d) y[c + 3 * d] = z[3 * c + d];
#pragma unroll
  for (int k2 = 1; k2 < 9; ++k2) y[k2] = cmul(y[k2], tw1[k2]);
  {
    const bool u4 = l & 16, u3 = l & 8, u2 = l & 4, u1 = l & 2, u0 = l & 1;
    const float s4 = u4 ? -1.f : 1.f, s3 = u3 ? -1.f : 1.f, s2 = u2 ? -1.f : 1.f, s1 = u1 ? -1.f : 1.f, s0 = u0 ? 1.f : -1.f;
    ts4 = u4 ? cf{-ts4.x, -ts4.y} : cf{1.f, 0.f};
    ts3 = u3 ? cf{-ts3.x, -ts3.y} : cf{1.f, 0.f};
    ts2 = u2 ? cf{-ts2.x, -ts2.y} : cf{1.f, 0.f};
    const cf ts1 = u1 ? (u0 ? cf{0.f, -1.f} : cf{-1.f, 0.f}) : (u0 ? cf{-1.f, 0.f} : cf{1.f, 0.f});
#pragma unroll
    for (int k = 0; k < 9; ++k) { cf t = {__builtin_fmaf(xor16f(y[k].x), s4, y[k].x), __builtin_fmaf(xor16f(y[k].y), s4, y[k].y)}; y[k] = cmul(t, ts4); }
#define BFLY9(CTRL, SG) asm volatile("s_nop 1\n\t" \
      "v_fmac_f32_dpp %0, %0, %18 " CTRL "\n\tv_fmac_f32_dpp %1, %1, %18 " CTRL "\n\tv_fmac_f32_dpp %2, %2, %18 " CTRL "\n\t" \
      "v_fmac_f32_dpp %3, %3, %18 " CTRL "\n\tv_fmac_f32_dpp %4, %4, %18 " CTRL "\n\tv_fmac_f32_dpp %5, %5, %18 " CTRL "\n\t" \
      "v_fmac_f32_dpp %6, %6, %18 " CTRL "\n\tv_fmac_f32_dpp %7, %7, %18 " CTRL "\n\tv_fmac_f32_dpp %8, %8, %18 " CTRL "\n\t" \
      "v_fmac_f32_dpp %9, %9, %18 " CTRL "\n\tv_fmac_f32_dpp %10, %10, %18 " CTRL "\n\tv_fmac_f32_dpp %11, %11, %18 " CTRL "\n\t" \
      "v_fmac_f32_dpp %12, %12, %18 " CTRL "\n\tv_fmac_f32_dpp %13, %13, %18 " CTRL "\n\tv_fmac_f32_dpp %14, %14, %18 " CTRL "\n\t" \
      "v_fmac_f32_dpp %15, %15, %18 " CTRL "\n\tv_fmac_f32_dpp %16, %16, %18 " CTRL "\n\tv_fmac_f32_dpp %17, %17, %18 " CTRL \
      : "+v"(y[0].x), "+v"(y[0].y), "+v"(y[1].x), "+v"(y[1].y), "+v"(y[2].x), "+v"(y[2].y), "+v"(y[3].x), "+v"(y[3].y), "+v"(y[4].x), "+v"(y[4].y), \
        "+v"(y[5].x), "+v"(y[5].y), "+v"(y[6].x), "+v"(y[6].y), "+v"(y[7].x), "+v"(y[7].y), "+v"(y[8].x), "+v"(y[8].y) : "v"(SG))
    BFLY9("row_ror:8 row_mask:0xf bank_mask:0xf", s3);
#pragma unroll
    for (int k = 0; k < 9; ++k) y[k] = cmul(y[k], ts3);
    BFLY9("row_half_mirror row_mask:0xf bank_mask:0xf", s2);
#pragma unroll
    for (int k = 0; k < 9; ++k) y[k] = cmul(y[k], ts2);
    BFLY9("quad_perm:[2,3,0,1] row_mask:0xf bank_mask:0xf", s1);
#pragma unroll
    for (int k = 0; k < 9; ++k) y[k] = cmul(y[k], ts1);
    BFLY9("quad_perm:[1,0,3,2] row_mask:0xf bank_mask:0xf", s0);
  }
  {
    const int k1 = __builtin_bitreverse32((u32)l) >> 27;
#pragma unroll
    for (int k2 = 0; k2 < 9; ++k2) zc[r * 288 + 9 * k1 + k2] = y[k2];
  }
  lds_arrive(smem, 3, lane);
}
template <int NT> __device__ __forceinline__ void phase1_unpack(char* smem, rsrc_t rw, const int bid, const int t) {
  constexpr int NR = (290 + NT - 1) / NT;
  const cf* zc = (const cf*)(smem + L_ZC);
  cf wk[NR];
#pragma unroll
  for (int rd = 0; rd < NR; ++rd) wk[rd] = ldtw(TW576.v, (t + NT * rd) >> 1);
  lds_wait(smem, 3, 4);
  const u32 zo = (u32)WS_Z + ((bid & 63) * 4 + (bid >> 6)) * 128;
#pragma unroll
  for (int rd = 0; rd < NR; ++rd) {
    const int it = t + NT * rd, k = it >> 1, rq = it & 1;
    if (k <= 144) {
      u32x4 v0, v1;
#pragma unroll
      for (int e2 = 0; e2 < 4; ++e2) {
        const int row = 4 * rq + e2;
        const cf a = zc[row * 288 + k], b = zc[row * 288 + (k ? 288 - k : 0)];
        const cf E = {0.5f * (a.x + b.x), 0.5f * (a.y - b.y)};
        const cf O = {0.5f * (a.y + b.y), 0.5f * (b.x - a.x)};
        const cf T = cmul(O, wk[rd]);
        v0[e2] = pack_bf16(E.x + T.x, E.y + T.y);
        v1[e2] = pack_bf16(E.x - T.x, T.y - E.y);
      }
      k1st(rw, zo + (k >> 2) * 32768 + (k & 3) * 32 + rq * 16, v0);
      k1st(rw, zo + ((288 - k) >> 2) * 32768 + ((288 - k) & 3) * 32 + rq * 16, v1);
    }
  }
}

__device__ __forceinline__ void phase1_h(char* smem, rsrc_t rw, const int bid, const int L, const int lane) {
  const u32 hb = (u32)(size_t)(smem + L_HROW);
  const int g = lane >> 4, q = (lane & 15) >> 2, pp = lane & 3;
  const u32 a = hb + q * 1216 + (320 * L + 16 * g + 4 * pp) * 2;
  u32x2 t[10];
  asm volatile("ds_read_b64_tr_b16 %0, %10\n\tds_read_b64_tr_b16 %1, %10 offset:4864\n\tds_read_b64_tr_b16 %2, %10 offset:128\n\tds_read_b64_tr_b16 %3, %10 offset:4992\n\t"
               "ds_read_b64_tr_b16 %4, %10 offset:256\n\tds_read_b64_tr_b16 %5, %10 offset:5120\n\tds_read_b64_tr_b16 %6, %10 offset:384\n\tds_read_b64_tr_b16 %7, %10 offset:5248\n\t"
               "ds_read_b64_tr_b16 %8, %10 offset:512\n\tds_read_b64_tr_b16 %9, %10 offset:5376\n\ts_waitcnt lgkmcnt(0)"
               : "=&v"(t[0]), "=&v"(t[1]), "=&v"(t[2]), "=&v"(t[3]), "=&v"(t[4]), "=&v"(t[5]), "=&v"(t[6]), "=&v"(t[7]), "=&v"(t[8]), "=&v"(t[9]) : "v"(a) : "memory");
#pragma unroll
  for (int i = 0; i < 5; ++i)
    if (i < 4 || L == 0) st16(rw, (u32)WS_HB + (bid * D + 320 * L + 64 * i + lane) * 16, (u32x4){t[2 * i].x, t[2 * i].y, t[2 * i + 1].x, t[2 * i + 1].y});
}
__device__ __forceinline__ void phase1_wt_load(char* stg, const u16* __restrict__ w, const int unit, const int lane) {
  const u32x4 sr = make_srd(w, S * DF * 2);
  const u32 base = (u32)(size_t)stg;
  const u32 row0 = (u32)(2 * (unit & 15) + 128 * (unit >> 4)) * (DF * 2);
#pragma unroll
  for (int e = 0; e < 4; ++e)
#pragma unroll
    for (int j = 0; j < 2; ++j)
      dma16n(base + e * 2048 + j * 1024, lane * 16, sr, row0 + e * (32 * DF * 2) + j * 1024);
}
__device__ __forceinline__ void phase1_wt(char* stg, rsrc_t rw, const int unit, const int H, const int lane) {
  const u16* pr = (const u16*)stg; const u16* pi = (const u16*)(stg + 8192);
  const int i = lane & 1;
  const u32 dst = (u32)WS_WT + unit * 128 + (lane & 7) * 16 + ((lane >> 3) + 40 * H) * 32768;
#pragma unroll
  for (int rd = 0; rd < 5; ++rd) {
    const int k = 160 * H + 32 * rd + (lane >> 1);
    if (k < DF) {
      u32x4 v;
#pragma unroll
      for (int e = 0; e < 4; ++e) { const int idx = e * 1024 + i * DF + k; v[e] = (u32)pr[idx] | ((u32)pi[idx] << 16); }
      st16(rw, dst + rd * (8 * 32768), v);
    }
  }
}

extern "C" __global__ __launch_bounds__(512) void k1_rows(const u16* __restrict__ x, const u16* __restrict__ wr, const u16* __restrict__ wi, const u16* __restrict__ w1, char* ws) {
  __shared__ __attribute__((aligned(16))) char smem[K1_LDS];
  const int bid = blockIdx.x;
  const int tid = threadIdx.x, lane = tid & 63;
  const int wave = __builtin_amdgcn_readfirstlane(tid >> 6);
  const int L = wave & 1;
  const rsrc_t rw = __builtin_amdgcn_make_buffer_rsrc(ws, 0, (int)WS_END, 0x00020000);
  if (wave < 4) {
    phase1_rows(smem, x, ws, rw, bid, tid);
  } else if (wave < 6) {
    if (bid < 21 && wave == 4) *(u32*)(ws + WS_FG + bid * 256 + lane * 4) = 0u;
    {
      const int n = ((bid >> 3) * 2 + L) * 64 + lane;
      u64 c1, c2;
      asm volatile("s_getpc_b64 vcc\n\ts_add_u32 vcc_lo, vcc_lo, k1_rows@rel32@lo+4\n\ts_addc_u32 vcc_hi, vcc_hi, k1_rows@rel32@hi+12\n\ts_mov_b64 %0, vcc" : "=s"(c1) :: "vcc", "scc");
      asm volatile("s_getpc_b64 vcc\n\ts_add_u32 vcc_lo, vcc_lo, k2_fused@rel32@lo+4\n\ts_addc_u32 vcc_hi, vcc_hi, k2_fused@rel32@hi+12\n\ts_mov_b64 %0, vcc" : "=s"(c2) :: "vcc", "scc");
      const int w = (bid >> 3) * 2 + L;
      const u32 pad = (u32)(size_t)smem + L_PF1;
      if (w < 2) touch(pad, n * 128, make_srd((const void*)c1, K1_CODE), 0);
      else if (w < 8) touch(pad, (n - 128) * 128, make_srd((const void*)c2, K2_CODE), 0);
      else if (w < 14) touch(pad, (n - 512) * 128, make_srd(&FTAB, 49152), 0);
      else if (w < 40) {
        const int kq = (bid & 7) >> 1;
        touch(pad, (u32)(2 * kq * 73728 + (n - 896) * 128), make_srd(IDFT, (u32)((2 * kq + (kq == 3 ? 3 : 2)) * 73728)), 0);
      }
    }
    BAR();
    {
      const u32x4 srdW1 = make_srd(w1, (u32)((size_t)S * F * 2));
#pragma unroll 1
      for (int n = ((bid >> 3) * 2 + L) * 64; n < PF1 * 640; n += 4096) touch((u32)(size_t)smem + L_PF1, w1_line(bid & 7, n + lane), srdW1, 0);
    }
    lds_wait(smem, 2, 4);
    phase1_h(smem, rw, bid, L, lane);
  } else {
    if (wave == 7 && lane < 16) *(u32*)(smem + L_SYNC + lane * 4) = 0u;
    phase1_wt_load(smem + L_WTS + L * 8192, L ? wi : wr, bid, lane);
    asm volatile("s_waitcnt lgkmcnt(0)\n\ts_barrier" ::: "memory");
    WAITV(0);
    lds_arrive(smem, 6, lane);
    lds_wait(smem, 6, 2);
    phase1_wt(smem + L_WTS, rw, bid, L, lane);
  }
  if (tid < 320) phase1_unpack<320>(smem, rw, bid, tid);
}

__device__ __forceinline__ void pack_frags(const f32x16& v, u32x4& f0, u32x4& f1) {
  f0 = (u32x4){pack_bf16(v[0], v[1]), pack_bf16(v[2], v[3]), pack_bf16(v[4], v[5]), pack_bf16(v[6], v[7])};
  f1 = (u32x4){pack_bf16(v[8], v[9]), pack_bf16(v[10], v[11]), pack_bf16(v[12], v[13]), pack_bf16(v[14], v[15])};
}
template <int INV> __device__ __forceinline__ void dft64(const char* tab, const int lane, f32x16 (&re)[2], f32x16 (&im)[2]) {
  u32x4 br[4], bi[4], bn[4];
  pack_frags(re[0], br[0], br[1]); pack_frags(re[1], br[2], br[3]);
  pack_frags(im[0], bi[0], bi[1]); pack_frags(im[1], bi[2], bi[3]);
#pragma unroll
  for (int m = 0; m < 4; ++m) bn[m] = (INV ? br[m] : bi[m]) ^ 0x80008000u;
  const u32x4* t2 = (const u32x4*)(tab + 8192) + lane;
#pragma unroll
  for (int ub = 0; ub < 2; ++ub) {
    f32x16 xr = {}, xi = {};
#pragma unroll
    for (int m = 0; m < 4; ++m) {
      const u32x4 fr = t2[(ub * 4 + m) * 64], fi = t2[(8 + ub * 4 + m) * 64];
      xr = mfma32(fr, br[m], xr);
      xi = mfma32(fr, bi[m], xi);
      xr = mfma32(fi, INV ? bi[m] : bn[m], xr);
      xi = mfma32(fi, INV ? bn[m] : br[m], xi);
    }
    re[ub] = xr; im[ub] = xi;
  }
}
template <int INV> __device__ __forceinline__ void twid(const char* tab, const int lane, f32x16 (&re)[2], f32x16 (&im)[2]) {
  const f32x4* tw = (const f32x4*)(tab + 32768) + lane;
#pragma unroll
  for (int rb = 0; rb < 2; ++rb)
#pragma unroll
    for (int p = 0; p < 8; ++p) {
      const f32x4 w = tw[(rb * 8 + p) * 64];
      cf a = {re[rb][2 * p], im[rb][2 * p]}, b = {re[rb][2 * p + 1], im[rb][2 * p + 1]};
      a = INV ? cmulc(a, cf{w.x, w.y}) : cmul(a, cf{w.x, w.y});
      b = INV ? cmulc(b, cf{w.z, w.w}) : cmul(b, cf{w.z, w.w});
      re[rb][2 * p] = a.x; im[rb][2 * p] = a.y; re[rb][2 * p + 1] = b.x; im[rb][2 * p + 1] = b.y;
    }
}
#define WAVE_LDS_FENCE() do { __builtin_amdgcn_fence(__ATOMIC_RELEASE, "wavefront"); __builtin_amdgcn_wave_barrier(); __builtin_amdgcn_fence(__ATOMIC_ACQUIRE, "wavefront"); } while (0)

__device__ __forceinline__ void fft_role(char* smem, char* ws, rsrc_t rw, const int fb, const int wave, const int lane, const u32x4 srdW1) {
  const int kk = fb * 8 + wave, k = kk < DF ? kk : DF - 1;
  const int r = lane & 31, h = lane >> 5;
  {
    const u32x4 srdWS = make_srd(ws, (u32)WS_END), srdT = make_srd(&FTAB, 49152);
    const u32 ldsb = (u32)(size_t)smem;
#pragma unroll
    for (int j = 0; j < 8; ++j) {
      const int q = lane ^ (2 * j);
      dma16(ldsb + FFT_ZS + (wave * 8 + j) * 1024, (u32)(((q & 1) * 32 + (q >> 1)) * 16), srdWS, (u32)WS_Z + (u32)fb * 65536 + (wave * 8 + j) * 1024);
    }
#pragma unroll
    for (int j = 0; j < 6; ++j) dma16(ldsb + FFT_TAB + (wave * 6 + j) * 1024, lane * 16, srdT, (wave * 6 + j) * 1024);
  }
  WAITV(0); BAR();
  u32x4 wv[2][4];
  {
    const u32 wo = (u32)WS_WT + (k >> 2) * 32768 + (h * 16 + (r >> 1)) * 128 + (k & 3) * 32 + (r & 1) * 16;
#pragma unroll
    for (int ub = 0; ub < 2; ++ub)
#pragma unroll
      for (int g = 0; g < 4; ++g) wv[ub][g] = __builtin_amdgcn_raw_buffer_load_b128(rw, wo + (8 * ub + 2 * g) * 2048, 0, 0);
  }
  {
    const int g8 = fb & 7, ng = g8 < 5 ? 40 : 32;
#pragma unroll 1
    for (int n = PF1 * 640 + ((fb >> 3) * 8 + wave) * 64; n < 32 * 640; n += ng * 64) touch((u32)(size_t)smem + L_PF, w1_line(g8, n + lane), srdW1, 0);
  }
  u32x4 za[2][4];
  {
    const int kl = k - fb * 8;
    const char* zs = smem + FFT_ZS + (kl >> 2) * 32768 + (r >> 1) * 1024;
#pragma unroll
    for (int rb = 0; rb < 2; ++rb)
#pragma unroll
      for (int m = 0; m < 4; ++m) {
        const int c = 2 * h + (m >> 1), slot = (kl & 3) * 2 + (m & 1);
        za[rb][m] = *(const u32x4*)(zs + rb * 16384 + ((((8 * c + slot) * 2 + (r & 1)) ^ (2 * ((r >> 1) & 7))) * 16));
      }
  }
  asm volatile("s_waitcnt lgkmcnt(0)\n\ts_barrier" : "+v"(za[0][0]), "+v"(za[0][1]), "+v"(za[0][2]), "+v"(za[0][3]), "+v"(za[1][0]), "+v"(za[1][1]), "+v"(za[1][2]), "+v"(za[1][3]) :: "memory");
  const char* tab = smem + FFT_TAB;
  char* img = smem + FFT_WAVE + wave * FFT_WAVE_B;
  f32x16 re[2], im[2];
  {
    const u32x4* t1 = (const u32x4*)tab + lane;
    re[0] = f32x16{}; re[1] = f32x16{}; im[0] = f32x16{}; im[1] = f32x16{};
#pragma unroll
    for (int m = 0; m < 4; ++m) {
      const u32x4 fr = t1[m * 64], fi = t1[(4 + m) * 64];
      re[0] = mfma32(za[0][m], fr, re[0]); im[0] = mfma32(za[0][m], fi, im[0]);
      re[1] = mfma32(za[1][m], fr, re[1]); im[1] = mfma32(za[1][m], fi, im[1]);
    }
  }
  twid<0>(tab, lane, re, im);
  dft64<0>(tab, lane, re, im);
#pragma unroll
  for (int ub = 0; ub < 2; ++ub)
#pragma unroll
    for (int rho = 0; rho < 16; ++rho) {
      const cf p = cmul(cf{re[ub][rho], im[ub][rho]}, cf{bflo(wv[ub][rho >> 2][rho & 3]), bfhi(wv[ub][rho >> 2][rho & 3])});
      re[ub][rho] = p.x; im[ub][rho] = p.y;
    }
  dft64<1>(tab, lane, re, im);
  twid<1>(tab, lane, re, im);
#pragma unroll
  for (int rb = 0; rb < 2; ++rb)
#pragma unroll
    for (int g = 0; g < 4; ++g) {
      char* p = img + r * 136 + (32 * rb + 8 * g + 4 * h) * 2;
      *(u32x2*)p = (u32x2){pack_bf16(re[rb][4 * g], re[rb][4 * g + 1]), pack_bf16(re[rb][4 * g + 2], re[rb][4 * g + 3])};
      *(u32x2*)(p + 4352) = (u32x2){pack_bf16(im[rb][4 * g], im[rb][4 * g + 1]), pack_bf16(im[rb][4 * g + 2], im[rb][4 * g + 3])};
    }
  u32x4 va[2][2][2];
  {
    const int blk = (lane >> 4) & 1, q = (lane & 15) >> 2, p = lane & 3;
    const u32 a = (u32)(size_t)img + (8 * h + q) * 136 + (16 * blk + 4 * p) * 2;
    u32x2 t[16];
    asm volatile("s_waitcnt lgkmcnt(0)\n\t"
                 "ds_read_b64_tr_b16 %0, %16\n\tds_read_b64_tr_b16 %1, %16 offset:544\n\tds_read_b64_tr_b16 %2, %16 offset:2176\n\tds_read_b64_tr_b16 %3, %16 offset:2720\n\t"
                 "ds_read_b64_tr_b16 %4, %16 offset:64\n\tds_read_b64_tr_b16 %5, %16 offset:608\n\tds_read_b64_tr_b16 %6, %16 offset:2240\n\tds_read_b64_tr_b16 %7, %16 offset:2784\n\t"
                 "ds_read_b64_tr_b16 %8, %16 offset:4352\n\tds_read_b64_tr_b16 %9, %16 offset:4896\n\tds_read_b64_tr_b16 %10, %16 offset:6528\n\tds_read_b64_tr_b16 %11, %16 offset:7072\n\t"
                 "ds_read_b64_tr_b16 %12, %16 offset:4416\n\tds_read_b64_tr_b16 %13, %16 offset:4960\n\tds_read_b64_tr_b16 %14, %16 offset:6592\n\tds_read_b64_tr_b16 %15, %16 offset:7136\n\t"
                 "s_waitcnt lgkmcnt(0)"
                 : "=&v"(t[0]), "=&v"(t[1]), "=&v"(t[2]), "=&v"(t[3]), "=&v"(t[4]), "=&v"(t[5]), "=&v"(t[6]), "=&v"(t[7]),
                   "=&v"(t[8]), "=&v"(t[9]), "=&v"(t[10]), "=&v"(t[11]), "=&v"(t[12]), "=&v"(t[13]), "=&v"(t[14]), "=&v"(t[15])
                 : "v"(a) : "memory");
#pragma unroll
    for (int i = 0; i < 8; ++i) va[i >> 2][(i >> 1) & 1][i & 1] = (u32x4){t[2 * i].x, t[2 * i].y, t[2 * i + 1].x, t[2 * i + 1].y};
  }
  {
    const u32x4* t4 = (const u32x4*)(tab + 24576) + lane;
    re[0] = f32x16{}; re[1] = f32x16{}; im[0] = f32x16{}; im[1] = f32x16{};
#pragma unroll
    for (int ks = 0; ks < 2; ++ks) {
      const u32x4 gr = t4[ks * 64], gi = t4[(2 + ks) * 64], gn = t4[(4 + ks) * 64];
#pragma unroll
      for (int rb = 0; rb < 2; ++rb) {
        re[rb] = mfma32(va[0][rb][ks], gr, re[rb]); im[rb] = mfma32(va[0][rb][ks], gi, im[rb]);
        re[rb] = mfma32(va[1][rb][ks], gn, re[rb]); im[rb] = mfma32(va[1][rb][ks], gr, im[rb]);
      }
    }
  }
#pragma unroll
  for (int rb = 0; rb < 2; ++rb)
#pragma unroll
    for (int g = 0; g < 4; ++g) {
      char* p = img + r * 144 + (32 * rb + 8 * g + 4 * h) * 2;
      *(u32x2*)p = (u32x2){pack_bf16(re[rb][4 * g], re[rb][4 * g + 1]), pack_bf16(re[rb][4 * g + 2], re[rb][4 * g + 3])};
      *(u32x2*)(p + 4608) = (u32x2){pack_bf16(im[rb][4 * g], im[rb][4 * g + 1]), pack_bf16(im[rb][4 * g + 2], im[rb][4 * g + 3])};
    }
  WAVE_LDS_FENCE();
  if (kk < DF) {
    const int rowRe = (k == DF - 1) ? 1 : 2 * k;
#pragma unroll
    for (int i = 0; i < 4; ++i) st16(rw, (u32)WS_YT + (rowRe * 256 + 64 * i + lane) * 16, *(const u32x4*)(img + (8 * i + (lane >> 3)) * 144 + (lane & 7) * 16));
    if (k > 0 && k < DF - 1) {
#pragma unroll
      for (int i = 0; i < 4; ++i) st16(rw, (u32)WS_YT + ((2 * k + 1) * 256 + 64 * i + lane) * 16, *(const u32x4*)(img + 4608 + (8 * i + (lane >> 3)) * 144 + (lane & 7) * 16));
    }
  }
}

__device__ __forceinline__ u32x2 gelu4(const f32x4 a, const u32x2 bb) {
  const float bv[4] = {bflo(bb.x), bfhi(bb.x), bflo(bb.y), bfhi(bb.y)};
  float r[4];
#pragma unroll
  for (int e = 0; e < 4; ++e) {
    const float u = a[e] + bv[e];
    const float z = u * __builtin_fmaf(u * u, -0.044715f * 2.0f * 0.7978845608028654f * 1.4426950408889634f, -2.0f * 0.7978845608028654f * 1.4426950408889634f);
    r[e] = u * __builtin_amdgcn_rcpf(1.0f + __builtin_amdgcn_exp2f(z));
  }
  return (u32x2){pack_bf16(r[0], r[1]), pack_bf16(r[2], r[3])};
}
__device__ __forceinline__ void g1_store2(const f32x4 a0, const f32x4 a1, const u32x2 bb0, const u32x2 bb1, rsrc_t rw, const u32 off) {
  const u32x2 x = gelu4(a0, bb0), y = gelu4(a1, bb1);
  const auto s0 = __builtin_amdgcn_permlane16_swap(x.x, y.x, false, false);
  const auto s1 = __builtin_amdgcn_permlane16_swap(x.y, y.y, false, false);
  st16(rw, off, (u32x4){s0[0], s1[0], s0[1], s1[1]});
}
__device__ __forceinline__ void count_in(char* ws, size_t off, int lane) {
  if (lane == 0) __hip_atomic_fetch_add((gu32*)(ws + off), 1u, __ATOMIC_RELAXED, __HIP_MEMORY_SCOPE_AGENT);
}

constexpr int g2_owner(int i, int j) { return j < 8 ? (j >> 1) : i; }
template <int Q>
__device__ __forceinline__ void g2_finish(f32x4 (&acc)[2][9], const u16* __restrict__ x, const u16* __restrict__ b2, char* ws, rsrc_t rw, u16* __restrict__ out,
                                          const int tile, const int s0, const int d0, const int wave, const int lane) {
  const int srow = s0 + wave * 32 + (lane & 15), dcol = d0 + 4 * (lane >> 4);
  u32x2 xr[2][9]; float bs[2];
#pragma unroll
  for (int i = 0; i < 2; ++i) {
    bs[i] = bflo((u32)b2[srow + 16 * i]);
#pragma unroll
    for (int j = 0; j < 9; ++j)
      if (g2_owner(i, j) == Q) xr[i][j] = *(const u32x2*)(x + (size_t)(srow + 16 * i) * D + dcol + 16 * j);
  }
  const u32 mine = (u32)WS_P + (u32)(((tile * 4 + Q) * 4 + wave) * 10) * 1024;
#pragma unroll
  for (int j = 0; j < 8; ++j)
    if ((j >> 1) != Q)
      st16(rw, mine + j * 1024 + lane * 16, (u32x4){pack_bf16(acc[0][j].x, acc[0][j].y), pack_bf16(acc[0][j].z, acc[0][j].w), pack_bf16(acc[1][j].x, acc[1][j].y), pack_bf16(acc[1][j].z, acc[1][j].w)});
#pragma unroll
  for (int i = 0; i < 2; ++i)
    if (i != Q) st8(rw, mine + (8 + i) * 1024 + lane * 8, (u32x2){pack_bf16(acc[i][8].x, acc[i][8].y), pack_bf16(acc[i][8].z, acc[i][8].w)});
  WAITV(0);
  if (lane == 0) set_flag(ws, WS_FP + ((tile * 4 + wave) * 4 + Q) * 4);
  wait_lanes(ws, (lane < 4 && lane != Q) ? (u32)WS_FP + ((tile * 4 + wave) * 4 + lane) * 4 : ~0u, 1);
  u32x4 pv[3][2]; u32x2 pe[3];
#pragma unroll
  for (int o = 0; o < 3; ++o) {
    const int src = o < Q ? o : o + 1;
    const u32 base = (u32)WS_P + (u32)(((tile * 4 + src) * 4 + wave) * 10) * 1024;
    pv[o][0] = ld16(rw, base + (2 * Q) * 1024 + lane * 16);
    pv[o][1] = ld16(rw, base + (2 * Q + 1) * 1024 + lane * 16);
    if (Q < 2) pe[o] = __builtin_amdgcn_raw_buffer_load_b64(rw, base + (8 + Q) * 1024 + lane * 8, 0, 16);
  }
#pragma unroll
  for (int i = 0; i < 2; ++i)
#pragma unroll
    for (int j = 0; j < 9; ++j)
      if (g2_owner(i, j) == Q) {
        f32x4 t = acc[i][j];
#pragma unroll
        for (int o = 0; o < 3; ++o) {
          const u32 lo = j < 8 ? pv[o][j & 1][2 * i] : pe[o].x, hi = j < 8 ? pv[o][j & 1][2 * i + 1] : pe[o].y;
          t += (f32x4){bflo(lo), bfhi(lo), bflo(hi), bfhi(hi)};
        }
        const u32x2 xv = xr[i][j];
        const u32x2 o2 = {pack_bf16(t.x + bs[i] + bflo(xv.x), t.y + bs[i] + bfhi(xv.x)), pack_bf16(t.z + bs[i] + bflo(xv.y), t.w + bs[i] + bfhi(xv.y))};
        *(u32x2*)(out + (size_t)(srow + 16 * i) * D + dcol + 16 * j) = o2;
      }
}

struct Geo {
  int f0, d1, ft, dt;
  int kq, s0, d2, tile, dt2, nD, eb;
};

extern "C" __global__ __launch_bounds__(512, 2) void k2_fused(const u16* __restrict__ x,
                                                   const u16* __restrict__ w1, const u16* __restrict__ b1, const u16* __restrict__ w2,
                                                   const u16* __restrict__ b2, char* ws, u16* __restrict__ out) {
  extern __shared__ __attribute__((aligned(16))) char smem[];
  const int bid = blockIdx.x;
  const int tid = threadIdx.x, lane = tid & 63;
  const int wave = __builtin_amdgcn_readfirstlane(tid >> 6);
  const u32 ldsb = (u32)(size_t)smem;
  const rsrc_t rw = __builtin_amdgcn_make_buffer_rsrc(ws, 0, (int)WS_END, 0x00020000);
  const bool isG1 = bid < G1_WGS, isFFT = !isG1 && bid < G1_WGS + FFT_WGS;
  Geo G;
  {
    const int xg = bid & 7, idx = bid >> 3;
    const int lin = xg * 27 + idx;
    G.ft = lin / 6; G.dt = lin - G.ft * 6; G.f0 = G.ft * 128; G.d1 = G.dt * 96;
    G.kq = xg >> 1;
    const int st = (xg & 1) * 8 + (idx >> 2);
    G.dt2 = idx & 3; G.s0 = st * 128; G.d2 = G.dt2 * 144; G.tile = st * 4 + G.dt2;
    G.nD = G.kq == 3 ? 3 : 2; G.eb = 2 * G.kq;
  }
  const int L = wave & 1;
  const int kk = lane >> 4;
  const u32 cs0 = ((lane & 15) ^ (kk << 1)) * 16, cs1 = ((lane & 15) ^ ((kk << 1) | 8)) * 16;
  const u32 mW = ldsb + L_W + L * 8192;
  const u32x4 srdW1 = make_srd(w1, (u32)((size_t)S * F * 2));
  const u32 vW0 = (u32)((64 * kk + 4 * L) * (F * 2) + G.f0 * 2) + cs0, vW1 = (u32)((64 * kk + 4 * L) * (F * 2) + G.f0 * 2) + cs1;
#define G1_W(t, sl) do { const u32 _s = (u32)(8 * ((t) & 7) + 512 * ((t) >> 3)) * (F * 2); \
    _Pragma("unroll") for (int j = 0; j < 8; ++j) dma16(mW + (sl) * 16384 + j * 1024, (j & 2) ? vW1 : vW0, srdW1, _s + ((j >> 1) + 256 * (j & 1)) * (F * 2)); } while (0)

  if (isFFT) {
    fft_role(smem, ws, rw, bid - G1_WGS, wave, lane, srdW1);
    WAITV(0); BAR();
    if (wave == 4 && lane == 0) set_flag(ws, WS_FY + (bid - G1_WGS) * 4);
  }

  if (wave >= 4) {
    const int wd = (wave >> 1) & 1, wf = wave & 1;
    const int fbase = G.f0 + wf * 64 + 32 + 4 * kk;
    const u32 goff = (u32)WS_GB + (u32)((((G.f0 + wf * 64 + 32) >> 3) + (kk & 1) * 2 + (kk >> 1)) * D + G.d1 + wd * 48 + (lane & 15)) * 16;
    const u32 fyoff = (u32)WS_FY + (lane < 4 * G.nD ? 4 * G.eb + lane : 36) * 4;
    if (wave >= 6) {
      if (isG1) {
        const u32x2 bb0 = *(const u32x2*)(b1 + fbase), bb1 = *(const u32x2*)(b1 + fbase + 16);
        if (wave == 7) {
#pragma unroll 1
          for (int i = 0; i < 35; ++i) BAR();
        } else {
          const gu32* fy = (const gu32*)(ws + fyoff);
          if (lane == 0) *(volatile LAS u32*)(LAS char*)(smem + L_PF + 256) = 0u;
          u32 seen = 0;
#pragma unroll 1
          for (int i = 0; i < 35; ++i) {
            BAR();
            if (i == 25 || i == 28 || i == 31) {
              if (i != 25 && __all(seen != 0u) && lane == 0) *(volatile LAS u32*)(LAS char*)(smem + L_PF + 256) = 1u;
              asm volatile("s_waitcnt lgkmcnt(0)" ::: "memory");
              if (i != 31) seen = __hip_atomic_load(fy, __ATOMIC_RELAXED, __HIP_MEMORY_SCOPE_AGENT);
            }
          }
        }
        const f32x4* ex = (const f32x4*)(smem + L_EX) + (wave - 4) * 384 + lane;
#pragma unroll
        for (int j = 0; j < 3; ++j) g1_store2(ex[j * 64], ex[(3 + j) * 64], bb0, bb1, rw, goff + j * 256);
        WAITV(0);
        count_in(ws, WS_FG + (G.ft * 6 + G.dt) * 4, lane);
        asm volatile("s_waitcnt lgkmcnt(0)\n\ts_barrier" ::: "memory");
      }
      return;
    }
    const u32x4 srdWS = make_srd(ws, (u32)WS_END);
    if (isG1) {
      u32 vH[6];
#pragma unroll
      for (int j = 0; j < 6; ++j) { const int p = j * 64 + lane; const int kc = p / 96, dd = p - kc * 96; vH[j] = (u32)WS_HB + (u32)(((4 * L + kc) * D + G.d1 + dd) * 16); }
      const u32 mH = ldsb + L_X + L * 6144;
#define G1_H(t, sl) do { _Pragma("unroll") for (int j = 0; j < 6; ++j) dma16(mH + (sl) * 12288 + j * 1024, vH[j], srdWS, (u32)(t) * (8 * D * 16)); } while (0)
#pragma unroll
      for (int t = 0; t < 4; ++t) { G1_W(t, t); G1_H(t, t); }
      WAITV(42); BAR();
      WAITV(28); BAR(); G1_W(4, 4); G1_H(4, 4);
      WAITV(28); BAR(); G1_W(5, 0); G1_H(5, 0);
      int sl = 1;
#pragma unroll 1
      for (int t = 2; t < 28; ++t) {
        WAITV(28); BAR();
        G1_W(t + 4, sl); G1_H(t + 4, sl);
        sl = sl == 4 ? 0 : sl + 1;
      }
      WAITV(28); BAR();
      WAITV(14); BAR();
      WAITV(0); BAR();
    }
    const u32x4 srdW2 = make_srd(w2, (u32)((size_t)F * S * 2));
    int ln2 = lane; asm volatile("" : "+v"(ln2));
    const int kk2 = ln2 >> 4;
    const u32 vA0 = (u32)((32 * L + kk2) * (S * 2) + G.s0 * 2) + ((ln2 & 15) ^ (kk2 << 1)) * 16, vA1 = (u32)((32 * L + kk2) * (S * 2) + G.s0 * 2) + ((ln2 & 15) ^ ((kk2 << 1) | 8)) * 16;
    u32 vG[9];
#pragma unroll
    for (int j = 0; j < 9; ++j) { const int p = j * 64 + ln2; const int kc = p / 144, dd = p - kc * 144; vG[j] = (u32)WS_GB + (u32)(((4 * L + kc) * D + G.d2 + dd) * 16); }
    const u32 mG = ldsb + L_X + L * 9216;
    const int nD = G.nD, nk = nD + 18;
#define G2_WY(e, sl) do { _Pragma("unroll") for (int j = 0; j < 8; ++j) dma16s(mW + (sl) * 16384 + j * 1024, (j & 2) ? vA1 : vA0, srdWS, (u32)WS_YT + (u32)(e) * (64 * S * 2) + j * (4 * S * 2)); } while (0)
#define G2_WW(t, sl) do { _Pragma("unroll") for (int j = 0; j < 8; ++j) dma16(mW + (sl) * 16384 + j * 1024, (j & 2) ? vA1 : vA0, srdW2, (u32)(t) * (64 * S * 2) + j * (4 * S * 2)); } while (0)
    const u32x4 srdI = make_srd((const char*)IDFT - WS_GB, (u32)(WS_GB + sizeof(IDFT)));
#define G2_GI(e, sl) do { _Pragma("unroll") for (int j = 0; j < 9; ++j) dma16(mG + (sl) * 18432 + j * 1024, vG[j], srdI, (u32)(e) * (8 * D * 16)); } while (0)
#define G2_G(t, sl) do { _Pragma("unroll") for (int j = 0; j < 9; ++j) dma16s(mG + (sl) * 18432 + j * 1024, vG[j], srdWS, (u32)(t) * (8 * D * 16)); } while (0)
    bool early = false;
    if (isG1) {
      const u32x2 bb0 = *(const u32x2*)(b1 + fbase), bb1 = *(const u32x2*)(b1 + fbase + 16);
      BAR(); BAR();
      early = __builtin_amdgcn_readfirstlane(*(volatile LAS u32*)(LAS char*)(smem + L_PF + 256)) != 0;
      if (early) { G2_WY(G.eb, 0); G2_WY(G.eb + 1, 1); if (nD == 3) G2_WY(G.eb + 2, 2); }
      G2_GI(G.eb, 0); G2_GI(G.eb + 1, 1);
      if (nD == 3) G2_GI(G.eb + 2, 2);
      BAR();
      const f32x4* ex = (const f32x4*)(smem + L_EX) + L * 384 + lane;
#pragma unroll
      for (int j = 0; j < 3; ++j) g1_store2(ex[j * 64], ex[(3 + j) * 64], bb0, bb1, rw, goff + j * 256);
      WAITV(0);
      count_in(ws, WS_FG + (G.ft * 6 + G.dt) * 4, lane);
      asm volatile("s_waitcnt lgkmcnt(0)\n\ts_barrier" ::: "memory");
    } else {
      G2_GI(G.eb, 0); G2_GI(G.eb + 1, 1);
      if (nD == 3) G2_GI(G.eb + 2, 2);
    }
    const u32 fgoff = lane < 18 ? (u32)WS_FG + ((9 * G.kq + (lane >> 1)) * 6 + ((G.dt2 * 3) >> 1) + (lane & 1)) * 4 : ~0u;
    bool gseen = false;
    if (!early) {
      wait_lanes(ws, fyoff, 1);
      G2_WY(G.eb, 0); G2_WY(G.eb + 1, 1);
      if (nD == 3) G2_WY(G.eb + 2, 2);
      gseen = look_lanes(ws, fgoff, 8);
    }
    WAITV(0); BAR();
    if (!gseen) wait_lanes(ws, fgoff, 8);
    const int t0 = 18 * G.kq - nD;
    if (nD == 2) {
      G2_WW(t0 + 2, 2); G2_G(t0 + 2, 2);
      G2_WW(t0 + 3, 3); G2_G(t0 + 3, 3);
      WAITV(17); BAR();
      G2_WW(t0 + 4, 0); G2_G(t0 + 4, 0);
    } else {
      G2_WW(t0 + 3, 3); G2_G(t0 + 3, 3);
      WAITV(0); BAR();
      G2_WW(t0 + 4, 0); G2_G(t0 + 4, 0);
      G2_WW(t0 + 5, 1); G2_G(t0 + 5, 1);
    }
#pragma unroll 1
    for (int sg = nD; sg < nk - 3; ++sg) {
      WAITV(17); BAR();
      const int sl = (sg + 3) & 3;
      G2_WW(t0 + sg + 3, sl); G2_G(t0 + sg + 3, sl);
    }
    WAITV(17); BAR();
    WAITV(0); BAR();
    return;
  }

  const int lg = lane >> 4, lq = (lane & 15) >> 2, lp = lane & 3;
  const u32 trbase = ldsb + L_W + (u32)((8 * lg + lq) * 256 + (lp & 1) * 8);
  const int swz = (lq << 1) | ((lg & 1) << 3);
  if (isG1) {
    const int wd = (wave >> 1) & 1, wf = wave & 1;
    u32 aA[4];
#pragma unroll
    for (int i = 0; i < 4; ++i) aA[i] = trbase + (u32)(((wf * 8 + 2 * i + (lp >> 1)) ^ swz) * 16);
    const u32 aB = ldsb + L_X + (u32)((lg * 96 + wd * 48 + (lane & 15)) * 16);
    const int fbase = G.f0 + wf * 64 + 4 * lg;
    const u32 goff = (u32)WS_GB + (u32)((((G.f0 + wf * 64) >> 3) + (lg & 1) * 2 + (lg >> 1)) * D + G.d1 + wd * 48 + (lane & 15)) * 16;
    const u32x2 bb0 = *(const u32x2*)(b1 + fbase), bb1 = *(const u32x2*)(b1 + fbase + 16);
    f32x4 acc[4][3];
#pragma unroll
    for (int i = 0; i < 4; ++i)
#pragma unroll
      for (int j = 0; j < 3; ++j) acc[i][j] = (f32x4){0.f, 0.f, 0.f, 0.f};
    u32x4 fb[2][3];
    u32x2 al[2][4], ah[2][4];
    u32 aA3[4];
#pragma unroll
    for (int i = 0; i < 4; ++i) aA3[i] = aA[i] + 3 * 16384;
#define G1_RDB(dst, J, SL, H) dst = rd128<12288 * (SL) + 6144 * (H) + 256 * (J)>(aB)
#define G1_RDA(I, SL, H, T) trd<16384 * ((SL) % 3) + 8192 * (H) + 1024 * (T)>((SL) < 3 ? aA[I] : aA3[I])
#define P1_M(I, J, C) do { acc[I][J] = mfma16((u32x4){al[C][I].x, al[C][I].y, ah[C][I].x, ah[C][I].y}, fb[C][J], acc[I][J]); SB0; } while (0)
#define P1_HALF(C, N, SL, H) do { \
    WL(8); P1_M(0, 0, C); G1_RDB(fb[N][0], 0, SL, H); SB0; \
    WL(8); P1_M(0, 1, C); al[N][0] = G1_RDA(0, SL, H, 0); SB0; \
    WL(8); P1_M(0, 2, C); ah[N][0] = G1_RDA(0, SL, H, 1); SB0; \
    WL(7); P1_M(1, 0, C); G1_RDB(fb[N][1], 1, SL, H); SB0; \
    P1_M(1, 1, C); G1_RDB(fb[N][2], 2, SL, H); SB0; \
    P1_M(1, 2, C); al[N][1] = G1_RDA(1, SL, H, 0); SB0; \
    WL(8); P1_M(2, 0, C); ah[N][1] = G1_RDA(1, SL, H, 1); SB0; \
    P1_M(2, 1, C); al[N][2] = G1_RDA(2, SL, H, 0); SB0; \
    P1_M(2, 2, C); ah[N][2] = G1_RDA(2, SL, H, 1); SB0; \
    WL(9); P1_M(3, 0, C); al[N][3] = G1_RDA(3, SL, H, 0); SB0; \
    P1_M(3, 1, C); ah[N][3] = G1_RDA(3, SL, H, 1); SB0; \
    P1_M(3, 2, C); } while (0)
#define P1_STEP(SL) do { BAR(); SB0; P1_HALF(0, 1, SL, 1); P1_HALF(1, 0, ((SL) + 1) % 5, 0); } while (0)
    asm volatile("s_waitcnt lgkmcnt(0)\n\ts_barrier" ::: "memory");
    G1_RDB(fb[0][0], 0, 0, 0); al[0][0] = G1_RDA(0, 0, 0, 0); ah[0][0] = G1_RDA(0, 0, 0, 1); G1_RDB(fb[0][1], 1, 0, 0); G1_RDB(fb[0][2], 2, 0, 0);
    al[0][1] = G1_RDA(1, 0, 0, 0); ah[0][1] = G1_RDA(1, 0, 0, 1); al[0][2] = G1_RDA(2, 0, 0, 0); ah[0][2] = G1_RDA(2, 0, 0, 1);
    al[0][3] = G1_RDA(3, 0, 0, 0); ah[0][3] = G1_RDA(3, 0, 0, 1);
    P1_STEP(0); P1_STEP(1);
#pragma unroll 1
    for (int t = 0; t < 6; ++t) { P1_STEP(2); P1_STEP(3); P1_STEP(4); P1_STEP(0); P1_STEP(1); }
    asm volatile("s_waitcnt lgkmcnt(0)\n\ts_barrier" ::: "memory");
    SB0;
    f32x4* ex = (f32x4*)(smem + L_EX);
#pragma unroll
    for (int ii = 0; ii < 2; ++ii)
#pragma unroll
      for (int j = 0; j < 3; ++j) ex[(wave * 6 + ii * 3 + j) * 64 + lane] = acc[2 + ii][j];
    asm volatile("s_waitcnt lgkmcnt(0)\n\ts_barrier" ::: "memory");
#pragma unroll
    for (int j = 0; j < 3; ++j) g1_store2(acc[0][j], acc[1][j], bb0, bb1, rw, goff + j * 256);
    WAITV(0);
    count_in(ws, WS_FG + (G.ft * 6 + G.dt) * 4, lane);
    BAR();
    {
      const u32x4 srdW2 = make_srd(w2, (u32)((size_t)F * S * 2));
#pragma unroll 1
      for (int n = ((bid >> 3) * 4 + wave) * 64 + lane; n < PFW2N * 1024; n += 27 * 256)
        touch(ldsb + L_PF, (u32)((1152 * G.kq + (n >> 4)) * (S * 2) + (bid & 1) * 2048 + (n & 15) * 128), srdW2, 0);
    }
  }
  BAR();
  {
    const u32 aW0 = trbase + (u32)(((wave * 4 + (lp >> 1)) ^ swz) * 16), aW1 = trbase + (u32)(((wave * 4 + 2 + (lp >> 1)) ^ swz) * 16);
    const u32 aG = ldsb + L_X + (u32)((lg * 144 + (lane & 15)) * 16);
    f32x4 acc[2][9];
#pragma unroll
    for (int i = 0; i < 2; ++i)
#pragma unroll
      for (int j = 0; j < 9; ++j) acc[i][j] = (f32x4){0.f, 0.f, 0.f, 0.f};
    u32x4 fg[2][9];
    u32x2 wl[2][2], wh[2][2];
    const u32 aG2 = aG + 2 * 18432;
#define G2_RDG(dst, J, SL, H) dst = rd128<18432 * ((SL) & 1) + 9216 * (H) + 256 * (J)>((SL) < 2 ? aG : aG2)
#define G2_RDW(a, SL, H, T) trd<16384 * (SL) + 8192 * (H) + 1024 * (T)>(a)
#define P2_M(I, J, C) do { acc[I][J] = mfma16(fg[C][J], (u32x4){wl[C][I].x, wl[C][I].y, wh[C][I].x, wh[C][I].y}, acc[I][J]); SB0; } while (0)
#define P2_HALF(C, N, SL, H) do { \
    WL(10); P2_M(0, 0, C); wl[N][0] = G2_RDW(aW0, SL, H, 0); SB0; \
    WL(9);  P2_M(1, 0, C); wh[N][0] = G2_RDW(aW0, SL, H, 1); SB0; \
    WL(9);  P2_M(0, 1, C); G2_RDG(fg[N][0], 0, SL, H); SB0; \
            P2_M(1, 1, C); wl[N][1] = G2_RDW(aW1, SL, H, 0); SB0; \
    WL(10); P2_M(0, 2, C); wh[N][1] = G2_RDW(aW1, SL, H, 1); SB0; \
            P2_M(1, 2, C); G2_RDG(fg[N][1], 1, SL, H); SB0; \
    WL(11); P2_M(0, 3, C); G2_RDG(fg[N][2], 2, SL, H); SB0; \
            P2_M(1, 3, C); G2_RDG(fg[N][3], 3, SL, H); SB0; \
    WL(12); P2_M(0, 4, C); G2_RDG(fg[N][4], 4, SL, H); SB0; \
            P2_M(1, 4, C); G2_RDG(fg[N][5], 5, SL, H); SB0; \
    WL(13); P2_M(0, 5, C); G2_RDG(fg[N][6], 6, SL, H); SB0; \
            P2_M(1, 5, C); G2_RDG(fg[N][7], 7, SL, H); SB0; \
    WL(14); P2_M(0, 6, C); G2_RDG(fg[N][8], 8, SL, H); SB0; \
            P2_M(1, 6, C); \
    WL(14); P2_M(0, 7, C); P2_M(1, 7, C); \
    WL(13); P2_M(0, 8, C); P2_M(1, 8, C); } while (0)
#define P2_BODY(SL) do { SB0; P2_HALF(0, 1, SL, 1); P2_HALF(1, 0, ((SL) + 1) & 3, 0); } while (0)
    wl[0][0] = G2_RDW(aW0, 0, 0, 0); wh[0][0] = G2_RDW(aW0, 0, 0, 1); G2_RDG(fg[0][0], 0, 0, 0); wl[0][1] = G2_RDW(aW1, 0, 0, 0); wh[0][1] = G2_RDW(aW1, 0, 0, 1);
    G2_RDG(fg[0][1], 1, 0, 0); G2_RDG(fg[0][2], 2, 0, 0); G2_RDG(fg[0][3], 3, 0, 0); G2_RDG(fg[0][4], 4, 0, 0);
    G2_RDG(fg[0][5], 5, 0, 0); G2_RDG(fg[0][6], 6, 0, 0); G2_RDG(fg[0][7], 7, 0, 0); G2_RDG(fg[0][8], 8, 0, 0);
    P2_BODY(0);
    if (G.nD == 2) BAR();
    P2_BODY(1);
#pragma unroll 1
    for (int it = 0; it < 4; ++it) { BAR(); P2_BODY(2); BAR(); P2_BODY(3); BAR(); P2_BODY(0); BAR(); P2_BODY(1); }
    BAR(); P2_BODY(2);
    if (G.nD == 3) BAR();
    P2_BODY(3);
    if (G.nD == 3) P2_BODY(0);
    asm volatile("s_waitcnt lgkmcnt(0)" ::: "memory");
    SB0;
    switch (G.kq) {
      case 0: g2_finish<0>(acc, x, b2, ws, rw, out, G.tile, G.s0, G.d2, wave, lane); break;
      case 1: g2_finish<1>(acc, x, b2, ws, rw, out, G.tile, G.s0, G.d2, wave, lane); break;
      case 2: g2_finish<2>(acc, x, b2, ws, rw, out, G.tile, G.s0, G.d2, wave, lane); break;
      default: g2_finish<3>(acc, x, b2, ws, rw, out, G.tile, G.s0, G.d2, wave, lane); break;
    }
  }
}

extern "C" void kernel_launch(void* const* d_in, const int* in_sizes, int n_in, void* d_out, int out_size, void* d_ws, size_t ws_size, hipStream_t stream) {
  (void)hipFuncSetAttribute((const void*)k2_fused, hipFuncAttributeMaxDynamicSharedMemorySize, MK_LDS);
  k1_rows<<<256, 512, 0, stream>>>((const u16*)d_in[0], (const u16*)d_in[1], (const u16*)d_in[2], (const u16*)d_in[4], (char*)d_ws);
  k2_fused<<<256, 512, MK_LDS, stream>>>((const u16*)d_in[0], (const u16*)d_in[4], (const u16*)d_in[5], (const u16*)d_in[6], (const u16*)d_in[7], (char*)d_ws, (u16*)d_out);
}
```

```cpp
#include <hip/hip_runtime.h>
#include <hip/hip_bf16.h>
#include <stdint.h>

typedef unsigned short u16;
typedef unsigned int u32;
using bf16x8 = __attribute__((ext_vector_type(8))) short;
using f32x4 = __attribute__((ext_vector_type(4))) float;
using f32x16 = __attribute__((ext_vector_type(16))) float;
using f32x2 = __attribute__((ext_vector_type(2))) float;
using u32x4 = __attribute__((ext_vector_type(4))) unsigned;
using u32x2 = __attribute__((ext_vector_type(2))) unsigned;
#define LAS __attribute__((address_space(3)))
#define GAS __attribute__((address_space(1)))
#define SB0 __builtin_amdgcn_sched_barrier(0)

constexpr int S = 2048, D = 576, DF = 289, F = 4608;
constexpr int KX = F + D;

constexpr size_t WS_HB = 0;
constexpr size_t WS_Z = WS_HB + (size_t)S * D * 2;
constexpr size_t WS_WT = WS_Z + (size_t)74 * 32768;
constexpr size_t WS_GB = WS_WT + (size_t)73 * 32768;
constexpr size_t WS_YT = WS_GB + (size_t)F * D * 2;
constexpr size_t WS_P = WS_YT + (size_t)D * S * 2;
constexpr size_t WS_P_BYTES = (size_t)256 * 4 * 18 * 1024;
constexpr size_t WS_FG = WS_P + WS_P_BYTES;
constexpr size_t WS_FY = WS_FG + 1024;
constexpr size_t WS_FP = WS_FY + 256;
constexpr size_t WS_END = WS_FP + 4096;

constexpr double kPi = 3.141592653589793238462643383279502884;
constexpr double c_sin_small(double x) { double t = x, s = x; for (int i = 1; i < 14; ++i) { t *= -x * x / ((2 * i) * (2 * i + 1)); s += t; } return s; }
constexpr double c_cos_small(double x) { double t = 1, s = 1; for (int i = 1; i < 14; ++i) { t *= -x * x / ((2 * i - 1) * (2 * i)); s += t; } return s; }
constexpr double c_cos(int m, int n) {
  m %= n; if (2 * m > n) m = n - m;
  if (4 * m > n) return -((8 * (n / 2 - m) > n) ? c_sin_small(2 * kPi * (m - n / 4) / n) : c_cos_small(2 * kPi * (n / 2 - m) / n));
  return (8 * m > n) ? c_sin_small(2 * kPi * (n / 4 - m) / n) : c_cos_small(2 * kPi * m / n);
}
constexpr double c_sin(int m, int n) { return c_cos((m % n) + n - n / 4, n); }
template <int N> struct TwTab { float v[2 * N]; constexpr TwTab() : v() { for (int m = 0; m < N; ++m) { v[2 * m] = (float)c_cos(m, N); v[2 * m + 1] = (float)(-c_sin(m, N)); } } };
__device__ const TwTab<576> TW576 = TwTab<576>();
#if defined(__HIP_DEVICE_COMPILE__)
constexpr TwTab<2048> TWB = TwTab<2048>();
constexpr unsigned short c_bf16(float f) { unsigned u = __builtin_bit_cast(unsigned, f); return (unsigned short)((u + 0x7fffu + ((u >> 16) & 1u)) >> 16); }
struct FftTab {
  unsigned short t1[2][4][64][8];
  unsigned short t2[2][2][4][64][8];
  unsigned short t4[3][2][64][8];
  unsigned short pad[1024];
  float tw[2][8][64][4];
  constexpr FftTab() : t1(), t2(), t4(), pad(), tw() {
    for (int l = 0; l < 64; ++l) {
      const int r = l & 31, h = l >> 5;
      for (int j = 0; j < 8; ++j) {
        for (int m = 0; m < 4; ++m) {
          const int a = 16 * h + 4 * m + (j >> 1), e = ((a * r) & 31) * 64;
          const float cr = TWB.v[2 * e], ci = TWB.v[2 * e + 1];
          t1[0][m][l][j] = c_bf16((j & 1) ? -ci : cr);
          t1[1][m][l][j] = c_bf16((j & 1) ? cr : ci);
          for (int ub = 0; ub < 2; ++ub) {
            const int b = 16 * m + 8 * (j >> 2) + 4 * h + (j & 3), e2 = (((32 * ub + r) * b) & 63) * 32;
            t2[0][ub][m][l][j] = c_bf16(TWB.v[2 * e2]);
            t2[1][ub][m][l][j] = c_bf16(TWB.v[2 * e2 + 1]);
          }
        }
        for (int ks = 0; ks < 2; ++ks) {
          const int u1 = 16 * ks + 8 * h + j, e = ((u1 * r) & 31) * 64;
          const float sc = 1.0f / (2048.0f * 576.0f);
          t4[0][ks][l][j] = c_bf16(TWB.v[2 * e] * sc);
          t4[1][ks][l][j] = c_bf16(-TWB.v[2 * e + 1] * sc);
          t4[2][ks][l][j] = c_bf16(TWB.v[2 * e + 1] * sc);
        }
      }
      for (int rb = 0; rb < 2; ++rb)
        for (int rho = 0; rho < 16; ++rho) {
          const int b = 32 * rb + (rho & 3) + 8 * (rho >> 2) + 4 * h, e = (b * r) & 2047;
          tw[rb][rho >> 1][l][2 * (rho & 1)] = TWB.v[2 * e];
          tw[rb][rho >> 1][l][2 * (rho & 1) + 1] = TWB.v[2 * e + 1];
        }
    }
  }
};
constexpr TwTab<576> TWC = TwTab<576>();
struct IdftPart {
  unsigned short v[8][576][8];
  constexpr IdftPart(int e) : v() {
    for (int kc = 0; kc < 8; ++kc)
      for (int j = 0; j < 4; ++j) {
        const int k = 32 * e + 4 * kc + j;
        int idx = 0;
        for (int d = 0; d < 576; ++d) {
          v[kc][d][2 * j] = c_bf16(k ? 2.f * TWC.v[2 * idx] : 1.f);
          v[kc][d][2 * j + 1] = c_bf16(k ? 2.f * TWC.v[2 * idx + 1] : ((d & 1) ? -1.f : 1.f));
          idx += k; if (idx >= 576) idx -= 576;
        }
      }
  }
};
constexpr IdftPart IP0(0), IP1(1), IP2(2), IP3(3), IP4(4), IP5(5), IP6(6), IP7(7), IP8(8);
__device__ const __attribute__((aligned(16))) IdftPart IDFT[9] = {IP0, IP1, IP2, IP3, IP4, IP5, IP6, IP7, IP8};
static_assert(sizeof(FftTab) == 49152, "table is moved in whole 1 KB pieces");
__device__ const __attribute__((aligned(16))) FftTab FTAB = FftTab();
#else
__device__ const __attribute__((aligned(16))) char IDFT[663552] = {};
__device__ const __attribute__((aligned(16))) char FTAB[49152] = {};
#endif

struct cf { float x, y; };
__device__ __forceinline__ cf cadd(cf a, cf b) { return {a.x + b.x, a.y + b.y}; }
__device__ __forceinline__ cf csub(cf a, cf b) { return {a.x - b.x, a.y - b.y}; }
__device__ __forceinline__ cf cmul(cf a, cf w) { return {__builtin_fmaf(a.x, w.x, -a.y * w.y), __builtin_fmaf(a.x, w.y, a.y * w.x)}; }
__device__ __forceinline__ cf cmulc(cf a, cf w) { return {__builtin_fmaf(a.x, w.x, a.y * w.y), __builtin_fmaf(a.y, w.x, -a.x * w.y)}; }
__device__ __forceinline__ cf ldtw(const float* t, int m) { f32x2 v = *(const f32x2*)(t + 2 * m); return {v.x, v.y}; }

__device__ __forceinline__ float bflo(u32 v) { return __uint_as_float(v << 16); }
__device__ __forceinline__ float bfhi(u32 v) { return __uint_as_float(v & 0xffff0000u); }
__device__ __forceinline__ u32 pack_bf16(float a, float b) {
  typedef __attribute__((ext_vector_type(2))) __bf16 bf2;
  f32x2 v = {a, b};
  return __builtin_bit_cast(u32, __builtin_convertvector(v, bf2));
}
__device__ __forceinline__ float rbf(float a) { return bflo(pack_bf16(a, a)); }

template <int CTRL> __device__ __forceinline__ float dppf(float v) {
  return __int_as_float(__builtin_amdgcn_update_dpp(0, __float_as_int(v), CTRL, 0xf, 0xf, true));
}
constexpr int DPP_X1 = 0xB1, DPP_X2 = 0x4E, DPP_X7 = 0x141, DPP_X8 = 0x128;
__device__ __forceinline__ float xor16f(float v) { return __int_as_float(__builtin_amdgcn_ds_swizzle(__float_as_int(v), 0x401F)); }
__device__ __forceinline__ float half_sum(float v) {
  v += dppf<DPP_X1>(v); v += dppf<DPP_X2>(v); v += dppf<DPP_X7>(v); v += dppf<DPP_X8>(v); v += xor16f(v); return v;
}

__device__ __forceinline__ u32x4 make_srd(const void* p, u32 bytes) {
  uint64_t a = (uint64_t)p; u32x4 r;
  r.x = __builtin_amdgcn_readfirstlane((u32)a);
  r.y = __builtin_amdgcn_readfirstlane((u32)(a >> 32)) & 0xffffu;
  r.z = bytes; r.w = 0x00020000u; return r;
}
__device__ __forceinline__ void dma16(u32 ldsaddr, u32 voff, u32x4 srd, u32 soff) {
  asm volatile("s_mov_b32 m0, %0\n\ts_nop 0\n\tbuffer_load_dwordx4 %1, %2, %3 offen lds" ::"s"(ldsaddr), "v"(voff), "s"(srd), "s"(soff) : "memory");
}
__device__ __forceinline__ void dma16n(u32 ldsaddr, u32 voff, u32x4 srd, u32 soff) {
  asm volatile("s_mov_b32 m0, %0\n\ts_nop 0\n\tbuffer_load_dwordx4 %1, %2, %3 offen nt lds" ::"s"(ldsaddr), "v"(voff), "s"(srd), "s"(soff) : "memory");
}
template <int OFF> __device__ __forceinline__ u32x2 trd(u32 addr) {
  u32x2 r; asm volatile("ds_read_b64_tr_b16 %0, %1 offset:%2" : "=v"(r) : "v"(addr), "n"(OFF) : "memory"); return r;
}
template <int OFF> __device__ __forceinline__ u32x4 rd128(u32 addr) {
  u32x4 r; asm volatile("ds_read_b128 %0, %1 offset:%2" : "=v"(r) : "v"(addr), "n"(OFF) : "memory"); return r;
}
__device__ __forceinline__ f32x4 mfma16(u32x4 a, u32x4 b, f32x4 c) {
  return __builtin_amdgcn_mfma_f32_16x16x32_bf16(__builtin_bit_cast(bf16x8, a), __builtin_bit_cast(bf16x8, b), c, 0, 0, 0);
}

__device__ __forceinline__ f32x16 mfma32(u32x4 a, u32x4 b, f32x16 c) {
  return __builtin_amdgcn_mfma_f32_32x32x16_bf16(__builtin_bit_cast(bf16x8, a), __builtin_bit_cast(bf16x8, b), c, 0, 0, 0);
}
typedef unsigned long long u64;
typedef GAS u64 gu64;
constexpr int G1_WGS = 216, FFT_WGS = 37;
constexpr int L_W = 0;
constexpr int L_X = 81920;
constexpr int L_ZC = 0, L_HROW = 18432, L_WTS = 28160, L_SYNC = 44544, L_PF1 = 44608, K1_LDS = 44864;
constexpr int L_EX = 3 * 16384;
constexpr int FFT_TAB = 0, FFT_WAVE = 49152, FFT_WAVE_B = 9216, FFT_ZS = 49152;
constexpr int L_PF = 155648;
constexpr int MK_LDS = 156160;
constexpr int PF1 = 8, PFW2N = 9;
constexpr u32 K1_CODE = 9728, K2_CODE = 38912;
constexpr unsigned SPIN_CAP = 1u << 16;

#define BAR() asm volatile("s_barrier" ::: "memory")
#define WAITV(n) asm volatile("s_waitcnt vmcnt(%0)" ::"n"(n) : "memory")
#define WL(n) do { asm volatile("s_waitcnt lgkmcnt(" #n ")" ::: "memory"); SB0; } while (0)

__device__ __forceinline__ void dma16s(u32 ldsaddr, u32 voff, u32x4 srd, u32 soff) {
  asm volatile("s_mov_b32 m0, %0\n\ts_nop 0\n\tbuffer_load_dwordx4 %1, %2, %3 offen sc1 lds" ::"s"(ldsaddr), "v"(voff), "s"(srd), "s"(soff) : "memory");
}
__device__ __forceinline__ void touch(u32 ldsaddr, u32 voff, u32x4 srd, u32 soff) {
  asm volatile("s_mov_b32 m0, %0\n\ts_nop 0\n\tbuffer_load_dword %1, %2, %3 offen lds" ::"s"(ldsaddr), "v"(voff), "s"(srd), "s"(soff) : "memory");
}
typedef __amdgpu_buffer_rsrc_t rsrc_t;
__device__ __forceinline__ void st16(rsrc_t r, u32 off, u32x4 v) { __builtin_amdgcn_raw_buffer_store_b128(v, r, off, 0, 16); }
__device__ __forceinline__ void st8(rsrc_t r, u32 off, u32x2 v) { __builtin_amdgcn_raw_buffer_store_b64(v, r, off, 0, 16); }
__device__ __forceinline__ u32x4 ld16(rsrc_t r, u32 off) { return __builtin_amdgcn_raw_buffer_load_b128(r, off, 0, 16); }
__device__ __forceinline__ u32 ld4(rsrc_t r, u32 off) { return __builtin_amdgcn_raw_buffer_load_b32(r, off, 0, 16); }
typedef GAS u32 gu32;
__device__ __forceinline__ void set_flag(char* ws, size_t off) { __hip_atomic_store((gu32*)(ws + off), 1u, __ATOMIC_RELAXED, __HIP_MEMORY_SCOPE_AGENT); }
__device__ __forceinline__ void k1st(rsrc_t r, u32 off, u32x4 v) { __builtin_amdgcn_raw_buffer_store_b128(v, r, off, 0, 0); }
__device__ __forceinline__ unsigned lds_arrive(char* smem, int ctr, int lane) {
  unsigned old = 0;
  asm volatile("s_waitcnt lgkmcnt(0)" ::: "memory");
  if (lane == 0) old = __hip_atomic_fetch_add((LAS unsigned*)(smem + L_SYNC + 4 * ctr), 1u, __ATOMIC_RELAXED, __HIP_MEMORY_SCOPE_WORKGROUP);
  return __builtin_amdgcn_readfirstlane(old);
}
__device__ __forceinline__ void lds_wait(char* smem, int ctr, unsigned n) {
  for (unsigned sp = 0; sp < (1u << 22); ++sp) {
    if (__builtin_amdgcn_readfirstlane(__hip_atomic_load((LAS unsigned*)(smem + L_SYNC + 4 * ctr), __ATOMIC_RELAXED, __HIP_MEMORY_SCOPE_WORKGROUP)) >= n) break;
    __builtin_amdgcn_s_sleep(1);
  }
  asm volatile("" ::: "memory");
}
__device__ __forceinline__ void wait_lanes(char* ws, u32 off, u32 want) {
  bool ok = off == ~0u;
  for (unsigned sp = 0; sp < SPIN_CAP; ++sp) {
    if (!ok) ok = __hip_atomic_load((gu32*)(ws + off), __ATOMIC_RELAXED, __HIP_MEMORY_SCOPE_AGENT) >= want;
    if (__all(ok)) break;
    __builtin_amdgcn_s_sleep(1);
  }
}
__device__ __forceinline__ bool look_lanes(char* ws, u32 off, u32 want) {
  bool ok = off == ~0u;
  if (!ok) ok = __hip_atomic_load((gu32*)(ws + off), __ATOMIC_RELAXED, __HIP_MEMORY_SCOPE_AGENT) >= want;
  return __all(ok);
}

__device__ __forceinline__ u32 w1_line(const int xg, const int n) {
  const int t = n / 640, r = n - t * 640, k = r / 10, c = r - k * 10;
  const int col = ((xg * 27) / 6) * 256 + c * 128;
  return (t < 32 && col < F * 2) ? (u32)((8 * (t & 7) + 512 * (t >> 3) + (k >> 3) + 64 * (k & 7)) * (F * 2) + col) : 0xfffffff0u;
}
__device__ __forceinline__ void dft3(cf& a, cf& b, cf& c) {
  const float h = 0.8660254037844386f;
  cf s = cadd(b, c), d = csub(b, c);
  cf m = {__builtin_fmaf(-0.5f, s.x, a.x), __builtin_fmaf(-0.5f, s.y, a.y)};
  a = cadd(a, s);
  b = {__builtin_fmaf(h, d.y, m.x), __builtin_fmaf(-h, d.x, m.y)};
  c = {__builtin_fmaf(-h, d.y, m.x), __builtin_fmaf(h, d.x, m.y)};
}
template <int CTRL> __device__ __forceinline__ cf bfly_dpp(cf v, float sg) {
  return {__builtin_fmaf(dppf<CTRL>(v.x), sg, v.x), __builtin_fmaf(dppf<CTRL>(v.y), sg, v.y)};
}

__device__ __forceinline__ void phase1_rows(char* smem, const u16* __restrict__ x, char* ws, rsrc_t rw, const int bid, const int tid) {
  const int lane = tid & 63, wave = tid >> 6;
  cf* zc = (cf*)(smem + L_ZC);
  char* hrow = smem + L_HROW;
  const int p = lane & 31, r = 2 * wave + (lane >> 5);
  const int l = p ^ ((p & 4) ? 3 : 0);
  const u32* xr = (const u32*)(x + (size_t)((bid & 63) + 64 * (8 * (bid >> 6) + r)) * D);
  u32 raw[9];
#pragma unroll
  for (int j = 0; j < 9; ++j) raw[j] = __builtin_nontemporal_load(xr + l + 32 * j);
  cf tw1[9];
#pragma unroll
  for (int k2 = 1; k2 < 9; ++k2) tw1[k2] = ldtw(TW576.v, 2 * l * k2);
  cf ts4 = ldtw(TW576.v, 18 * (l & 15)), ts3 = ldtw(TW576.v, 36 * (l & 7)), ts2 = ldtw(TW576.v, 72 * (l & 3));
  BAR();
  float e[9], o[9];
#pragma unroll
  for (int j = 0; j < 9; ++j) { e[j] = bflo(raw[j]); o[j] = bfhi(raw[j]); }
#pragma unroll
  for (int pass = 0; pass < 2; ++pass) {
    float s = 0;
#pragma unroll
    for (int j = 0; j < 9; ++j) s += e[j] + o[j];
    const float mean = half_sum(s) * (1.0f / D);
    float q = 0;
#pragma unroll
    for (int j = 0; j < 9; ++j) { const float a = e[j] - mean, b = o[j] - mean; q = __builtin_fmaf(a, a, q); q = __builtin_fmaf(b, b, q); }
    const float var = half_sum(q) * (1.0f / D);
    const float m = rbf(mean);
    const float inv = __builtin_amdgcn_rcpf(rbf(__builtin_sqrtf(rbf(rbf(var) + 1e-5f))));
#pragma unroll
    for (int j = 0; j < 9; ++j) {
      const u32 d = pack_bf16(e[j] - m, o[j] - m);
      const u32 n = pack_bf16(bflo(d) * inv, bfhi(d) * inv);
      if (pass == 0) { e[j] = bflo(n); o[j] = bfhi(n); }
      else *(u32*)(hrow + r * 1216 + (l + 32 * j) * 4) = n;
    }
  }
  lds_arrive(smem, 2, lane);
  cf z[9];
#pragma unroll
  for (int j = 0; j < 9; ++j) z[j] = {e[j], o[j]};
  dft3(z[0], z[3], z[6]); dft3(z[1], z[4], z[7]); dft3(z[2], z[5], z[8]);
  {
    const cf w1 = {0.766044443118978f, -0.6427876096865393f}, w2 = {0.17364817766693041f, -0.984807753012208f}, w4 = {-0.9396926207859083f, -0.3420201433256689f};
    z[4] = cmul(z[4], w1); z[7] = cmul(z[7], w2); z[5] = cmul(z[5], w2); z[8] = cmul(z[8], w4);
  }
  dft3(z[0], z[1], z[2]); dft3(z[3], z[4], z[5]); dft3(z[6], z[7], z[8]);
  cf y[9];
#pragma unroll
  for (int c = 0; c < 3; ++c)
#pragma unroll
    for (int d = 0; d < 3; ++d) y[c + 3 * d] = z[3 * c + d];
#pragma unroll
  for (int k2 = 1; k2 < 9; ++k2) y[k2] = cmul(y[k2], tw1[k2]);
  {
    const bool u4 = l & 16, u3 = l & 8, u2 = l & 4, u1 = l & 2, u0 = l & 1;
    const float s4 = u4 ? -1.f : 1.f, s3 = u3 ? -1.f : 1.f, s2 = u2 ? -1.f : 1.f, s1 = u1 ? -1.f : 1.f, s0 = u0 ? 1.f : -1.f;
    ts4 = u4 ? cf{-ts4.x, -ts4.y} : cf{1.f, 0.f};
    ts3 = u3 ? cf{-ts3.x, -ts3.y} : cf{1.f, 0.f};
    ts2 = u2 ? cf{-ts2.x, -ts2.y} : cf{1.f, 0.f};
    const cf ts1 = u1 ? (u0 ? cf{0.f, -1.f} : cf{-1.f, 0.f}) : (u0 ? cf{-1.f, 0.f} : cf{1.f, 0.f});
#pragma unroll
    for (int k = 0; k < 9; ++k) { cf t = {__builtin_fmaf(xor16f(y[k].x), s4, y[k].x), __builtin_fmaf(xor16f(y[k].y), s4, y[k].y)}; y[k] = cmul(t, ts4); }
#define BFLY9(CTRL, SG) asm volatile("s_nop 1\n\t" \
      "v_fmac_f32_dpp %0, %0, %18 " CTRL "\n\tv_fmac_f32_dpp %1, %1, %18 " CTRL "\n\tv_fmac_f32_dpp %2, %2, %18 " CTRL "\n\t" \
      "v_fmac_f32_dpp %3, %3, %18 " CTRL "\n\tv_fmac_f32_dpp %4, %4, %18 " CTRL "\n\tv_fmac_f32_dpp %5, %5, %18 " CTRL "\n\t" \
      "v_fmac_f32_dpp %6, %6, %18 " CTRL "\n\tv_fmac_f32_dpp %7, %7, %18 " CTRL "\n\tv_fmac_f32_dpp %8, %8, %18 " CTRL "\n\t" \
      "v_fmac_f32_dpp %9, %9, %18 " CTRL "\n\tv_fmac_f32_dpp %10, %10, %18 " CTRL "\n\tv_fmac_f32_dpp %11, %11, %18 " CTRL "\n\t" \
      "v_fmac_f32_dpp %12, %12, %18 " CTRL "\n\tv_fmac_f32_dpp %13, %13, %18 " CTRL "\n\tv_fmac_f32_dpp %14, %14, %18 " CTRL "\n\t" \
      "v_fmac_f32_dpp %15, %15, %18 " CTRL "\n\tv_fmac_f32_dpp %16, %16, %18 " CTRL "\n\tv_fmac_f32_dpp %17, %17, %18 " CTRL \
      : "+v"(y[0].x), "+v"(y[0].y), "+v"(y[1].x), "+v"(y[1].y), "+v"(y[2].x), "+v"(y[2].y), "+v"(y[3].x), "+v"(y[3].y), "+v"(y[4].x), "+v"(y[4].y), \
        "+v"(y[5].x), "+v"(y[5].y), "+v"(y[6].x), "+v"(y[6].y), "+v"(y[7].x), "+v"(y[7].y), "+v"(y[8].x), "+v"(y[8].y) : "v"(SG))
    BFLY9("row_ror:8 row_mask:0xf bank_mask:0xf", s3);
#pragma unroll
    for (int k = 0; k < 9; ++k) y[k] = cmul(y[k], ts3);
    BFLY9("row_half_mirror row_mask:0xf bank_mask:0xf", s2);
#pragma unroll
    for (int k = 0; k < 9; ++k) y[k] = cmul(y[k], ts2);
    BFLY9("quad_perm:[2,3,0,1] row_mask:0xf bank_mask:0xf", s1);
#pragma unroll
    for (int k = 0; k < 9; ++k) y[k] = cmul(y[k], ts1);
    BFLY9("quad_perm:[1,0,3,2] row_mask:0xf bank_mask:0xf", s0);
  }
  {
    const int k1 = __builtin_bitreverse32((u32)l) >> 27;
#pragma unroll
    for (int k2 = 0; k2 < 9; ++k2) zc[r * 288 + 9 * k1 + k2] = y[k2];
  }
  lds_arrive(smem, 3, lane);
}
template <int NT> __device__ __forceinline__ void phase1_unpack(char* smem, rsrc_t rw, const int bid, const int t) {
  constexpr int NR = (290 + NT - 1) / NT;
  const cf* zc = (const cf*)(smem + L_ZC);
  cf wk[NR];
#pragma unroll
  for (int rd = 0; rd < NR; ++rd) wk[rd] = ldtw(TW576.v, (t + NT * rd) >> 1);
  lds_wait(smem, 3, 4);
  const u32 zo = (u32)WS_Z + ((bid & 63) * 4 + (bid >> 6)) * 128;
#pragma unroll
  for (int rd = 0; rd < NR; ++rd) {
    const int it = t + NT * rd, k = it >> 1, rq = it & 1;
    if (k <= 144) {
      u32x4 v0, v1;
#pragma unroll
      for (int e2 = 0; e2 < 4; ++e2) {
        const int row = 4 * rq + e2;
        const cf a = zc[row * 288 + k], b = zc[row * 288 + (k ? 288 - k : 0)];
        const cf E = {0.5f * (a.x + b.x), 0.5f * (a.y - b.y)};
        const cf O = {0.5f * (a.y + b.y), 0.5f * (b.x - a.x)};
        const cf T = cmul(O, wk[rd]);
        v0[e2] = pack_bf16(E.x + T.x, E.y + T.y);
        v1[e2] = pack_bf16(E.x - T.x, T.y - E.y);
      }
      k1st(rw, zo + (k >> 2) * 32768 + (k & 3) * 32 + rq * 16, v0);
      k1st(rw, zo + ((288 - k) >> 2) * 32768 + ((288 - k) & 3) * 32 + rq * 16, v1);
    }
  }
}

__device__ __forceinline__ void phase1_h(char* smem, rsrc_t rw, const int bid, const int L, const int lane) {
  const u32 hb = (u32)(size_t)(smem + L_HROW);
  const int g = lane >> 4, q = (lane & 15) >> 2, pp = lane & 3;
  const u32 a = hb + q * 1216 + (320 * L + 16 * g + 4 * pp) * 2;
  u32x2 t[10];
  asm volatile("ds_read_b64_tr_b16 %0, %10\n\tds_read_b64_tr_b16 %1, %10 offset:4864\n\tds_read_b64_tr_b16 %2, %10 offset:128\n\tds_read_b64_tr_b16 %3, %10 offset:4992\n\t"
               "ds_read_b64_tr_b16 %4, %10 offset:256\n\tds_read_b64_tr_b16 %5, %10 offset:5120\n\tds_read_b64_tr_b16 %6, %10 offset:384\n\tds_read_b64_tr_b16 %7, %10 offset:5248\n\t"
               "ds_read_b64_tr_b16 %8, %10 offset:512\n\tds_read_b64_tr_b16 %9, %10 offset:5376\n\ts_waitcnt lgkmcnt(0)"
               : "=&v"(t[0]), "=&v"(t[1]), "=&v"(t[2]), "=&v"(t[3]), "=&v"(t[4]), "=&v"(t[5]), "=&v"(t[6]), "=&v"(t[7]), "=&v"(t[8]), "=&v"(t[9]) : "v"(a) : "memory");
#pragma unroll
  for (int i = 0; i < 5; ++i)
    if (i < 4 || L == 0) st16(rw, (u32)WS_HB + (bid * D + 320 * L + 64 * i + lane) * 16, (u32x4){t[2 * i].x, t[2 * i].y, t[2 * i + 1].x, t[2 * i + 1].y});
}
__device__ __forceinline__ void phase1_wt_load(char* stg, const u16* __restrict__ w, const int unit, const int lane) {
  const u32x4 sr = make_srd(w, S * DF * 2);
  const u32 base = (u32)(size_t)stg;
  const u32 row0 = (u32)(2 * (unit & 15) + 128 * (unit >> 4)) * (DF * 2);
#pragma unroll
  for (int e = 0; e < 4; ++e)
#pragma unroll
    for (int j = 0; j < 2; ++j)
      dma16n(base + e * 2048 + j * 1024, lane * 16, sr, row0 + e * (32 * DF * 2) + j * 1024);
}
__device__ __forceinline__ void phase1_wt(char* stg, rsrc_t rw, const int unit, const int H, const int lane) {
  const u16* pr = (const u16*)stg; const u16* pi = (const u16*)(stg + 8192);
  const int i = lane & 1;
  const u32 dst = (u32)WS_WT + unit * 128 + (lane & 7) * 16 + ((lane >> 3) + 40 * H) * 32768;
#pragma unroll
  for (int rd = 0; rd < 5; ++rd) {
    const int k = 160 * H + 32 * rd + (lane >> 1);
    if (k < DF) {
      u32x4 v;
#pragma unroll
      for (int e = 0; e < 4; ++e) { const int idx = e * 1024 + i * DF + k; v[e] = (u32)pr[idx] | ((u32)pi[idx] << 16); }
      st16(rw, dst + rd * (8 * 32768), v);
    }
  }
}

extern "C" __global__ __launch_bounds__(512) void k1_rows(const u16* __restrict__ x, const u16* __restrict__ wr, const u16* __restrict__ wi, const u16* __restrict__ w1, const u16* __restrict__ w2, const u16* __restrict__ out, char* ws) {
  __shared__ __attribute__((aligned(16))) char smem[K1_LDS];
  const int bid = blockIdx.x;
  const int tid = threadIdx.x, lane = tid & 63;
  const int wave = __builtin_amdgcn_readfirstlane(tid >> 6);
  const int L = wave & 1;
  const rsrc_t rw = __builtin_amdgcn_make_buffer_rsrc(ws, 0, (int)WS_END, 0x00020000);
  if (wave < 4) {
    phase1_rows(smem, x, ws, rw, bid, tid);
  } else if (wave < 6) {
    if (bid < 21 && wave == 4) *(u32*)(ws + WS_FG + bid * 256 + lane * 4) = 0u;
    {
      const int n = ((bid >> 3) * 2 + L) * 64 + lane;
      u64 c1, c2;
      asm volatile("s_getpc_b64 vcc\n\ts_add_u32 vcc_lo, vcc_lo, k1_rows@rel32@lo+4\n\ts_addc_u32 vcc_hi, vcc_hi, k1_rows@rel32@hi+12\n\ts_mov_b64 %0, vcc" : "=s"(c1) :: "vcc", "scc");
      asm volatile("s_getpc_b64 vcc\n\ts_add_u32 vcc_lo, vcc_lo, k2_fused@rel32@lo+4\n\ts_addc_u32 vcc_hi, vcc_hi, k2_fused@rel32@hi+12\n\ts_mov_b64 %0, vcc" : "=s"(c2) :: "vcc", "scc");
      const int w = (bid >> 3) * 2 + L;
      const u32 pad = (u32)(size_t)smem + L_PF1;
      if (w < 2) touch(pad, n * 128, make_srd((const void*)c1, K1_CODE), 0);
      else if (w < 8) touch(pad, (n - 128) * 128, make_srd((const void*)c2, K2_CODE), 0);
      else if (w < 14) touch(pad, (n - 512) * 128, make_srd(&FTAB, 49152), 0);
      else if (w < 40) {
        const int kq = (bid & 7) >> 1;
        touch(pad, (u32)(2 * kq * 73728 + (n - 896) * 128), make_srd(IDFT, (u32)((2 * kq + (kq == 3 ? 3 : 2)) * 73728)), 0);
      }
    }
    BAR();
    {
      const u32x4 srdW1 = make_srd(w1, (u32)((size_t)S * F * 2));
#pragma unroll 1
      for (int n = ((bid >> 3) * 2 + L) * 64; n < PF1 * 640; n += 4096) touch((u32)(size_t)smem + L_PF1, w1_line(bid & 7, n + lane), srdW1, 0);
    }
    lds_wait(smem, 2, 4);
    phase1_h(smem, rw, bid, L, lane);
  } else {
    if (wave == 7 && lane < 16) *(u32*)(smem + L_SYNC + lane * 4) = 0u;
    {
      constexpr u32 TLBG = 2097152u;
      const u32 pad = (u32)(size_t)smem + L_PF1;
      const u64 o0 = (u64)(((bid >> 3) * 2 + L) * 64) * TLBG;
      const u32 o = (u32)o0 + lane * TLBG;
      if (o0 < WS_END) touch(pad, o, make_srd(ws, (u32)WS_END), 0);
      if (o0 < (u64)S * F * 2) { touch(pad, o, make_srd(w1, S * F * 2), 0); touch(pad, o, make_srd(w2, S * F * 2), 0); }
      if (o0 < (u64)S * D * 2) touch(pad, o, make_srd(out, S * D * 2), 0);
    }
    phase1_wt_load(smem + L_WTS + L * 8192, L ? wi : wr, bid, lane);
    asm volatile("s_waitcnt lgkmcnt(0)\n\ts_barrier" ::: "memory");
    WAITV(0);
    lds_arrive(smem, 6, lane);
    lds_wait(smem, 6, 2);
    phase1_wt(smem + L_WTS, rw, bid, L, lane);
  }
  if (tid < 320) phase1_unpack<320>(smem, rw, bid, tid);
}

__device__ __forceinline__ void pack_frags(const f32x16& v, u32x4& f0, u32x4& f1) {
  f0 = (u32x4){pack_bf16(v[0], v[1]), pack_bf16(v[2], v[3]), pack_bf16(v[4], v[5]), pack_bf16(v[6], v[7])};
  f1 = (u32x4){pack_bf16(v[8], v[9]), pack_bf16(v[10], v[11]), pack_bf16(v[12], v[13]), pack_bf16(v[14], v[15])};
}
template <int INV> __device__ __forceinline__ void dft64(const char* tab, const int lane, f32x16 (&re)[2], f32x16 (&im)[2]) {
  u32x4 br[4], bi[4], bn[4];
  pack_frags(re[0], br[0], br[1]); pack_frags(re[1], br[2], br[3]);
  pack_frags(im[0], bi[0], bi[1]); pack_frags(im[1], bi[2], bi[3]);
#pragma unroll
  for (int m = 0; m < 4; ++m) bn[m] = (INV ? br[m] : bi[m]) ^ 0x80008000u;
  const u32x4* t2 = (const u32x4*)(tab + 8192) + lane;
#pragma unroll
  for (int ub = 0; ub < 2; ++ub) {
    f32x16 xr = {}, xi = {};
#pragma unroll
    for (int m = 0; m < 4; ++m) {
      const u32x4 fr = t2[(ub * 4 + m) * 64], fi = t2[(8 + ub * 4 + m) * 64];
      xr = mfma32(fr, br[m], xr);
      xi = mfma32(fr, bi[m], xi);
      xr = mfma32(fi, INV ? bi[m] : bn[m], xr);
      xi = mfma32(fi, INV ? bn[m] : br[m], xi);
    }
    re[ub] = xr; im[ub] = xi;
  }
}
template <int INV> __device__ __forceinline__ void twid(const char* tab, const int lane, f32x16 (&re)[2], f32x16 (&im)[2]) {
  const f32x4* tw = (const f32x4*)(tab + 32768) + lane;
#pragma unroll
  for (int rb = 0; rb < 2; ++rb)
#pragma unroll
    for (int p = 0; p < 8; ++p) {
      const f32x4 w = tw[(rb * 8 + p) * 64];
      cf a = {re[rb][2 * p], im[rb][2 * p]}, b = {re[rb][2 * p + 1], im[rb][2 * p + 1]};
      a = INV ? cmulc(a, cf{w.x, w.y}) : cmul(a, cf{w.x, w.y});
      b = INV ? cmulc(b, cf{w.z, w.w}) : cmul(b, cf{w.z, w.w});
      re[rb][2 * p] = a.x; im[rb][2 * p] = a.y; re[rb][2 * p + 1] = b.x; im[rb][2 * p + 1] = b.y;
    }
}
#define WAVE_LDS_FENCE() do { __builtin_amdgcn_fence(__ATOMIC_RELEASE, "wavefront"); __builtin_amdgcn_wave_barrier(); __builtin_amdgcn_fence(__ATOMIC_ACQUIRE, "wavefront"); } while (0)

__device__ __forceinline__ void fft_role(char* smem, char* ws, rsrc_t rw, const int fb, const int wave, const int lane, const u32x4 srdW1) {
  const int kk = fb * 8 + wave, k = kk < DF ? kk : DF - 1;
  const int r = lane & 31, h = lane >> 5;
  {
    const u32x4 srdWS = make_srd(ws, (u32)WS_END), srdT = make_srd(&FTAB, 49152);
    const u32 ldsb = (u32)(size_t)smem;
#pragma unroll
    for (int j = 0; j < 8; ++j) {
      const int q = lane ^ (2 * j);
      dma16(ldsb + FFT_ZS + (wave * 8 + j) * 1024, (u32)(((q & 1) * 32 + (q >> 1)) * 16), srdWS, (u32)WS_Z + (u32)fb * 65536 + (wave * 8 + j) * 1024);
    }
#pragma unroll
    for (int j = 0; j < 6; ++j) dma16(ldsb + FFT_TAB + (wave * 6 + j) * 1024, lane * 16, srdT, (wave * 6 + j) * 1024);
  }
  WAITV(0); BAR();
  u32x4 wv[2][4];
  {
    const u32 wo = (u32)WS_WT + (k >> 2) * 32768 + (h * 16 + (r >> 1)) * 128 + (k & 3) * 32 + (r & 1) * 16;
#pragma unroll
    for (int ub = 0; ub < 2; ++ub)
#pragma unroll
      for (int g = 0; g < 4; ++g) wv[ub][g] = __builtin_amdgcn_raw_buffer_load_b128(rw, wo + (8 * ub + 2 * g) * 2048, 0, 0);
  }
  {
    const int g8 = fb & 7, ng = g8 < 5 ? 40 : 32;
#pragma unroll 1
    for (int n = PF1 * 640 + ((fb >> 3) * 8 + wave) * 64; n < 32 * 640; n += ng * 64) touch((u32)(size_t)smem + L_PF, w1_line(g8, n + lane), srdW1, 0);
  }
  u32x4 za[2][4];
  {
    const int kl = k - fb * 8;
    const char* zs = smem + FFT_ZS + (kl >> 2) * 32768 + (r >> 1) * 1024;
#pragma unroll
    for (int rb = 0; rb < 2; ++rb)
#pragma unroll
      for (int m = 0; m < 4; ++m) {
        const int c = 2 * h + (m >> 1), slot = (kl & 3) * 2 + (m & 1);
        za[rb][m] = *(const u32x4*)(zs + rb * 16384 + ((((8 * c + slot) * 2 + (r & 1)) ^ (2 * ((r >> 1) & 7))) * 16));
      }
  }
  asm volatile("s_waitcnt lgkmcnt(0)\n\ts_barrier" : "+v"(za[0][0]), "+v"(za[0][1]), "+v"(za[0][2]), "+v"(za[0][3]), "+v"(za[1][0]), "+v"(za[1][1]), "+v"(za[1][2]), "+v"(za[1][3]) :: "memory");
  const char* tab = smem + FFT_TAB;
  char* img = smem + FFT_WAVE + wave * FFT_WAVE_B;
  f32x16 re[2], im[2];
  {
    const u32x4* t1 = (const u32x4*)tab + lane;
    re[0] = f32x16{}; re[1] = f32x16{}; im[0] = f32x16{}; im[1] = f32x16{};
#pragma unroll
    for (int m = 0; m < 4; ++m) {
      const u32x4 fr = t1[m * 64], fi = t1[(4 + m) * 64];
      re[0] = mfma32(za[0][m], fr, re[0]); im[0] = mfma32(za[0][m], fi, im[0]);
      re[1] = mfma32(za[1][m], fr, re[1]); im[1] = mfma32(za[1][m], fi, im[1]);
    }
  }
  twid<0>(tab, lane, re, im);
  dft64<0>(tab, lane, re, im);
#pragma unroll
  for (int ub = 0; ub < 2; ++ub)
#pragma unroll
    for (int rho = 0; rho < 16; ++rho) {
      const cf p = cmul(cf{re[ub][rho], im[ub][rho]}, cf{bflo(wv[ub][rho >> 2][rho & 3]), bfhi(wv[ub][rho >> 2][rho & 3])});
      re[ub][rho] = p.x; im[ub][rho] = p.y;
    }
  dft64<1>(tab, lane, re, im);
  twid<1>(tab, lane, re, im);
#pragma unroll
  for (int rb = 0; rb < 2; ++rb)
#pragma unroll
    for (int g = 0; g < 4; ++g) {
      char* p = img + r * 136 + (32 * rb + 8 * g + 4 * h) * 2;
      *(u32x2*)p = (u32x2){pack_bf16(re[rb][4 * g], re[rb][4 * g + 1]), pack_bf16(re[rb][4 * g + 2], re[rb][4 * g + 3])};
      *(u32x2*)(p + 4352) = (u32x2){pack_bf16(im[rb][4 * g], im[rb][4 * g + 1]), pack_bf16(im[rb][4 * g + 2], im[rb][4 * g + 3])};
    }
  u32x4 va[2][2][2];
  {
    const int blk = (lane >> 4) & 1, q = (lane & 15) >> 2, p = lane & 3;
    const u32 a = (u32)(size_t)img + (8 * h + q) * 136 + (16 * blk + 4 * p) * 2;
    u32x2 t[16];
    asm volatile("s_waitcnt lgkmcnt(0)\n\t"
                 "ds_read_b64_tr_b16 %0, %16\n\tds_read_b64_tr_b16 %1, %16 offset:544\n\tds_read_b64_tr_b16 %2, %16 offset:2176\n\tds_read_b64_tr_b16 %3, %16 offset:2720\n\t"
                 "ds_read_b64_tr_b16 %4, %16 offset:64\n\tds_read_b64_tr_b16 %5, %16 offset:608\n\tds_read_b64_tr_b16 %6, %16 offset:2240\n\tds_read_b64_tr_b16 %7, %16 offset:2784\n\t"
                 "ds_read_b64_tr_b16 %8, %16 offset:4352\n\tds_read_b64_tr_b16 %9, %16 offset:4896\n\tds_read_b64_tr_b16 %10, %16 offset:6528\n\tds_read_b64_tr_b16 %11, %16 offset:7072\n\t"
                 "ds_read_b64_tr_b16 %12, %16 offset:4416\n\tds_read_b64_tr_b16 %13, %16 offset:4960\n\tds_read_b64_tr_b16 %14, %16 offset:6592\n\tds_read_b64_tr_b16 %15, %16 offset:7136\n\t"
                 "s_waitcnt lgkmcnt(0)"
                 : "=&v"(t[0]), "=&v"(t[1]), "=&v"(t[2]), "=&v"(t[3]), "=&v"(t[4]), "=&v"(t[5]), "=&v"(t[6]), "=&v"(t[7]),
                   "=&v"(t[8]), "=&v"(t[9]), "=&v"(t[10]), "=&v"(t[11]), "=&v"(t[12]), "=&v"(t[13]), "=&v"(t[14]), "=&v"(t[15])
                 : "v"(a) : "memory");
#pragma unroll
    for (int i = 0; i < 8; ++i) va[i >> 2][(i >> 1) & 1][i & 1] = (u32x4){t[2 * i].x, t[2 * i].y, t[2 * i + 1].x, t[2 * i + 1].y};
  }
  {
    const u32x4* t4 = (const u32x4*)(tab + 24576) + lane;
    re[0] = f32x16{}; re[1] = f32x16{}; im[0] = f32x16{}; im[1] = f32x16{};
#pragma unroll
    for (int ks = 0; ks < 2; ++ks) {
      const u32x4 gr = t4[ks * 64], gi = t4[(2 + ks) * 64], gn = t4[(4 + ks) * 64];
#pragma unroll
      for (int rb = 0; rb < 2; ++rb) {
        re[rb] = mfma32(va[0][rb][ks], gr, re[rb]); im[rb] = mfma32(va[0][rb][ks], gi, im[rb]);
        re[rb] = mfma32(va[1][rb][ks], gn, re[rb]); im[rb] = mfma32(va[1][rb][ks], gr, im[rb]);
      }
    }
  }
#pragma unroll
  for (int rb = 0; rb < 2; ++rb)
#pragma unroll
    for (int g = 0; g < 4; ++g) {
      char* p = img + r * 144 + (32 * rb + 8 * g + 4 * h) * 2;
      *(u32x2*)p = (u32x2){pack_bf16(re[rb][4 * g], re[rb][4 * g + 1]), pack_bf16(re[rb][4 * g + 2], re[rb][4 * g + 3])};
      *(u32x2*)(p + 4608) = (u32x2){pack_bf16(im[rb][4 * g], im[rb][4 * g + 1]), pack_bf16(im[rb][4 * g + 2], im[rb][4 * g + 3])};
    }
  WAVE_LDS_FENCE();
  if (kk < DF) {
    const int rowRe = (k == DF - 1) ? 1 : 2 * k;
#pragma unroll
    for (int i = 0; i < 4; ++i) st16(rw, (u32)WS_YT + (rowRe * 256 + 64 * i + lane) * 16, *(const u32x4*)(img + (8 * i + (lane >> 3)) * 144 + (lane & 7) * 16));
    if (k > 0 && k < DF - 1) {
#pragma unroll
      for (int i = 0; i < 4; ++i) st16(rw, (u32)WS_YT + ((2 * k + 1) * 256 + 64 * i + lane) * 16, *(const u32x4*)(img + 4608 + (8 * i + (lane >> 3)) * 144 + (lane & 7) * 16));
    }
  }
}

__device__ __forceinline__ u32x2 gelu4(const f32x4 a, const u32x2 bb) {
  const float bv[4] = {bflo(bb.x), bfhi(bb.x), bflo(bb.y), bfhi(bb.y)};
  float r[4];
#pragma unroll
  for (int e = 0; e < 4; ++e) {
    const float u = a[e] + bv[e];
    const float z = u * __builtin_fmaf(u * u, -0.044715f * 2.0f * 0.7978845608028654f * 1.4426950408889634f, -2.0f * 0.7978845608028654f * 1.4426950408889634f);
    r[e] = u * __builtin_amdgcn_rcpf(1.0f + __builtin_amdgcn_exp2f(z));
  }
  return (u32x2){pack_bf16(r[0], r[1]), pack_bf16(r[2], r[3])};
}
__device__ __forceinline__ void g1_store2(const f32x4 a0, const f32x4 a1, const u32x2 bb0, const u32x2 bb1, rsrc_t rw, const u32 off) {
  const u32x2 x = gelu4(a0, bb0), y = gelu4(a1, bb1);
  const auto s0 = __builtin_amdgcn_permlane16_swap(x.x, y.x, false, false);
  const auto s1 = __builtin_amdgcn_permlane16_swap(x.y, y.y, false, false);
  st16(rw, off, (u32x4){s0[0], s1[0], s0[1], s1[1]});
}
__device__ __forceinline__ void count_in(char* ws, size_t off, int lane) {
  if (lane == 0) __hip_atomic_fetch_add((gu32*)(ws + off), 1u, __ATOMIC_RELAXED, __HIP_MEMORY_SCOPE_AGENT);
}

constexpr int g2_owner(int i, int j) { return j < 8 ? (j >> 1) : i; }
template <int Q>
__device__ __forceinline__ void g2_finish(f32x4 (&acc)[2][9], const u16* __restrict__ x, const u16* __restrict__ b2, char* ws, rsrc_t rw, u16* __restrict__ out,
                                          const int tile, const int s0, const int d0, const int wave, const int lane) {
  const int srow = s0 + wave * 32 + (lane & 15), dcol = d0 + 4 * (lane >> 4);
  u32x2 xr[2][9]; float bs[2];
#pragma unroll
  for (int i = 0; i < 2; ++i) {
    bs[i] = bflo((u32)b2[srow + 16 * i]);
#pragma unroll
    for (int j = 0; j < 9; ++j)
      if (g2_owner(i, j) == Q) xr[i][j] = *(const u32x2*)(x + (size_t)(srow + 16 * i) * D + dcol + 16 * j);
  }
  const u32 mine = (u32)WS_P + (u32)(((tile * 4 + Q) * 4 + wave) * 10) * 1024;
#pragma unroll
  for (int j = 0; j < 8; ++j)
    if ((j >> 1) != Q)
      st16(rw, mine + j * 1024 + lane * 16, (u32x4){pack_bf16(acc[0][j].x, acc[0][j].y), pack_bf16(acc[0][j].z, acc[0][j].w), pack_bf16(acc[1][j].x, acc[1][j].y), pack_bf16(acc[1][j].z, acc[1][j].w)});
#pragma unroll
  for (int i = 0; i < 2; ++i)
    if (i != Q) st8(rw, mine + (8 + i) * 1024 + lane * 8, (u32x2){pack_bf16(acc[i][8].x, acc[i][8].y), pack_bf16(acc[i][8].z, acc[i][8].w)});
  WAITV(0);
  if (lane == 0) set_flag(ws, WS_FP + ((tile * 4 + wave) * 4 + Q) * 4);
  wait_lanes(ws, (lane < 4 && lane != Q) ? (u32)WS_FP + ((tile * 4 + wave) * 4 + lane) * 4 : ~0u, 1);
  u32x4 pv[3][2]; u32x2 pe[3];
#pragma unroll
  for (int o = 0; o < 3; ++o) {
    const int src = o < Q ? o : o + 1;
    const u32 base = (u32)WS_P + (u32)(((tile * 4 + src) * 4 + wave) * 10) * 1024;
    pv[o][0] = ld16(rw, base + (2 * Q) * 1024 + lane * 16);
    pv[o][1] = ld16(rw, base + (2 * Q + 1) * 1024 + lane * 16);
    if (Q < 2) pe[o] = __builtin_amdgcn_raw_buffer_load_b64(rw, base + (8 + Q) * 1024 + lane * 8, 0, 16);
  }
#pragma unroll
  for (int i = 0; i < 2; ++i)
#pragma unroll
    for (int j = 0; j < 9; ++j)
      if (g2_owner(i, j) == Q) {
        f32x4 t = acc[i][j];
#pragma unroll
        for (int o = 0; o < 3; ++o) {
          const u32 lo = j < 8 ? pv[o][j & 1][2 * i] : pe[o].x, hi = j < 8 ? pv[o][j & 1][2 * i + 1] : pe[o].y;
          t += (f32x4){bflo(lo), bfhi(lo), bflo(hi), bfhi(hi)};
        }
        const u32x2 xv = xr[i][j];
        const u32x2 o2 = {pack_bf16(t.x + bs[i] + bflo(xv.x), t.y + bs[i] + bfhi(xv.x)), pack_bf16(t.z + bs[i] + bflo(xv.y), t.w + bs[i] + bfhi(xv.y))};
        *(u32x2*)(out + (size_t)(srow + 16 * i) * D + dcol + 16 * j) = o2;
      }
}

struct Geo {
  int f0, d1, ft, dt;
  int kq, s0, d2, tile, dt2, nD, eb;
};

extern "C" __global__ __launch_bounds__(512, 2) void k2_fused(const u16* __restrict__ x,
                                                   const u16* __restrict__ w1, const u16* __restrict__ b1, const u16* __restrict__ w2,
                                                   const u16* __restrict__ b2, char* ws, u16* __restrict__ out) {
  extern __shared__ __attribute__((aligned(16))) char smem[];
  const int bid = blockIdx.x;
  const int tid = threadIdx.x, lane = tid & 63;
  const int wave = __builtin_amdgcn_readfirstlane(tid >> 6);
  const u32 ldsb = (u32)(size_t)smem;
  const rsrc_t rw = __builtin_amdgcn_make_buffer_rsrc(ws, 0, (int)WS_END, 0x00020000);
  const bool isG1 = bid < G1_WGS, isFFT = !isG1 && bid < G1_WGS + FFT_WGS;
  Geo G;
  {
    const int xg = bid & 7, idx = bid >> 3;
    const int lin = xg * 27 + idx;
    G.ft = lin / 6; G.dt = lin - G.ft * 6; G.f0 = G.ft * 128; G.d1 = G.dt * 96;
    G.kq = xg >> 1;
    const int st = (xg & 1) * 8 + (idx >> 2);
    G.dt2 = idx & 3; G.s0 = st * 128; G.d2 = G.dt2 * 144; G.tile = st * 4 + G.dt2;
    G.nD = G.kq == 3 ? 3 : 2; G.eb = 2 * G.kq;
  }
  const int L = wave & 1;
  const int kk = lane >> 4;
  const u32 cs0 = ((lane & 15) ^ (kk << 1)) * 16, cs1 = ((lane & 15) ^ ((kk << 1) | 8)) * 16;
  const u32 mW = ldsb + L_W + L * 8192;
  const u32x4 srdW1 = make_srd(w1, (u32)((size_t)S * F * 2));
  const u32 vW0 = (u32)((64 * kk + 4 * L) * (F * 2) + G.f0 * 2) + cs0, vW1 = (u32)((64 * kk + 4 * L) * (F * 2) + G.f0 * 2) + cs1;
#define G1_W(t, sl) do { const u32 _s = (u32)(8 * ((t) & 7) + 512 * ((t) >> 3)) * (F * 2); \
    _Pragma("unroll") for (int j = 0; j < 8; ++j) dma16(mW + (sl) * 16384 + j * 1024, (j & 2) ? vW1 : vW0, srdW1, _s + ((j >> 1) + 256 * (j & 1)) * (F * 2)); } while (0)

  if (isFFT) {
    fft_role(smem, ws, rw, bid - G1_WGS, wave, lane, srdW1);
    WAITV(0); BAR();
    if (wave == 4 && lane == 0) set_flag(ws, WS_FY + (bid - G1_WGS) * 4);
  }

  if (wave >= 4) {
    const int wd = (wave >> 1) & 1, wf = wave & 1;
    const int fbase = G.f0 + wf * 64 + 32 + 4 * kk;
    const u32 goff = (u32)WS_GB + (u32)((((G.f0 + wf * 64 + 32) >> 3) + (kk & 1) * 2 + (kk >> 1)) * D + G.d1 + wd * 48 + (lane & 15)) * 16;
    const u32 fyoff = (u32)WS_FY + (lane < 4 * G.nD ? 4 * G.eb + lane : 36) * 4;
    if (wave >= 6) {
      if (isG1) {
        const u32x2 bb0 = *(const u32x2*)(b1 + fbase), bb1 = *(const u32x2*)(b1 + fbase + 16);
        if (wave == 7) {
#pragma unroll 1
          for (int i = 0; i < 35; ++i) BAR();
        } else {
          const gu32* fy = (const gu32*)(ws + fyoff);
          if (lane == 0) *(volatile LAS u32*)(LAS char*)(smem + L_PF + 256) = 0u;
          u32 seen = 0;
#pragma unroll 1
          for (int i = 0; i < 35; ++i) {
            BAR();
            if (i == 25 || i == 28 || i == 31) {
              if (i != 25 && __all(seen != 0u) && lane == 0) *(volatile LAS u32*)(LAS char*)(smem + L_PF + 256) = 1u;
              asm volatile("s_waitcnt lgkmcnt(0)" ::: "memory");
              if (i != 31) seen = __hip_atomic_load(fy, __ATOMIC_RELAXED, __HIP_MEMORY_SCOPE_AGENT);
            }
          }
        }
        const f32x4* ex = (const f32x4*)(smem + L_EX) + (wave - 4) * 384 + lane;
#pragma unroll
        for (int j = 0; j < 3; ++j) g1_store2(ex[j * 64], ex[(3 + j) * 64], bb0, bb1, rw, goff + j * 256);
        WAITV(0);
        count_in(ws, WS_FG + (G.ft * 6 + G.dt) * 4, lane);
        asm volatile("s_waitcnt lgkmcnt(0)\n\ts_barrier" ::: "memory");
      }
      return;
    }
    const u32x4 srdWS = make_srd(ws, (u32)WS_END);
    if (isG1) {
      u32 vH[6];
#pragma unroll
      for (int j = 0; j < 6; ++j) { const int p = j * 64 + lane; const int kc = p / 96, dd = p - kc * 96; vH[j] = (u32)WS_HB + (u32)(((4 * L + kc) * D + G.d1 + dd) * 16); }
      const u32 mH = ldsb + L_X + L * 6144;
#define G1_H(t, sl) do { _Pragma("unroll") for (int j = 0; j < 6; ++j) dma16(mH + (sl) * 12288 + j * 1024, vH[j], srdWS, (u32)(t) * (8 * D * 16)); } while (0)
#pragma unroll
      for (int t = 0; t < 4; ++t) { G1_W(t, t); G1_H(t, t); }
      WAITV(42); BAR();
      WAITV(28); BAR(); G1_W(4, 4); G1_H(4, 4);
      WAITV(28); BAR(); G1_W(5, 0); G1_H(5, 0);
      int sl = 1;
#pragma unroll 1
      for (int t = 2; t < 28; ++t) {
        WAITV(28); BAR();
        G1_W(t + 4, sl); G1_H(t + 4, sl);
        sl = sl == 4 ? 0 : sl + 1;
      }
      WAITV(28); BAR();
      WAITV(14); BAR();
      WAITV(0); BAR();
    }
    const u32x4 srdW2 = make_srd(w2, (u32)((size_t)F * S * 2));
    int ln2 = lane; asm volatile("" : "+v"(ln2));
    const int kk2 = ln2 >> 4;
    const u32 vA0 = (u32)((32 * L + kk2) * (S * 2) + G.s0 * 2) + ((ln2 & 15) ^ (kk2 << 1)) * 16, vA1 = (u32)((32 * L + kk2) * (S * 2) + G.s0 * 2) + ((ln2 & 15) ^ ((kk2 << 1) | 8)) * 16;
    u32 vG[9];
#pragma unroll
    for (int j = 0; j < 9; ++j) { const int p = j * 64 + ln2; const int kc = p / 144, dd = p - kc * 144; vG[j] = (u32)WS_GB + (u32)(((4 * L + kc) * D + G.d2 + dd) * 16); }
    const u32 mG = ldsb + L_X + L * 9216;
    const int nD = G.nD, nk = nD + 18;
#define G2_WY(e, sl) do { _Pragma("unroll") for (int j = 0; j < 8; ++j) dma16s(mW + (sl) * 16384 + j * 1024, (j & 2) ? vA1 : vA0, srdWS, (u32)WS_YT + (u32)(e) * (64 * S * 2) + j * (4 * S * 2)); } while (0)
#define G2_WW(t, sl) do { _Pragma("unroll") for (int j = 0; j < 8; ++j) dma16(mW + (sl) * 16384 + j * 1024, (j & 2) ? vA1 : vA0, srdW2, (u32)(t) * (64 * S * 2) + j * (4 * S * 2)); } while (0)
    const u32x4 srdI = make_srd((const char*)IDFT - WS_GB, (u32)(WS_GB + sizeof(IDFT)));
#define G2_GI(e, sl) do { _Pragma("unroll") for (int j = 0; j < 9; ++j) dma16(mG + (sl) * 18432 + j * 1024, vG[j], srdI, (u32)(e) * (8 * D * 16)); } while (0)
#define G2_G(t, sl) do { _Pragma("unroll") for (int j = 0; j < 9; ++j) dma16s(mG + (sl) * 18432 + j * 1024, vG[j], srdWS, (u32)(t) * (8 * D * 16)); } while (0)
    bool early = false;
    if (isG1) {
      const u32x2 bb0 = *(const u32x2*)(b1 + fbase), bb1 = *(const u32x2*)(b1 + fbase + 16);
      BAR(); BAR();
      early = __builtin_amdgcn_readfirstlane(*(volatile LAS u32*)(LAS char*)(smem + L_PF + 256)) != 0;
      if (early) { G2_WY(G.eb, 0); G2_WY(G.eb + 1, 1); if (nD == 3) G2_WY(G.eb + 2, 2); }
      G2_GI(G.eb, 0); G2_GI(G.eb + 1, 1);
      if (nD == 3) G2_GI(G.eb + 2, 2);
      BAR();
      const f32x4* ex = (const f32x4*)(smem + L_EX) + L * 384 + lane;
#pragma unroll
      for (int j = 0; j < 3; ++j) g1_store2(ex[j * 64], ex[(3 + j) * 64], bb0, bb1, rw, goff + j * 256);
      WAITV(0);
      count_in(ws, WS_FG + (G.ft * 6 + G.dt) * 4, lane);
      asm volatile("s_waitcnt lgkmcnt(0)\n\ts_barrier" ::: "memory");
    } else {
      G2_GI(G.eb, 0); G2_GI(G.eb + 1, 1);
      if (nD == 3) G2_GI(G.eb + 2, 2);
    }
    const u32 fgoff = lane < 18 ? (u32)WS_FG + ((9 * G.kq + (lane >> 1)) * 6 + ((G.dt2 * 3) >> 1) + (lane & 1)) * 4 : ~0u;
    bool gseen = false;
    if (!early) {
      wait_lanes(ws, fyoff, 1);
      G2_WY(G.eb, 0); G2_WY(G.eb + 1, 1);
      if (nD == 3) G2_WY(G.eb + 2, 2);
      gseen = look_lanes(ws, fgoff, 8);
    }
    WAITV(0); BAR();
    if (!gseen) wait_lanes(ws, fgoff, 8);
    const int t0 = 18 * G.kq - nD;
    if (nD == 2) {
      G2_WW(t0 + 2, 2); G2_G(t0 + 2, 2);
      G2_WW(t0 + 3, 3); G2_G(t0 + 3, 3);
      WAITV(17); BAR();
      G2_WW(t0 + 4, 0); G2_G(t0 + 4, 0);
    } else {
      G2_WW(t0 + 3, 3); G2_G(t0 + 3, 3);
      WAITV(0); BAR();
      G2_WW(t0 + 4, 0); G2_G(t0 + 4, 0);
      G2_WW(t0 + 5, 1); G2_G(t0 + 5, 1);
    }
#pragma unroll 1
    for (int sg = nD; sg < nk - 3; ++sg) {
      WAITV(17); BAR();
      const int sl = (sg + 3) & 3;
      G2_WW(t0 + sg + 3, sl); G2_G(t0 + sg + 3, sl);
    }
    WAITV(17); BAR();
    WAITV(0); BAR();
    return;
  }

  const int lg = lane >> 4, lq = (lane & 15) >> 2, lp = lane & 3;
  const u32 trbase = ldsb + L_W + (u32)((8 * lg + lq) * 256 + (lp & 1) * 8);
  const int swz = (lq << 1) | ((lg & 1) << 3);
  if (isG1) {
    const int wd = (wave >> 1) & 1, wf = wave & 1;
    u32 aA[4];
#pragma unroll
    for (int i = 0; i < 4; ++i) aA[i] = trbase + (u32)(((wf * 8 + 2 * i + (lp >> 1)) ^ swz) * 16);
    const u32 aB = ldsb + L_X + (u32)((lg * 96 + wd * 48 + (lane & 15)) * 16);
    const int fbase = G.f0 + wf * 64 + 4 * lg;
    const u32 goff = (u32)WS_GB + (u32)((((G.f0 + wf * 64) >> 3) + (lg & 1) * 2 + (lg >> 1)) * D + G.d1 + wd * 48 + (lane & 15)) * 16;
    const u32x2 bb0 = *(const u32x2*)(b1 + fbase), bb1 = *(const u32x2*)(b1 + fbase + 16);
    f32x4 acc[4][3];
#pragma unroll
    for (int i = 0; i < 4; ++i)
#pragma unroll
      for (int j = 0; j < 3; ++j) acc[i][j] = (f32x4){0.f, 0.f, 0.f, 0.f};
    u32x4 fb[2][3];
    u32x2 al[2][4], ah[2][4];
    u32 aA3[4];
#pragma unroll
    for (int i = 0; i < 4; ++i) aA3[i] = aA[i] + 3 * 16384;
#define G1_RDB(dst, J, SL, H) dst = rd128<12288 * (SL) + 6144 * (H) + 256 * (J)>(aB)
#define G1_RDA(I, SL, H, T) trd<16384 * ((SL) % 3) + 8192 * (H) + 1024 * (T)>((SL) < 3 ? aA[I] : aA3[I])
#define P1_M(I, J, C) do { acc[I][J] = mfma16((u32x4){al[C][I].x, al[C][I].y, ah[C][I].x, ah[C][I].y}, fb[C][J], acc[I][J]); SB0; } while (0)
#define P1_HALF(C, N, SL, H) do { \
    WL(8); P1_M(0, 0, C); G1_RDB(fb[N][0], 0, SL, H); SB0; \
    WL(8); P1_M(0, 1, C); al[N][0] = G1_RDA(0, SL, H, 0); SB0; \
    WL(8); P1_M(0, 2, C); ah[N][0] = G1_RDA(0, SL, H, 1); SB0; \
    WL(7); P1_M(1, 0, C); G1_RDB(fb[N][1], 1, SL, H); SB0; \
    P1_M(1, 1, C); G1_RDB(fb[N][2], 2, SL, H); SB0; \
    P1_M(1, 2, C); al[N][1] = G1_RDA(1, SL, H, 0); SB0; \
    WL(8); P1_M(2, 0, C); ah[N][1] = G1_RDA(1, SL, H, 1); SB0; \
    P1_M(2, 1, C); al[N][2] = G1_RDA(2, SL, H, 0); SB0; \
    P1_M(2, 2, C); ah[N][2] = G1_RDA(2, SL, H, 1); SB0; \
    WL(9); P1_M(3, 0, C); al[N][3] = G1_RDA(3, SL, H, 0); SB0; \
    P1_M(3, 1, C); ah[N][3] = G1_RDA(3, SL, H, 1); SB0; \
    P1_M(3, 2, C); } while (0)
#define P1_STEP(SL) do { BAR(); SB0; P1_HALF(0, 1, SL, 1); P1_HALF(1, 0, ((SL) + 1) % 5, 0); } while (0)
    asm volatile("s_waitcnt lgkmcnt(0)\n\ts_barrier" ::: "memory");
    G1_RDB(fb[0][0], 0, 0, 0); al[0][0] = G1_RDA(0, 0, 0, 0); ah[0][0] = G1_RDA(0, 0, 0, 1); G1_RDB(fb[0][1], 1, 0, 0); G1_RDB(fb[0][2], 2, 0, 0);
    al[0][1] = G1_RDA(1, 0, 0, 0); ah[0][1] = G1_RDA(1, 0, 0, 1); al[0][2] = G1_RDA(2, 0, 0, 0); ah[0][2] = G1_RDA(2, 0, 0, 1);
    al[0][3] = G1_RDA(3, 0, 0, 0); ah[0][3] = G1_RDA(3, 0, 0, 1);
    P1_STEP(0); P1_STEP(1);
#pragma unroll 1
    for (int t = 0; t < 6; ++t) { P1_STEP(2); P1_STEP(3); P1_STEP(4); P1_STEP(0); P1_STEP(1); }
    asm volatile("s_waitcnt lgkmcnt(0)\n\ts_barrier" ::: "memory");
    SB0;
    f32x4* ex = (f32x4*)(smem + L_EX);
#pragma unroll
    for (int ii = 0; ii < 2; ++ii)
#pragma unroll
      for (int j = 0; j < 3; ++j) ex[(wave * 6 + ii * 3 + j) * 64 + lane] = acc[2 + ii][j];
    asm volatile("s_waitcnt lgkmcnt(0)\n\ts_barrier" ::: "memory");
#pragma unroll
    for (int j = 0; j < 3; ++j) g1_store2(acc[0][j], acc[1][j], bb0, bb1, rw, goff + j * 256);
    WAITV(0);
    count_in(ws, WS_FG + (G.ft * 6 + G.dt) * 4, lane);
    BAR();
    {
      const u32x4 srdW2 = make_srd(w2, (u32)((size_t)F * S * 2));
#pragma unroll 1
      for (int n = ((bid >> 3) * 4 + wave) * 64 + lane; n < PFW2N * 1024; n += 27 * 256)
        touch(ldsb + L_PF, (u32)((1152 * G.kq + (n >> 4)) * (S * 2) + (bid & 1) * 2048 + (n & 15) * 128), srdW2, 0);
    }
  }
  BAR();
  {
    const u32 aW0 = trbase + (u32)(((wave * 4 + (lp >> 1)) ^ swz) * 16), aW1 = trbase + (u32)(((wave * 4 + 2 + (lp >> 1)) ^ swz) * 16);
    const u32 aG = ldsb + L_X + (u32)((lg * 144 + (lane & 15)) * 16);
    f32x4 acc[2][9];
#pragma unroll
    for (int i = 0; i < 2; ++i)
#pragma unroll
      for (int j = 0; j < 9; ++j) acc[i][j] = (f32x4){0.f, 0.f, 0.f, 0.f};
    u32x4 fg[2][9];
    u32x2 wl[2][2], wh[2][2];
    const u32 aG2 = aG + 2 * 18432;
#define G2_RDG(dst, J, SL, H) dst = rd128<18432 * ((SL) & 1) + 9216 * (H) + 256 * (J)>((SL) < 2 ? aG : aG2)
#define G2_RDW(a, SL, H, T) trd<16384 * (SL) + 8192 * (H) + 1024 * (T)>(a)
#define P2_M(I, J, C) do { acc[I][J] = mfma16(fg[C][J], (u32x4){wl[C][I].x, wl[C][I].y, wh[C][I].x, wh[C][I].y}, acc[I][J]); SB0; } while (0)
#define P2_HALF(C, N, SL, H) do { \
    WL(10); P2_M(0, 0, C); wl[N][0] = G2_RDW(aW0, SL, H, 0); SB0; \
    WL(9);  P2_M(1, 0, C); wh[N][0] = G2_RDW(aW0, SL, H, 1); SB0; \
    WL(9);  P2_M(0, 1, C); G2_RDG(fg[N][0], 0, SL, H); SB0; \
            P2_M(1, 1, C); wl[N][1] = G2_RDW(aW1, SL, H, 0); SB0; \
    WL(10); P2_M(0, 2, C); wh[N][1] = G2_RDW(aW1, SL, H, 1); SB0; \
            P2_M(1, 2, C); G2_RDG(fg[N][1], 1, SL, H); SB0; \
    WL(11); P2_M(0, 3, C); G2_RDG(fg[N][2], 2, SL, H); SB0; \
            P2_M(1, 3, C); G2_RDG(fg[N][3], 3, SL, H); SB0; \
    WL(12); P2_M(0, 4, C); G2_RDG(fg[N][4], 4, SL, H); SB0; \
            P2_M(1, 4, C); G2_RDG(fg[N][5], 5, SL, H); SB0; \
    WL(13); P2_M(0, 5, C); G2_RDG(fg[N][6], 6, SL, H); SB0; \
            P2_M(1, 5, C); G2_RDG(fg[N][7], 7, SL, H); SB0; \
    WL(14); P2_M(0, 6, C); G2_RDG(fg[N][8], 8, SL, H); SB0; \
            P2_M(1, 6, C); \
    WL(14); P2_M(0, 7, C); P2_M(1, 7, C); \
    WL(13); P2_M(0, 8, C); P2_M(1, 8, C); } while (0)
#define P2_BODY(SL) do { SB0; P2_HALF(0, 1, SL, 1); P2_HALF(1, 0, ((SL) + 1) & 3, 0); } while (0)
    wl[0][0] = G2_RDW(aW0, 0, 0, 0); wh[0][0] = G2_RDW(aW0, 0, 0, 1); G2_RDG(fg[0][0], 0, 0, 0); wl[0][1] = G2_RDW(aW1, 0, 0, 0); wh[0][1] = G2_RDW(aW1, 0, 0, 1);
    G2_RDG(fg[0][1], 1, 0, 0); G2_RDG(fg[0][2], 2, 0, 0); G2_RDG(fg[0][3], 3, 0, 0); G2_RDG(fg[0][4], 4, 0, 0);
    G2_RDG(fg[0][5], 5, 0, 0); G2_RDG(fg[0][6], 6, 0, 0); G2_RDG(fg[0][7], 7, 0, 0); G2_RDG(fg[0][8], 8, 0, 0);
    P2_BODY(0);
    if (G.nD == 2) BAR();
    P2_BODY(1);
#pragma unroll 1
    for (int it = 0; it < 4; ++it) { BAR(); P2_BODY(2); BAR(); P2_BODY(3); BAR(); P2_BODY(0); BAR(); P2_BODY(1); }
    BAR(); P2_BODY(2);
    if (G.nD == 3) BAR();
    P2_BODY(3);
    if (G.nD == 3) P2_BODY(0);
    asm volatile("s_waitcnt lgkmcnt(0)" ::: "memory");
    SB0;
    switch (G.kq) {
      case 0: g2_finish<0>(acc, x, b2, ws, rw, out, G.tile, G.s0, G.d2, wave, lane); break;
      case 1: g2_finish<1>(acc, x, b2, ws, rw, out, G.tile, G.s0, G.d2, wave, lane); break;
      case 2: g2_finish<2>(acc, x, b2, ws, rw, out, G.tile, G.s0, G.d2, wave, lane); break;
      default: g2_finish<3>(acc, x, b2, ws, rw, out, G.tile, G.s0, G.d2, wave, lane); break;
    }
  }
}

extern "C" void kernel_launch(void* const* d_in, const int* in_sizes, int n_in, void* d_out, int out_size, void* d_ws, size_t ws_size, hipStream_t stream) {
  (void)hipFuncSetAttribute((const void*)k2_fused, hipFuncAttributeMaxDynamicSharedMemorySize, MK_LDS);
  k1_rows<<<256, 512, 0, stream>>>((const u16*)d_in[0], (const u16*)d_in[1], (const u16*)d_in[2], (const u16*)d_in[4], (const u16*)d_in[6], (const u16*)d_out, (char*)d_ws);
  k2_fused<<<256, 512, MK_LDS, stream>>>((const u16*)d_in[0], (const u16*)d_in[4], (const u16*)d_in[5], (const u16*)d_in[6], (const u16*)d_in[7], (char*)d_ws, (u16*)d_out);
}
```

```cpp
#include <hip/hip_runtime.h>
#include <hip/hip_bf16.h>
#include <stdint.h>

typedef unsigned short u16;
typedef unsigned int u32;
using bf16x8 = __attribute__((ext_vector_type(8))) short;
using f32x4 = __attribute__((ext_vector_type(4))) float;
using f32x16 = __attribute__((ext_vector_type(16))) float;
using f32x2 = __attribute__((ext_vector_type(2))) float;
using u32x4 = __attribute__((ext_vector_type(4))) unsigned;
using u32x2 = __attribute__((ext_vector_type(2))) unsigned;
#define LAS __attribute__((address_space(3)))
#define GAS __attribute__((address_space(1)))
#define SB0 __builtin_amdgcn_sched_barrier(0)

constexpr int S = 2048, D = 576, DF = 289, F = 4608;

constexpr size_t WS_HB = 0;
constexpr size_t WS_Z = WS_HB + (size_t)S * D * 2;
constexpr size_t WS_WT = WS_Z + (size_t)74 * 32768;
constexpr size_t WS_GB = WS_WT + (size_t)73 * 32768;
constexpr size_t WS_YT = WS_GB + (size_t)F * D * 2;
constexpr size_t WS_P = WS_YT + (size_t)D * S * 2;
constexpr size_t WS_P_BYTES = (size_t)256 * 4 * 10 * 1024;
constexpr size_t WS_FG = WS_P + WS_P_BYTES;
constexpr size_t WS_FY = WS_FG + 216 * 128;
constexpr size_t WS_FP = WS_FY + 8 * 256;
constexpr size_t WS_END = WS_FP + 256 * 64;

constexpr double kPi = 3.141592653589793238462643383279502884;
constexpr double c_sin_small(double x) { double t = x, s = x; for (int i = 1; i < 14; ++i) { t *= -x * x / ((2 * i) * (2 * i + 1)); s += t; } return s; }
constexpr double c_cos_small(double x) { double t = 1, s = 1; for (int i = 1; i < 14; ++i) { t *= -x * x / ((2 * i - 1) * (2 * i)); s += t; } return s; }
constexpr double c_cos(int m, int n) {
  m %= n; if (2 * m > n) m = n - m;
  if (4 * m > n) return -((8 * (n / 2 - m) > n) ? c_sin_small(2 * kPi * (m - n / 4) / n) : c_cos_small(2 * kPi * (n / 2 - m) / n));
  return (8 * m > n) ? c_sin_small(2 * kPi * (n / 4 - m) / n) : c_cos_small(2 * kPi * m / n);
}
constexpr double c_sin(int m, int n) { return c_cos((m % n) + n - n / 4, n); }
template <int N> struct TwTab { float v[2 * N]; constexpr TwTab() : v() { for (int m = 0; m < N; ++m) { v[2 * m] = (float)c_cos(m, N); v[2 * m + 1] = (float)(-c_sin(m, N)); } } };
__device__ const TwTab<576> TW576 = TwTab<576>();
#if defined(__HIP_DEVICE_COMPILE__)
constexpr TwTab<2048> TWB = TwTab<2048>();
constexpr unsigned short c_bf16(float f) { unsigned u = __builtin_bit_cast(unsigned, f); return (unsigned short)((u + 0x7fffu + ((u >> 16) & 1u)) >> 16); }
struct FftTab {
  unsigned short t1[2][4][64][8];
  unsigned short t2[2][2][4][64][8];
  unsigned short t4[3][2][64][8];
  unsigned short pad[1024];
  float tw[2][8][64][4];
  constexpr FftTab() : t1(), t2(), t4(), pad(), tw() {
    for (int l = 0; l < 64; ++l) {
      const int r = l & 31, h = l >> 5;
      for (int j = 0; j < 8; ++j) {
        for (int m = 0; m < 4; ++m) {
          const int a = 16 * h + 4 * m + (j >> 1), e = ((a * r) & 31) * 64;
          const float cr = TWB.v[2 * e], ci = TWB.v[2 * e + 1];
          t1[0][m][l][j] = c_bf16((j & 1) ? -ci : cr);
          t1[1][m][l][j] = c_bf16((j & 1) ? cr : ci);
          for (int ub = 0; ub < 2; ++ub) {
            const int b = 16 * m + 8 * (j >> 2) + 4 * h + (j & 3), e2 = (((32 * ub + r) * b) & 63) * 32;
            t2[0][ub][m][l][j] = c_bf16(TWB.v[2 * e2]);
            t2[1][ub][m][l][j] = c_bf16(TWB.v[2 * e2 + 1]);
          }
        }
        for (int ks = 0; ks < 2; ++ks) {
          const int u1 = 16 * ks + 8 * h + j, e = ((u1 * r) & 31) * 64;
          const float sc = 1.0f / (2048.0f * 576.0f);
          t4[0][ks][l][j] = c_bf16(TWB.v[2 * e] * sc);
          t4[1][ks][l][j] = c_bf16(-TWB.v[2 * e + 1] * sc);
          t4[2][ks][l][j] = c_bf16(TWB.v[2 * e + 1] * sc);
        }
      }
      for (int rb = 0; rb < 2; ++rb)
        for (int rho = 0; rho < 16; ++rho) {
          const int b = 32 * rb + (rho & 3) + 8 * (rho >> 2) + 4 * h, e = (b * r) & 2047;
          tw[rb][rho >> 1][l][2 * (rho & 1)] = TWB.v[2 * e];
          tw[rb][rho >> 1][l][2 * (rho & 1) + 1] = TWB.v[2 * e + 1];
        }
    }
  }
};
constexpr TwTab<576> TWC = TwTab<576>();
struct IdftPart {
  unsigned short v[8][576][8];
  constexpr IdftPart(int e) : v() {
    for (int kc = 0; kc < 8; ++kc)
      for (int j = 0; j < 4; ++j) {
        const int k = 32 * e + 4 * kc + j;
        int idx = 0;
        for (int d = 0; d < 576; ++d) {
          v[kc][d][2 * j] = c_bf16(k ? 2.f * TWC.v[2 * idx] : 1.f);
          v[kc][d][2 * j + 1] = c_bf16(k ? 2.f * TWC.v[2 * idx + 1] : ((d & 1) ? -1.f : 1.f));
          idx += k; if (idx >= 576) idx -= 576;
        }
      }
  }
};
constexpr IdftPart IP0(0), IP1(1), IP2(2), IP3(3), IP4(4), IP5(5), IP6(6), IP7(7), IP8(8);
__device__ const __attribute__((aligned(16))) IdftPart IDFT[9] = {IP0, IP1, IP2, IP3, IP4, IP5, IP6, IP7, IP8};
static_assert(sizeof(FftTab) == 49152, "table is moved in whole 1 KB pieces");
__device__ const __attribute__((aligned(16))) FftTab FTAB = FftTab();
#else
__device__ const __attribute__((aligned(16))) char IDFT[663552] = {};
__device__ const __attribute__((aligned(16))) char FTAB[49152] = {};
#endif

struct cf { float x, y; };
__device__ __forceinline__ cf cadd(cf a, cf b) { return {a.x + b.x, a.y + b.y}; }
__device__ __forceinline__ cf csub(cf a, cf b) { return {a.x - b.x, a.y - b.y}; }
__device__ __forceinline__ cf cmul(cf a, cf w) { return {__builtin_fmaf(a.x, w.x, -a.y * w.y), __builtin_fmaf(a.x, w.y, a.y * w.x)}; }
__device__ __forceinline__ cf cmulc(cf a, cf w) { return {__builtin_fmaf(a.x, w.x, a.y * w.y), __builtin_fmaf(a.y, w.x, -a.x * w.y)}; }
__device__ __forceinline__ cf ldtw(const float* t, int m) { f32x2 v = *(const f32x2*)(t + 2 * m); return {v.x, v.y}; }

__device__ __forceinline__ float bflo(u32 v) { return __uint_as_float(v << 16); }
__device__ __forceinline__ float bfhi(u32 v) { return __uint_as_float(v & 0xffff0000u); }
__device__ __forceinline__ u32 pack_bf16(float a, float b) {
  typedef __attribute__((ext_vector_type(2))) __bf16 bf2;
  f32x2 v = {a, b};
  return __builtin_bit_cast(u32, __builtin_convertvector(v, bf2));
}
__device__ __forceinline__ float rbf(float a) { return bflo(pack_bf16(a, a)); }

template <int CTRL> __device__ __forceinline__ float dppf(float v) {
  return __int_as_float(__builtin_amdgcn_update_dpp(0, __float_as_int(v), CTRL, 0xf, 0xf, true));
}
constexpr int DPP_X1 = 0xB1, DPP_X2 = 0x4E, DPP_X7 = 0x141, DPP_X8 = 0x128;
__device__ __forceinline__ float xor16f(float v) { return __int_as_float(__builtin_amdgcn_ds_swizzle(__float_as_int(v), 0x401F)); }
__device__ __forceinline__ float half_sum(float v) {
  v += dppf<DPP_X1>(v); v += dppf<DPP_X2>(v); v += dppf<DPP_X7>(v); v += dppf<DPP_X8>(v); v += xor16f(v); return v;
}

__device__ __forceinline__ u32x4 make_srd(const void* p, u32 bytes) {
  uint64_t a = (uint64_t)p; u32x4 r;
  r.x = __builtin_amdgcn_readfirstlane((u32)a);
  r.y = __builtin_amdgcn_readfirstlane((u32)(a >> 32)) & 0xffffu;
  r.z = bytes; r.w = 0x00020000u; return r;
}
__device__ __forceinline__ void dma16(u32 ldsaddr, u32 voff, u32x4 srd, u32 soff) {
  asm volatile("s_mov_b32 m0, %0\n\ts_nop 0\n\tbuffer_load_dwordx4 %1, %2, %3 offen lds" ::"s"(ldsaddr), "v"(voff), "s"(srd), "s"(soff) : "memory");
}
__device__ __forceinline__ void dma16n(u32 ldsaddr, u32 voff, u32x4 srd, u32 soff) {
  asm volatile("s_mov_b32 m0, %0\n\ts_nop 0\n\tbuffer_load_dwordx4 %1, %2, %3 offen nt lds" ::"s"(ldsaddr), "v"(voff), "s"(srd), "s"(soff) : "memory");
}
template <int OFF> __device__ __forceinline__ u32x2 trd(u32 addr) {
  u32x2 r; asm volatile("ds_read_b64_tr_b16 %0, %1 offset:%2" : "=v"(r) : "v"(addr), "n"(OFF) : "memory"); return r;
}
template <int OFF> __device__ __forceinline__ u32x4 rd128(u32 addr) {
  u32x4 r; asm volatile("ds_read_b128 %0, %1 offset:%2" : "=v"(r) : "v"(addr), "n"(OFF) : "memory"); return r;
}
__device__ __forceinline__ f32x4 mfma16(u32x4 a, u32x4 b, f32x4 c) {
  return __builtin_amdgcn_mfma_f32_16x16x32_bf16(__builtin_bit_cast(bf16x8, a), __builtin_bit_cast(bf16x8, b), c, 0, 0, 0);
}

__device__ __forceinline__ f32x16 mfma32(u32x4 a, u32x4 b, f32x16 c) {
  return __builtin_amdgcn_mfma_f32_32x32x16_bf16(__builtin_bit_cast(bf16x8, a), __builtin_bit_cast(bf16x8, b), c, 0, 0, 0);
}
typedef unsigned long long u64;
typedef GAS u64 gu64;
constexpr int G1_WGS = 216, FFT_WGS = 37;
constexpr int L_W = 0;
constexpr int L_X = 81920;
constexpr int L_ZC = 0, L_HROW = 18432, L_WTS = 28160, L_SYNC = 44544, L_PF1 = 44608, K1_LDS = 44864;
constexpr int L_EX = 3 * 16384;
constexpr int FFT_TAB = 0, FFT_WAVE = 49152, FFT_WAVE_B = 9216, FFT_ZS = 49152;
constexpr int L_PF = 155648;
constexpr int MK_LDS = 156160;
constexpr int PF1 = 8, PF2 = 32, PFW2N = 18, PFW2H = 2, POLLS = 4;
constexpr u32 K1_CODE = 9728, K2_CODE = 40960;
constexpr unsigned SPIN_CAP = 1u << 16;

#define BAR() asm volatile("s_barrier" ::: "memory")
#define WAITV(n) asm volatile("s_waitcnt vmcnt(%0)" ::"n"(n) : "memory")
#define WL(n) do { asm volatile("s_waitcnt lgkmcnt(" #n ")" ::: "memory"); SB0; } while (0)

__device__ __forceinline__ void dma16s(u32 ldsaddr, u32 voff, u32x4 srd, u32 soff) {
  asm volatile("s_mov_b32 m0, %0\n\ts_nop 0\n\tbuffer_load_dwordx4 %1, %2, %3 offen sc1 lds" ::"s"(ldsaddr), "v"(voff), "s"(srd), "s"(soff) : "memory");
}
__device__ __forceinline__ void touch(u32 ldsaddr, u32 voff, u32x4 srd, u32 soff) {
  asm volatile("s_mov_b32 m0, %0\n\ts_nop 0\n\tbuffer_load_dword %1, %2, %3 offen lds" ::"s"(ldsaddr), "v"(voff), "s"(srd), "s"(soff) : "memory");
}
typedef __amdgpu_buffer_rsrc_t rsrc_t;
__device__ __forceinline__ void st16(rsrc_t r, u32 off, u32x4 v) { __builtin_amdgcn_raw_buffer_store_b128(v, r, off, 0, 16); }
__device__ __forceinline__ void st8(rsrc_t r, u32 off, u32x2 v) { __builtin_amdgcn_raw_buffer_store_b64(v, r, off, 0, 16); }
__device__ __forceinline__ u32x4 ld16(rsrc_t r, u32 off) { return __builtin_amdgcn_raw_buffer_load_b128(r, off, 0, 16); }
__device__ __forceinline__ u32 ld4(rsrc_t r, u32 off) { return __builtin_amdgcn_raw_buffer_load_b32(r, off, 0, 16); }
typedef GAS u32 gu32;
__device__ __forceinline__ void set_flag(char* ws, size_t off) { __hip_atomic_store((gu32*)(ws + off), 1u, __ATOMIC_RELAXED, __HIP_MEMORY_SCOPE_AGENT); }
__device__ __forceinline__ void k1st(rsrc_t r, u32 off, u32x4 v) { __builtin_amdgcn_raw_buffer_store_b128(v, r, off, 0, 0); }
__device__ __forceinline__ unsigned lds_arrive(char* smem, int ctr, int lane) {
  unsigned old = 0;
  asm volatile("s_waitcnt lgkmcnt(0)" ::: "memory");
  if (lane == 0) old = __hip_atomic_fetch_add((LAS unsigned*)(smem + L_SYNC + 4 * ctr), 1u, __ATOMIC_RELAXED, __HIP_MEMORY_SCOPE_WORKGROUP);
  return __builtin_amdgcn_readfirstlane(old);
}
__device__ __forceinline__ void lds_wait(char* smem, int ctr, unsigned n) {
  for (unsigned sp = 0; sp < (1u << 22); ++sp) {
    if (__builtin_amdgcn_readfirstlane(__hip_atomic_load((LAS unsigned*)(smem + L_SYNC + 4 * ctr), __ATOMIC_RELAXED, __HIP_MEMORY_SCOPE_WORKGROUP)) >= n) break;
    __builtin_amdgcn_s_sleep(1);
  }
  asm volatile("" ::: "memory");
}
__device__ __forceinline__ void wait_lanes(char* ws, u32 off, u32 want) {
  bool ok = off == ~0u;
  for (unsigned sp = 0; sp < SPIN_CAP; ++sp) {
    if (!ok) ok = __hip_atomic_load((gu32*)(ws + off), __ATOMIC_RELAXED, __HIP_MEMORY_SCOPE_AGENT) >= want;
    if (__all(ok)) break;
    __builtin_amdgcn_s_sleep(POLLS);
  }
}
__device__ __forceinline__ void relay_wait(volatile LAS u32* w, u32 want) {
  for (unsigned sp = 0; sp < (1u << 24); ++sp) {
    if ((u32)__builtin_amdgcn_readfirstlane(*w) >= want) break;
    __builtin_amdgcn_s_sleep(1);
  }
  asm volatile("" ::: "memory");
}
__device__ __forceinline__ bool look_lanes(char* ws, u32 off, u32 want) {
  bool ok = off == ~0u;
  if (!ok) ok = __hip_atomic_load((gu32*)(ws + off), __ATOMIC_RELAXED, __HIP_MEMORY_SCOPE_AGENT) >= want;
  return __all(ok);
}

__device__ __forceinline__ u32 w1_line(const int xg, const int n) {
  const int t = n / 640, r = n - t * 640, k = r / 10, c = r - k * 10;
  const int col = ((xg * 27) / 6) * 256 + c * 128;
  return (t < 32 && col < F * 2) ? (u32)((8 * (t & 7) + 512 * (t >> 3) + (k >> 3) + 64 * (k & 7)) * (F * 2) + col) : 0xfffffff0u;
}
__device__ __forceinline__ void dft3(cf& a, cf& b, cf& c) {
  const float h = 0.8660254037844386f;
  cf s = cadd(b, c), d = csub(b, c);
  cf m = {__builtin_fmaf(-0.5f, s.x, a.x), __builtin_fmaf(-0.5f, s.y, a.y)};
  a = cadd(a, s);
  b = {__builtin_fmaf(h, d.y, m.x), __builtin_fmaf(-h, d.x, m.y)};
  c = {__builtin_fmaf(-h, d.y, m.x), __builtin_fmaf(h, d.x, m.y)};
}

__device__ __forceinline__ void phase1_rows(char* smem, const u16* __restrict__ x, char* ws, rsrc_t rw, const int bid, const int tid) {
  const int lane = tid & 63, wave = tid >> 6;
  cf* zc = (cf*)(smem + L_ZC);
  char* hrow = smem + L_HROW;
  const int p = lane & 31, r = 2 * wave + (lane >> 5);
  const int l = p ^ ((p & 4) ? 3 : 0);
  const u32* xr = (const u32*)(x + (size_t)((bid & 63) + 64 * (8 * (bid >> 6) + r)) * D);
  u32 raw[9];
#pragma unroll
  for (int j = 0; j < 9; ++j) raw[j] = __builtin_nontemporal_load(xr + l + 32 * j);
  cf tw1[9];
#pragma unroll
  for (int k2 = 1; k2 < 9; ++k2) tw1[k2] = ldtw(TW576.v, 2 * l * k2);
  cf ts4 = ldtw(TW576.v, 18 * (l & 15)), ts3 = ldtw(TW576.v, 36 * (l & 7)), ts2 = ldtw(TW576.v, 72 * (l & 3));
  BAR();
  float e[9], o[9];
#pragma unroll
  for (int j = 0; j < 9; ++j) { e[j] = bflo(raw[j]); o[j] = bfhi(raw[j]); }
#pragma unroll
  for (int pass = 0; pass < 2; ++pass) {
    float s = 0;
#pragma unroll
    for (int j = 0; j < 9; ++j) s += e[j] + o[j];
    const float mean = half_sum(s) * (1.0f / D);
    float q = 0;
#pragma unroll
    for (int j = 0; j < 9; ++j) { const float a = e[j] - mean, b = o[j] - mean; q = __builtin_fmaf(a, a, q); q = __builtin_fmaf(b, b, q); }
    const float var = half_sum(q) * (1.0f / D);
    const float m = rbf(mean);
    const float inv = __builtin_amdgcn_rcpf(rbf(__builtin_sqrtf(rbf(rbf(var) + 1e-5f))));
#pragma unroll
    for (int j = 0; j < 9; ++j) {
      const u32 d = pack_bf16(e[j] - m, o[j] - m);
      const u32 n = pack_bf16(bflo(d) * inv, bfhi(d) * inv);
      if (pass == 0) { e[j] = bflo(n); o[j] = bfhi(n); }
      else *(u32*)(hrow + r * 1216 + (l + 32 * j) * 4) = n;
    }
  }
  lds_arrive(smem, 2, lane);
  cf z[9];
#pragma unroll
  for (int j = 0; j < 9; ++j) z[j] = {e[j], o[j]};
  dft3(z[0], z[3], z[6]); dft3(z[1], z[4], z[7]); dft3(z[2], z[5], z[8]);
  {
    const cf w1 = {0.766044443118978f, -0.6427876096865393f}, w2 = {0.17364817766693041f, -0.984807753012208f}, w4 = {-0.9396926207859083f, -0.3420201433256689f};
    z[4] = cmul(z[4], w1); z[7] = cmul(z[7], w2); z[5] = cmul(z[5], w2); z[8] = cmul(z[8], w4);
  }
  dft3(z[0], z[1], z[2]); dft3(z[3], z[4], z[5]); dft3(z[6], z[7], z[8]);
  cf y[9];
#pragma unroll
  for (int c = 0; c < 3; ++c)
#pragma unroll
    for (int d = 0; d < 3; ++d) y[c + 3 * d] = z[3 * c + d];
#pragma unroll
  for (int k2 = 1; k2 < 9; ++k2) y[k2] = cmul(y[k2], tw1[k2]);
  {
    const bool u4 = l & 16, u3 = l & 8, u2 = l & 4, u1 = l & 2, u0 = l & 1;
    const float s4 = u4 ? -1.f : 1.f, s3 = u3 ? -1.f : 1.f, s2 = u2 ? -1.f : 1.f, s1 = u1 ? -1.f : 1.f, s0 = u0 ? 1.f : -1.f;
    ts4 = u4 ? cf{-ts4.x, -ts4.y} : cf{1.f, 0.f};
    ts3 = u3 ? cf{-ts3.x, -ts3.y} : cf{1.f, 0.f};
    ts2 = u2 ? cf{-ts2.x, -ts2.y} : cf{1.f, 0.f};
    const cf ts1 = u1 ? (u0 ? cf{0.f, -1.f} : cf{-1.f, 0.f}) : (u0 ? cf{-1.f, 0.f} : cf{1.f, 0.f});
#pragma unroll
    for (int k = 0; k < 9; ++k) { cf t = {__builtin_fmaf(xor16f(y[k].x), s4, y[k].x), __builtin_fmaf(xor16f(y[k].y), s4, y[k].y)}; y[k] = cmul(t, ts4); }
#define BFLY9(CTRL, SG) asm volatile("s_nop 1\n\t" \
      "v_fmac_f32_dpp %0, %0, %18 " CTRL "\n\tv_fmac_f32_dpp %1, %1, %18 " CTRL "\n\tv_fmac_f32_dpp %2, %2, %18 " CTRL "\n\t" \
      "v_fmac_f32_dpp %3, %3, %18 " CTRL "\n\tv_fmac_f32_dpp %4, %4, %18 " CTRL "\n\tv_fmac_f32_dpp %5, %5, %18 " CTRL "\n\t" \
      "v_fmac_f32_dpp %6, %6, %18 " CTRL "\n\tv_fmac_f32_dpp %7, %7, %18 " CTRL "\n\tv_fmac_f32_dpp %8, %8, %18 " CTRL "\n\t" \
      "v_fmac_f32_dpp %9, %9, %18 " CTRL "\n\tv_fmac_f32_dpp %10, %10, %18 " CTRL "\n\tv_fmac_f32_dpp %11, %11, %18 " CTRL "\n\t" \
      "v_fmac_f32_dpp %12, %12, %18 " CTRL "\n\tv_fmac_f32_dpp %13, %13, %18 " CTRL "\n\tv_fmac_f32_dpp %14, %14, %18 " CTRL "\n\t" \
      "v_fmac_f32_dpp %15, %15, %18 " CTRL "\n\tv_fmac_f32_dpp %16, %16, %18 " CTRL "\n\tv_fmac_f32_dpp %17, %17, %18 " CTRL \
      : "+v"(y[0].x), "+v"(y[0].y), "+v"(y[1].x), "+v"(y[1].y), "+v"(y[2].x), "+v"(y[2].y), "+v"(y[3].x), "+v"(y[3].y), "+v"(y[4].x), "+v"(y[4].y), \
        "+v"(y[5].x), "+v"(y[5].y), "+v"(y[6].x), "+v"(y[6].y), "+v"(y[7].x), "+v"(y[7].y), "+v"(y[8].x), "+v"(y[8].y) : "v"(SG))
    BFLY9("row_ror:8 row_mask:0xf bank_mask:0xf", s3);
#pragma unroll
    for (int k = 0; k < 9; ++k) y[k] = cmul(y[k], ts3);
    BFLY9("row_half_mirror row_mask:0xf bank_mask:0xf", s2);
#pragma unroll
    for (int k = 0; k < 9; ++k) y[k] = cmul(y[k], ts2);
    BFLY9("quad_perm:[2,3,0,1] row_mask:0xf bank_mask:0xf", s1);
#pragma unroll
    for (int k = 0; k < 9; ++k) y[k] = cmul(y[k], ts1);
    BFLY9("quad_perm:[1,0,3,2] row_mask:0xf bank_mask:0xf", s0);
  }
  {
    const int k1 = __builtin_bitreverse32((u32)l) >> 27;
#pragma unroll
    for (int k2 = 0; k2 < 9; ++k2) zc[r * 288 + 9 * k1 + k2] = y[k2];
  }
  lds_arrive(smem, 3, lane);
}
template <int NT> __device__ __forceinline__ void phase1_unpack(char* smem, rsrc_t rw, const int bid, const int t) {
  constexpr int NR = (290 + NT - 1) / NT;
  const cf* zc = (const cf*)(smem + L_ZC);
  cf wk[NR];
#pragma unroll
  for (int rd = 0; rd < NR; ++rd) wk[rd] = ldtw(TW576.v, (t + NT * rd) >> 1);
  lds_wait(smem, 3, 4);
  const u32 zo = (u32)WS_Z + ((bid & 63) * 4 + (bid >> 6)) * 128;
#pragma unroll
  for (int rd = 0; rd < NR; ++rd) {
    const int it = t + NT * rd, k = it >> 1, rq = it & 1;
    if (k <= 144) {
      u32x4 v0, v1;
#pragma unroll
      for (int e2 = 0; e2 < 4; ++e2) {
        const int row = 4 * rq + e2;
        const cf a = zc[row * 288 + k], b = zc[row * 288 + (k ? 288 - k : 0)];
        const cf E = {0.5f * (a.x + b.x), 0.5f * (a.y - b.y)};
        const cf O = {0.5f * (a.y + b.y), 0.5f * (b.x - a.x)};
        const cf T = cmul(O, wk[rd]);
        v0[e2] = pack_bf16(E.x + T.x, E.y + T.y);
        v1[e2] = pack_bf16(E.x - T.x, T.y - E.y);
      }
      k1st(rw, zo + (k >> 2) * 32768 + (k & 3) * 32 + rq * 16, v0);
      k1st(rw, zo + ((288 - k) >> 2) * 32768 + ((288 - k) & 3) * 32 + rq * 16, v1);
    }
  }
}

__device__ __forceinline__ void phase1_h(char* smem, rsrc_t rw, const int bid, const int L, const int lane) {
  const u32 hb = (u32)(size_t)(smem + L_HROW);
  const int g = lane >> 4, q = (lane & 15) >> 2, pp = lane & 3;
  const u32 a = hb + q * 1216 + (320 * L + 16 * g + 4 * pp) * 2;
  u32x2 t[10];
  asm volatile("ds_read_b64_tr_b16 %0, %10\n\tds_read_b64_tr_b16 %1, %10 offset:4864\n\tds_read_b64_tr_b16 %2, %10 offset:128\n\tds_read_b64_tr_b16 %3, %10 offset:4992\n\t"
               "ds_read_b64_tr_b16 %4, %10 offset:256\n\tds_read_b64_tr_b16 %5, %10 offset:5120\n\tds_read_b64_tr_b16 %6, %10 offset:384\n\tds_read_b64_tr_b16 %7, %10 offset:5248\n\t"
               "ds_read_b64_tr_b16 %8, %10 offset:512\n\tds_read_b64_tr_b16 %9, %10 offset:5376\n\ts_waitcnt lgkmcnt(0)"
               : "=&v"(t[0]), "=&v"(t[1]), "=&v"(t[2]), "=&v"(t[3]), "=&v"(t[4]), "=&v"(t[5]), "=&v"(t[6]), "=&v"(t[7]), "=&v"(t[8]), "=&v"(t[9]) : "v"(a) : "memory");
#pragma unroll
  for (int i = 0; i < 5; ++i)
    if (i < 4 || L == 0) st16(rw, (u32)WS_HB + (bid * D + 320 * L + 64 * i + lane) * 16, (u32x4){t[2 * i].x, t[2 * i].y, t[2 * i + 1].x, t[2 * i + 1].y});
}
__device__ __forceinline__ void phase1_wt_load(char* stg, const u16* __restrict__ w, const int unit, const int lane) {
  const u32x4 sr = make_srd(w, S * DF * 2);
  const u32 base = (u32)(size_t)stg;
  const u32 row0 = (u32)(2 * (unit & 15) + 128 * (unit >> 4)) * (DF * 2);
#pragma unroll
  for (int e = 0; e < 4; ++e)
#pragma unroll
    for (int j = 0; j < 2; ++j)
      dma16n(base + e * 2048 + j * 1024, lane * 16, sr, row0 + e * (32 * DF * 2) + j * 1024);
}
__device__ __forceinline__ void phase1_wt(char* stg, rsrc_t rw, const int unit, const int H, const int lane) {
  const u16* pr = (const u16*)stg; const u16* pi = (const u16*)(stg + 8192);
  const int i = lane & 1;
  const u32 dst = (u32)WS_WT + unit * 128 + (lane & 7) * 16 + ((lane >> 3) + 40 * H) * 32768;
#pragma unroll
  for (int rd = 0; rd < 5; ++rd) {
    const int k = 160 * H + 32 * rd + (lane >> 1);
    if (k < DF) {
      u32x4 v;
#pragma unroll
      for (int e = 0; e < 4; ++e) { const int idx = e * 1024 + i * DF + k; v[e] = (u32)pr[idx] | ((u32)pi[idx] << 16); }
      st16(rw, dst + rd * (8 * 32768), v);
    }
  }
}

extern "C" __global__ __launch_bounds__(512) void k1_rows(const u16* __restrict__ x, const u16* __restrict__ wr, const u16* __restrict__ wi, const u16* __restrict__ w1, char* ws) {
  __shared__ __attribute__((aligned(16))) char smem[K1_LDS];
  const int bid = blockIdx.x;
  const int tid = threadIdx.x, lane = tid & 63;
  const int wave = __builtin_amdgcn_readfirstlane(tid >> 6);
  const int L = wave & 1;
  const rsrc_t rw = __builtin_amdgcn_make_buffer_rsrc(ws, 0, (int)WS_END, 0x00020000);
  if (wave < 4) {
    phase1_rows(smem, x, ws, rw, bid, tid);
  } else if (wave < 6) {
    if (wave == 4) {
      if (lane < 4) *(u32*)(ws + WS_FP + bid * 64 + lane * 4) = 0u;
      if (lane == 4 && bid < G1_WGS) *(u32*)(ws + WS_FG + bid * 128) = 0u;
      if (bid < 8) *(u32*)(ws + WS_FY + bid * 256 + lane * 4) = 0u;
    }
    {
      const int n = ((bid >> 3) * 2 + L) * 64 + lane;
      u64 c1, c2;
      asm volatile("s_getpc_b64 vcc\n\ts_add_u32 vcc_lo, vcc_lo, k1_rows@rel32@lo+4\n\ts_addc_u32 vcc_hi, vcc_hi, k1_rows@rel32@hi+12\n\ts_mov_b64 %0, vcc" : "=s"(c1) :: "vcc", "scc");
      asm volatile("s_getpc_b64 vcc\n\ts_add_u32 vcc_lo, vcc_lo, k2_fused@rel32@lo+4\n\ts_addc_u32 vcc_hi, vcc_hi, k2_fused@rel32@hi+12\n\ts_mov_b64 %0, vcc" : "=s"(c2) :: "vcc", "scc");
      const int w = (bid >> 3) * 2 + L;
      const u32 pad = (u32)(size_t)smem + L_PF1;
      if (w < 2) touch(pad, n * 128, make_srd((const void*)c1, K1_CODE), 0);
      else if (w < 8) touch(pad, (n - 128) * 128, make_srd((const void*)c2, K2_CODE), 0);
      else if (w < 14) touch(pad, (n - 512) * 128, make_srd(&FTAB, 49152), 0);
      else if (w < 40) {
        const int kq = (bid & 7) >> 1;
        touch(pad, (u32)(2 * kq * 73728 + (n - 896) * 128), make_srd(IDFT, (u32)((2 * kq + (kq == 3 ? 3 : 2)) * 73728)), 0);
      }
    }
    BAR();
    {
      const u32x4 srdW1 = make_srd(w1, (u32)((size_t)S * F * 2));
#pragma unroll 1
      for (int n = ((bid >> 3) * 2 + L) * 64; n < PF1 * 640; n += 4096) touch((u32)(size_t)smem + L_PF1, w1_line(bid & 7, n + lane), srdW1, 0);
    }
    lds_wait(smem, 2, 4);
    phase1_h(smem, rw, bid, L, lane);
  } else {
    if (wave == 7 && lane < 16) *(u32*)(smem + L_SYNC + lane * 4) = 0u;
    phase1_wt_load(smem + L_WTS + L * 8192, L ? wi : wr, bid, lane);
    asm volatile("s_waitcnt lgkmcnt(0)\n\ts_barrier" ::: "memory");
    WAITV(0);
    lds_arrive(smem, 6, lane);
    lds_wait(smem, 6, 2);
    phase1_wt(smem + L_WTS, rw, bid, L, lane);
  }
  if (tid < 320) phase1_unpack<320>(smem, rw, bid, tid);
}

__device__ __forceinline__ void pack_frags(const f32x16& v, u32x4& f0, u32x4& f1) {
  f0 = (u32x4){pack_bf16(v[0], v[1]), pack_bf16(v[2], v[3]), pack_bf16(v[4], v[5]), pack_bf16(v[6], v[7])};
  f1 = (u32x4){pack_bf16(v[8], v[9]), pack_bf16(v[10], v[11]), pack_bf16(v[12], v[13]), pack_bf16(v[14], v[15])};
}
template <int INV> __device__ __forceinline__ void dft64(const char* tab, const int lane, f32x16 (&re)[2], f32x16 (&im)[2]) {
  u32x4 br[4], bi[4], bn[4];
  pack_frags(re[0], br[0], br[1]); pack_frags(re[1], br[2], br[3]);
  pack_frags(im[0], bi[0], bi[1]); pack_frags(im[1], bi[2], bi[3]);
#pragma unroll
  for (int m = 0; m < 4; ++m) bn[m] = (INV ? br[m] : bi[m]) ^ 0x80008000u;
  const u32x4* t2 = (const u32x4*)(tab + 8192) + lane;
#pragma unroll
  for (int ub = 0; ub < 2; ++ub) {
    f32x16 xr = {}, xi = {};
#pragma unroll
    for (int m = 0; m < 4; ++m) {
      const u32x4 fr = t2[(ub * 4 + m) * 64], fi = t2[(8 + ub * 4 + m) * 64];
      xr = mfma32(fr, br[m], xr);
      xi = mfma32(fr, bi[m], xi);
      xr = mfma32(fi, INV ? bi[m] : bn[m], xr);
      xi = mfma32(fi, INV ? bn[m] : br[m], xi);
    }
    re[ub] = xr; im[ub] = xi;
  }
}
template <int INV> __device__ __forceinline__ void twid(const char* tab, const int lane, f32x16 (&re)[2], f32x16 (&im)[2]) {
  const f32x4* tw = (const f32x4*)(tab + 32768) + lane;
#pragma unroll
  for (int rb = 0; rb < 2; ++rb)
#pragma unroll
    for (int p = 0; p < 8; ++p) {
      const f32x4 w = tw[(rb * 8 + p) * 64];
      cf a = {re[rb][2 * p], im[rb][2 * p]}, b = {re[rb][2 * p + 1], im[rb][2 * p + 1]};
      a = INV ? cmulc(a, cf{w.x, w.y}) : cmul(a, cf{w.x, w.y});
      b = INV ? cmulc(b, cf{w.z, w.w}) : cmul(b, cf{w.z, w.w});
      re[rb][2 * p] = a.x; im[rb][2 * p] = a.y; re[rb][2 * p + 1] = b.x; im[rb][2 * p + 1] = b.y;
    }
}
#define WAVE_LDS_FENCE() do { __builtin_amdgcn_fence(__ATOMIC_RELEASE, "wavefront"); __builtin_amdgcn_wave_barrier(); __builtin_amdgcn_fence(__ATOMIC_ACQUIRE, "wavefront"); } while (0)

__device__ __forceinline__ void fft_role(char* smem, char* ws, rsrc_t rw, const int fb, const int wave, const int lane, const u32x4 srdW1) {
  const int kk = fb * 8 + wave, k = kk < DF ? kk : DF - 1;
  const int r = lane & 31, h = lane >> 5;
  {
    const u32x4 srdWS = make_srd(ws, (u32)WS_END), srdT = make_srd(&FTAB, 49152);
    const u32 ldsb = (u32)(size_t)smem;
#pragma unroll
    for (int j = 0; j < 8; ++j) {
      const int q = lane ^ (2 * j);
      dma16(ldsb + FFT_ZS + (wave * 8 + j) * 1024, (u32)(((q & 1) * 32 + (q >> 1)) * 16), srdWS, (u32)WS_Z + (u32)fb * 65536 + (wave * 8 + j) * 1024);
    }
#pragma unroll
    for (int j = 0; j < 6; ++j) dma16(ldsb + FFT_TAB + (wave * 6 + j) * 1024, lane * 16, srdT, (wave * 6 + j) * 1024);
  }
  WAITV(0); BAR();
  u32x4 wv[2][4];
  {
    const u32 wo = (u32)WS_WT + (k >> 2) * 32768 + (h * 16 + (r >> 1)) * 128 + (k & 3) * 32 + (r & 1) * 16;
#pragma unroll
    for (int ub = 0; ub < 2; ++ub)
#pragma unroll
      for (int g = 0; g < 4; ++g) wv[ub][g] = __builtin_amdgcn_raw_buffer_load_b128(rw, wo + (8 * ub + 2 * g) * 2048, 0, 0);
  }
  {
    const int g8 = fb & 7, ng = 40;
#pragma unroll 1
    for (int n = PF1 * 640 + ((fb >> 3) * 8 + wave) * 64; n < PF2 * 640; n += ng * 64) touch((u32)(size_t)smem + L_PF, w1_line(g8, n + lane), srdW1, 0);
  }
  u32x4 za[2][4];
  {
    const int kl = k - fb * 8;
    const char* zs = smem + FFT_ZS + (kl >> 2) * 32768 + (r >> 1) * 1024;
#pragma unroll
    for (int rb = 0; rb < 2; ++rb)
#pragma unroll
      for (int m = 0; m < 4; ++m) {
        const int c = 2 * h + (m >> 1), slot = (kl & 3) * 2 + (m & 1);
        za[rb][m] = *(const u32x4*)(zs + rb * 16384 + ((((8 * c + slot) * 2 + (r & 1)) ^ (2 * ((r >> 1) & 7))) * 16));
      }
  }
  asm volatile("s_waitcnt lgkmcnt(0)\n\ts_barrier" : "+v"(za[0][0]), "+v"(za[0][1]), "+v"(za[0][2]), "+v"(za[0][3]), "+v"(za[1][0]), "+v"(za[1][1]), "+v"(za[1][2]), "+v"(za[1][3]) :: "memory");
  const char* tab = smem + FFT_TAB;
  char* img = smem + FFT_WAVE + wave * FFT_WAVE_B;
  f32x16 re[2], im[2];
  {
    const u32x4* t1 = (const u32x4*)tab + lane;
    re[0] = f32x16{}; re[1] = f32x16{}; im[0] = f32x16{}; im[1] = f32x16{};
#pragma unroll
    for (int m = 0; m < 4; ++m) {
      const u32x4 fr = t1[m * 64], fi = t1[(4 + m) * 64];
      re[0] = mfma32(za[0][m], fr, re[0]); im[0] = mfma32(za[0][m], fi, im[0]);
      re[1] = mfma32(za[1][m], fr, re[1]); im[1] = mfma32(za[1][m], fi, im[1]);
    }
  }
  twid<0>(tab, lane, re, im);
  dft64<0>(tab, lane, re, im);
#pragma unroll
  for (int ub = 0; ub < 2; ++ub)
#pragma unroll
    for (int rho = 0; rho < 16; ++rho) {
      const cf p = cmul(cf{re[ub][rho], im[ub][rho]}, cf{bflo(wv[ub][rho >> 2][rho & 3]), bfhi(wv[ub][rho >> 2][rho & 3])});
      re[ub][rho] = p.x; im[ub][rho] = p.y;
    }
  dft64<1>(tab, lane, re, im);
  twid<1>(tab, lane, re, im);
#pragma unroll
  for (int rb = 0; rb < 2; ++rb)
#pragma unroll
    for (int g = 0; g < 4; ++g) {
      char* p = img + r * 136 + (32 * rb + 8 * g + 4 * h) * 2;
      *(u32x2*)p = (u32x2){pack_bf16(re[rb][4 * g], re[rb][4 * g + 1]), pack_bf16(re[rb][4 * g + 2], re[rb][4 * g + 3])};
      *(u32x2*)(p + 4352) = (u32x2){pack_bf16(im[rb][4 * g], im[rb][4 * g + 1]), pack_bf16(im[rb][4 * g + 2], im[rb][4 * g + 3])};
    }
  u32x4 va[2][2][2];
  {
    const int blk = (lane >> 4) & 1, q = (lane & 15) >> 2, p = lane & 3;
    const u32 a = (u32)(size_t)img + (8 * h + q) * 136 + (16 * blk + 4 * p) * 2;
    u32x2 t[16];
    asm volatile("s_waitcnt lgkmcnt(0)\n\t"
                 "ds_read_b64_tr_b16 %0, %16\n\tds_read_b64_tr_b16 %1, %16 offset:544\n\tds_read_b64_tr_b16 %2, %16 offset:2176\n\tds_read_b64_tr_b16 %3, %16 offset:2720\n\t"
                 "ds_read_b64_tr_b16 %4, %16 offset:64\n\tds_read_b64_tr_b16 %5, %16 offset:608\n\tds_read_b64_tr_b16 %6, %16 offset:2240\n\tds_read_b64_tr_b16 %7, %16 offset:2784\n\t"
                 "ds_read_b64_tr_b16 %8, %16 offset:4352\n\tds_read_b64_tr_b16 %9, %16 offset:4896\n\tds_read_b64_tr_b16 %10, %16 offset:6528\n\tds_read_b64_tr_b16 %11, %16 offset:7072\n\t"
                 "ds_read_b64_tr_b16 %12, %16 offset:4416\n\tds_read_b64_tr_b16 %13, %16 offset:4960\n\tds_read_b64_tr_b16 %14, %16 offset:6592\n\tds_read_b64_tr_b16 %15, %16 offset:7136\n\t"
                 "s_waitcnt lgkmcnt(0)"
                 : "=&v"(t[0]), "=&v"(t[1]), "=&v"(t[2]), "=&v"(t[3]), "=&v"(t[4]), "=&v"(t[5]), "=&v"(t[6]), "=&v"(t[7]),
                   "=&v"(t[8]), "=&v"(t[9]), "=&v"(t[10]), "=&v"(t[11]), "=&v"(t[12]), "=&v"(t[13]), "=&v"(t[14]), "=&v"(t[15])
                 : "v"(a) : "memory");
#pragma unroll
    for (int i = 0; i < 8; ++i) va[i >> 2][(i >> 1) & 1][i & 1] = (u32x4){t[2 * i].x, t[2 * i].y, t[2 * i + 1].x, t[2 * i + 1].y};
  }
  {
    const u32x4* t4 = (const u32x4*)(tab + 24576) + lane;
    re[0] = f32x16{}; re[1] = f32x16{}; im[0] = f32x16{}; im[1] = f32x16{};
#pragma unroll
    for (int ks = 0; ks < 2; ++ks) {
      const u32x4 gr = t4[ks * 64], gi = t4[(2 + ks) * 64], gn = t4[(4 + ks) * 64];
#pragma unroll
      for (int rb = 0; rb < 2; ++rb) {
        re[rb] = mfma32(va[0][rb][ks], gr, re[rb]); im[rb] = mfma32(va[0][rb][ks], gi, im[rb]);
        re[rb] = mfma32(va[1][rb][ks], gn, re[rb]); im[rb] = mfma32(va[1][rb][ks], gr, im[rb]);
      }
    }
  }
#pragma unroll
  for (int rb = 0; rb < 2; ++rb)
#pragma unroll
    for (int g = 0; g < 4; ++g) {
      char* p = img + r * 144 + (32 * rb + 8 * g + 4 * h) * 2;
      *(u32x2*)p = (u32x2){pack_bf16(re[rb][4 * g], re[rb][4 * g + 1]), pack_bf16(re[rb][4 * g + 2], re[rb][4 * g + 3])};
      *(u32x2*)(p + 4608) = (u32x2){pack_bf16(im[rb][4 * g], im[rb][4 * g + 1]), pack_bf16(im[rb][4 * g + 2], im[rb][4 * g + 3])};
    }
  WAVE_LDS_FENCE();
  if (kk < DF) {
    const int rowRe = (k == DF - 1) ? 1 : 2 * k;
#pragma unroll
    for (int i = 0; i < 4; ++i) st16(rw, (u32)WS_YT + (rowRe * 256 + 64 * i + lane) * 16, *(const u32x4*)(img + (8 * i + (lane >> 3)) * 144 + (lane & 7) * 16));
    if (k > 0 && k < DF - 1) {
#pragma unroll
      for (int i = 0; i < 4; ++i) st16(rw, (u32)WS_YT + ((2 * k + 1) * 256 + 64 * i + lane) * 16, *(const u32x4*)(img + 4608 + (8 * i + (lane >> 3)) * 144 + (lane & 7) * 16));
    }
  }
}

__device__ __forceinline__ u32x2 gelu4(const f32x4 a, const u32x2 bb) {
  const float bv[4] = {bflo(bb.x), bfhi(bb.x), bflo(bb.y), bfhi(bb.y)};
  float r[4];
#pragma unroll
  for (int e = 0; e < 4; ++e) {
    const float u = a[e] + bv[e];
    const float z = u * __builtin_fmaf(u * u, -0.044715f * 2.0f * 0.7978845608028654f * 1.4426950408889634f, -2.0f * 0.7978845608028654f * 1.4426950408889634f);
    r[e] = u * __builtin_amdgcn_rcpf(1.0f + __builtin_amdgcn_exp2f(z));
  }
  return (u32x2){pack_bf16(r[0], r[1]), pack_bf16(r[2], r[3])};
}
__device__ __forceinline__ void g1_store2(const f32x4 a0, const f32x4 a1, const u32x2 bb0, const u32x2 bb1, rsrc_t rw, const u32 off) {
  const u32x2 x = gelu4(a0, bb0), y = gelu4(a1, bb1);
  const auto s0 = __builtin_amdgcn_permlane16_swap(x.x, y.x, false, false);
  const auto s1 = __builtin_amdgcn_permlane16_swap(x.y, y.y, false, false);
  st16(rw, off, (u32x4){s0[0], s1[0], s0[1], s1[1]});
}
__device__ __forceinline__ void count_in(char* ws, size_t off, int lane) {
  if (lane == 0) __hip_atomic_fetch_add((gu32*)(ws + off), 1u, __ATOMIC_RELAXED, __HIP_MEMORY_SCOPE_AGENT);
}

constexpr int g2_owner(int i, int j) { return j < 8 ? (j >> 1) : i; }
template <int Q>
__device__ __forceinline__ void g2_finish(f32x4 (&acc)[2][9], const u32x2 (&xr)[5], const float (&bs)[2], char* ws, rsrc_t rw, u16* __restrict__ out,
                                          const int tile, const int s0, const int d0, const int wave, const int lane) {
  const int srow = s0 + wave * 32 + (lane & 15), dcol = d0 + 4 * (lane >> 4);
  const u32 mine = (u32)WS_P + (u32)(((tile * 4 + Q) * 4 + wave) * 10) * 1024;
#pragma unroll
  for (int j = 0; j < 8; ++j)
    if ((j >> 1) != Q)
      st16(rw, mine + j * 1024 + lane * 16, (u32x4){pack_bf16(acc[0][j].x, acc[0][j].y), pack_bf16(acc[0][j].z, acc[0][j].w), pack_bf16(acc[1][j].x, acc[1][j].y), pack_bf16(acc[1][j].z, acc[1][j].w)});
#pragma unroll
  for (int i = 0; i < 2; ++i)
    if (i != Q) st8(rw, mine + (8 + i) * 1024 + lane * 8, (u32x2){pack_bf16(acc[i][8].x, acc[i][8].y), pack_bf16(acc[i][8].z, acc[i][8].w)});
  WAITV(0);
  if (lane == 0) set_flag(ws, WS_FP + (tile * 4 + wave) * 64 + Q * 4);
  wait_lanes(ws, (lane < 4 && lane != Q) ? (u32)WS_FP + (tile * 4 + wave) * 64 + lane * 4 : ~0u, 1);
  u32x4 pv[3][2]; u32x2 pe[3];
#pragma unroll
  for (int o = 0; o < 3; ++o) {
    const int src = o < Q ? o : o + 1;
    const u32 base = (u32)WS_P + (u32)(((tile * 4 + src) * 4 + wave) * 10) * 1024;
    pv[o][0] = ld16(rw, base + (2 * Q) * 1024 + lane * 16);
    pv[o][1] = ld16(rw, base + (2 * Q + 1) * 1024 + lane * 16);
    if (Q < 2) pe[o] = __builtin_amdgcn_raw_buffer_load_b64(rw, base + (8 + Q) * 1024 + lane * 8, 0, 16);
  }
#pragma unroll
  for (int i = 0; i < 2; ++i)
#pragma unroll
    for (int j = 0; j < 9; ++j)
      if (g2_owner(i, j) == Q) {
        f32x4 t = acc[i][j];
#pragma unroll
        for (int o = 0; o < 3; ++o) {
          const u32 lo = j < 8 ? pv[o][j & 1][2 * i] : pe[o].x, hi = j < 8 ? pv[o][j & 1][2 * i + 1] : pe[o].y;
          t += (f32x4){bflo(lo), bfhi(lo), bflo(hi), bfhi(hi)};
        }
        const u32x2 xv = xr[j < 8 ? 2 * (j & 1) + i : 4];
        const u32x2 o2 = {pack_bf16(t.x + bs[i] + bflo(xv.x), t.y + bs[i] + bfhi(xv.x)), pack_bf16(t.z + bs[i] + bflo(xv.y), t.w + bs[i] + bfhi(xv.y))};
        *(u32x2*)(out + (size_t)(srow + 16 * i) * D + dcol + 16 * j) = o2;
      }
}

struct Geo {
  int f0, d1, ft, dt;
  int kq, s0, d2, tile, dt2, nD, eb;
};

extern "C" __global__ __launch_bounds__(512, 2) void k2_fused(const u16* __restrict__ x,
                                                   const u16* __restrict__ w1, const u16* __restrict__ b1, const u16* __restrict__ w2,
                                                   const u16* __restrict__ b2, char* ws, u16* __restrict__ out) {
  extern __shared__ __attribute__((aligned(16))) char smem[];
  const int bid = blockIdx.x;
  const int tid = threadIdx.x, lane = tid & 63;
  const int wave = __builtin_amdgcn_readfirstlane(tid >> 6);
  const u32 ldsb = (u32)(size_t)smem;
  const rsrc_t rw = __builtin_amdgcn_make_buffer_rsrc(ws, 0, (int)WS_END, 0x00020000);
  const bool isG1 = bid < G1_WGS, isFFT = !isG1 && bid < G1_WGS + FFT_WGS;
  Geo G;
  {
    const int xg = bid & 7, idx = bid >> 3;
    const int lin = xg * 27 + idx;
    G.ft = lin / 6; G.dt = lin - G.ft * 6; G.f0 = G.ft * 128; G.d1 = G.dt * 96;
    G.kq = xg >> 1;
    const int st = (xg & 1) * 8 + (idx >> 2);
    G.dt2 = idx & 3; G.s0 = st * 128; G.d2 = G.dt2 * 144; G.tile = st * 4 + G.dt2;
    G.nD = G.kq == 3 ? 3 : 2; G.eb = 2 * G.kq;
  }
  const int L = wave & 1;
  const int kk = lane >> 4;
  const u32 cs0 = ((lane & 15) ^ (kk << 1)) * 16, cs1 = ((lane & 15) ^ ((kk << 1) | 8)) * 16;
  const u32 mW = ldsb + L_W + L * 8192;
  const u32x4 srdW1 = make_srd(w1, (u32)((size_t)S * F * 2));
  const u32 vW0 = (u32)((64 * kk + 4 * L) * (F * 2) + G.f0 * 2) + cs0, vW1 = (u32)((64 * kk + 4 * L) * (F * 2) + G.f0 * 2) + cs1;
#define G1_W(t, sl) do { const u32 _s = (u32)(8 * ((t) & 7) + 512 * ((t) >> 3)) * (F * 2); \
    _Pragma("unroll") for (int j = 0; j < 8; ++j) dma16(mW + (sl) * 16384 + j * 1024, (j & 2) ? vW1 : vW0, srdW1, _s + ((j >> 1) + 256 * (j & 1)) * (F * 2)); } while (0)

  if (isFFT) {
    fft_role(smem, ws, rw, bid - G1_WGS, wave, lane, srdW1);
    if (wave == 4 && lane == 0) *(volatile LAS u32*)(LAS char*)(smem + L_PF + 256) = 0u;
    asm volatile("s_waitcnt vmcnt(0) lgkmcnt(0)\n\ts_barrier" ::: "memory");
    if (wave == 4 && lane < 8) set_flag(ws, WS_FY + lane * 256 + (bid - G1_WGS) * 4);
  } else if (!isG1) {
#pragma unroll 1
    for (int n = PF1 * 640 + (32 + wave) * 64; n < PF2 * 640; n += 40 * 64) touch(ldsb + L_PF, w1_line(bid & 7, n + lane), srdW1, 0);
  }

  if (wave >= 4) {
    const int wd = (wave >> 1) & 1, wf = wave & 1;
    const int fbase = G.f0 + wf * 64 + 32 + 4 * kk;
    const u32 goff = (u32)WS_GB + (u32)((((G.f0 + wf * 64 + 32) >> 3) + (kk & 1) * 2 + (kk >> 1)) * D + G.d1 + wd * 48 + (lane & 15)) * 16;
    if (wave >= 6) {
      if (isG1) {
        const u32x2 bb0 = *(const u32x2*)(b1 + fbase), bb1 = *(const u32x2*)(b1 + fbase + 16);
        const u32x4 srdW2h = make_srd(w2, (u32)((size_t)F * S * 2));
        const gu32* fy = (const gu32*)(ws + WS_FY + (bid & 7) * 256 + (lane < 4 * G.nD ? 4 * G.eb + lane : 36) * 4);
        u32 seen = 0;
#pragma unroll 1
        for (int i = 0; i < 35; ++i) {
          BAR();
          if (wave == 6 && (i == 26 || i == 29 || i == 32)) {
            if (i != 26 && __all(seen != 0u) && lane == 0) *(volatile LAS u32*)(LAS char*)(smem + L_PF + 256) = 1u;
            asm volatile("s_waitcnt lgkmcnt(0)" ::: "memory");
            if (i != 32) seen = __hip_atomic_load(fy, __ATOMIC_RELAXED, __HIP_MEMORY_SCOPE_AGENT);
          }
          if (i == 32 - L) {
#pragma unroll
            for (int q = 0; q < PFW2H; ++q) {
              const int n = ((q * 27 + (bid >> 3)) * 2 + L) * 64 + lane;
              touch(ldsb + L_PF, (u32)((1152 * G.kq + (n >> 4)) * (S * 2) + (bid & 1) * 2048 + (n & 15) * 128), srdW2h, 0);
            }
          }
        }
        const f32x4* ex = (const f32x4*)(smem + L_EX) + (wave - 4) * 384 + lane;
#pragma unroll
        for (int j = 0; j < 3; ++j) g1_store2(ex[j * 64], ex[(3 + j) * 64], bb0, bb1, rw, goff + j * 256);
        WAITV(0);
        count_in(ws, WS_FG + (G.ft * 6 + G.dt) * 128, lane);
        asm volatile("s_waitcnt lgkmcnt(0)\n\ts_barrier" ::: "memory");
      }
      return;
    }
    if (!isFFT && L == 0 && lane == 0) *(volatile LAS u32*)(LAS char*)(smem + L_PF + 256) = 0u;
    asm volatile("s_waitcnt lgkmcnt(0)" ::: "memory");
    if (!isG1 && !isFFT) BAR();
    const u32x4 srdWS = make_srd(ws, (u32)WS_END);
    if (isG1) {
      u32 vH[6];
#pragma unroll
      for (int j = 0; j < 6; ++j) { const int p = j * 64 + lane; const int kc = p / 96, dd = p - kc * 96; vH[j] = (u32)WS_HB + (u32)(((4 * L + kc) * D + G.d1 + dd) * 16); }
      const u32 mH = ldsb + L_X + L * 6144;
#define G1_H(t, sl) do { _Pragma("unroll") for (int j = 0; j < 6; ++j) dma16(mH + (sl) * 12288 + j * 1024, vH[j], srdWS, (u32)(t) * (8 * D * 16)); } while (0)
#pragma unroll
      for (int t = 0; t < 4; ++t) { G1_W(t, t); G1_H(t, t); }
      WAITV(42); BAR();
      WAITV(28); BAR(); G1_W(4, 4); G1_H(4, 4);
      WAITV(28); BAR(); G1_W(5, 0); G1_H(5, 0);
      int sl = 1;
#pragma unroll 1
      for (int t = 2; t < 28; ++t) {
        WAITV(28); BAR();
        G1_W(t + 4, sl); G1_H(t + 4, sl);
        sl = sl == 4 ? 0 : sl + 1;
      }
      WAITV(28); BAR();
      WAITV(14); BAR();
      WAITV(0); BAR();
    }
    const u32x4 srdW2 = make_srd(w2, (u32)((size_t)F * S * 2));
    int ln2 = lane; asm volatile("" : "+v"(ln2));
    const int kk2 = ln2 >> 4;
    const u32 vA0 = (u32)((32 * L + kk2) * (S * 2) + G.s0 * 2) + ((ln2 & 15) ^ (kk2 << 1)) * 16, vA1 = (u32)((32 * L + kk2) * (S * 2) + G.s0 * 2) + ((ln2 & 15) ^ ((kk2 << 1) | 8)) * 16;
    u32 vG[9];
#pragma unroll
    for (int j = 0; j < 9; ++j) { const int p = j * 64 + ln2; const int kc = p / 144, dd = p - kc * 144; vG[j] = (u32)WS_GB + (u32)(((4 * L + kc) * D + G.d2 + dd) * 16); }
    const u32 mG = ldsb + L_X + L * 9216;
    const int nD = G.nD, nk = nD + 18;
#define G2_WY(e, sl) do { _Pragma("unroll") for (int j = 0; j < 8; ++j) dma16s(mW + (sl) * 16384 + j * 1024, (j & 2) ? vA1 : vA0, srdWS, (u32)WS_YT + (u32)(e) * (64 * S * 2) + j * (4 * S * 2)); } while (0)
#define G2_WW(t, sl) do { _Pragma("unroll") for (int j = 0; j < 8; ++j) dma16(mW + (sl) * 16384 + j * 1024, (j & 2) ? vA1 : vA0, srdW2, (u32)(t) * (64 * S * 2) + j * (4 * S * 2)); } while (0)
    const u32x4 srdI = make_srd((const char*)IDFT - WS_GB, (u32)(WS_GB + sizeof(IDFT)));
#define G2_GI(e, sl) do { _Pragma("unroll") for (int j = 0; j < 9; ++j) dma16(mG + (sl) * 18432 + j * 1024, vG[j], srdI, (u32)(e) * (8 * D * 16)); } while (0)
#define G2_G(t, sl) do { _Pragma("unroll") for (int j = 0; j < 9; ++j) dma16s(mG + (sl) * 18432 + j * 1024, vG[j], srdWS, (u32)(t) * (8 * D * 16)); } while (0)
    volatile LAS u32* relay = (volatile LAS u32*)(LAS char*)(smem + L_PF + 256);
    bool early = false;
    if (isG1) {
      const u32x2 bb0 = *(const u32x2*)(b1 + fbase), bb1 = *(const u32x2*)(b1 + fbase + 16);
      BAR(); BAR();
      early = __builtin_amdgcn_readfirstlane(*relay) != 0;
      if (early) { G2_WY(G.eb, 0); G2_WY(G.eb + 1, 1); if (nD == 3) G2_WY(G.eb + 2, 2); }
      G2_GI(G.eb, 0); G2_GI(G.eb + 1, 1);
      if (nD == 3) G2_GI(G.eb + 2, 2);
      BAR();
      const f32x4* ex = (const f32x4*)(smem + L_EX) + L * 384 + lane;
#pragma unroll
      for (int j = 0; j < 3; ++j) g1_store2(ex[j * 64], ex[(3 + j) * 64], bb0, bb1, rw, goff + j * 256);
      WAITV(0);
      count_in(ws, WS_FG + (G.ft * 6 + G.dt) * 128, lane);
      asm volatile("s_waitcnt lgkmcnt(0)\n\ts_barrier" ::: "memory");
    } else {
      G2_GI(G.eb, 0); G2_GI(G.eb + 1, 1);
      if (nD == 3) G2_GI(G.eb + 2, 2);
    }
    const u32 fgoff = lane < 18 ? (u32)WS_FG + ((9 * G.kq + (lane >> 1)) * 6 + ((G.dt2 * 3) >> 1) + (lane & 1)) * 128 : ~0u;
    if (!early) {
      if (L == 0) { wait_lanes(ws, (u32)WS_FY + (bid & 7) * 256 + (lane < 4 * nD ? 4 * G.eb + lane : 36) * 4, 1); if (lane == 0) *relay = 1u; }
      else relay_wait(relay, 1u);
      G2_WY(G.eb, 0); G2_WY(G.eb + 1, 1);
      if (nD == 3) G2_WY(G.eb + 2, 2);
    }
    bool gseen = false;
    if (L == 0 && !early) { gseen = look_lanes(ws, fgoff, 8); if (gseen && lane == 0) *relay = 2u; }
    WAITV(0); BAR();
    const int t0 = 18 * G.kq - nD;
    if (nD == 2) G2_WW(t0 + 2, 2);
    G2_WW(t0 + 3, 3);
    if (L == 0) { if (!gseen) { wait_lanes(ws, fgoff, 8); if (lane == 0) *relay = 2u; } }
    else relay_wait(relay, 2u);
    if (nD == 2) {
      G2_G(t0 + 2, 2); G2_G(t0 + 3, 3);
      WAITV(9); BAR();
      G2_WW(t0 + 4, 0); G2_G(t0 + 4, 0);
    } else {
      G2_G(t0 + 3, 3);
      WAITV(0); BAR();
      G2_WW(t0 + 4, 0); G2_G(t0 + 4, 0);
      G2_WW(t0 + 5, 1); G2_G(t0 + 5, 1);
    }
#pragma unroll 1
    for (int sg = nD; sg < nk - 3; ++sg) {
      WAITV(17); BAR();
      const int sl = (sg + 3) & 3;
      G2_WW(t0 + sg + 3, sl); G2_G(t0 + sg + 3, sl);
    }
    WAITV(17); BAR();
    WAITV(0); BAR();
    return;
  }

  if (!isG1 && !isFFT) BAR();
  const int lg = lane >> 4, lq = (lane & 15) >> 2, lp = lane & 3;
  const u32 trbase = ldsb + L_W + (u32)((8 * lg + lq) * 256 + (lp & 1) * 8);
  const int swz = (lq << 1) | ((lg & 1) << 3);
  u32x2 xr[5]; float bs[2];
#define G2_XLOAD() do { \
    const u16* xp = x + (size_t)(G.s0 + wave * 32 + (lane & 15)) * D + G.d2 + 4 * lg; \
    _Pragma("unroll") for (int q = 0; q < 4; ++q) xr[q] = *(const u32x2*)(xp + (q & 1) * 16 * D + (2 * G.kq + (q >> 1)) * 16); \
    xr[4] = *(const u32x2*)(xp + (G.kq & 1) * 16 * D + 128); \
    bs[0] = bflo((u32)b2[G.s0 + wave * 32 + (lane & 15)]); bs[1] = bflo((u32)b2[G.s0 + wave * 32 + 16 + (lane & 15)]); } while (0)
  if (isG1) {
    const int wd = (wave >> 1) & 1, wf = wave & 1;
    u32 aA[4];
#pragma unroll
    for (int i = 0; i < 4; ++i) aA[i] = trbase + (u32)(((wf * 8 + 2 * i + (lp >> 1)) ^ swz) * 16);
    const u32 aB = ldsb + L_X + (u32)((lg * 96 + wd * 48 + (lane & 15)) * 16);
    const int fbase = G.f0 + wf * 64 + 4 * lg;
    const u32 goff = (u32)WS_GB + (u32)((((G.f0 + wf * 64) >> 3) + (lg & 1) * 2 + (lg >> 1)) * D + G.d1 + wd * 48 + (lane & 15)) * 16;
    const u32x2 bb0 = *(const u32x2*)(b1 + fbase), bb1 = *(const u32x2*)(b1 + fbase + 16);
    f32x4 acc[4][3];
#pragma unroll
    for (int i = 0; i < 4; ++i)
#pragma unroll
      for (int j = 0; j < 3; ++j) acc[i][j] = (f32x4){0.f, 0.f, 0.f, 0.f};
    u32x4 fb[2][3];
    u32x2 al[2][4], ah[2][4];
    u32 aA3[4];
#pragma unroll
    for (int i = 0; i < 4; ++i) aA3[i] = aA[i] + 3 * 16384;
#define G1_RDB(dst, J, SL, H) dst = rd128<12288 * (SL) + 6144 * (H) + 256 * (J)>(aB)
#define G1_RDA(I, SL, H, T) trd<16384 * ((SL) % 3) + 8192 * (H) + 1024 * (T)>((SL) < 3 ? aA[I] : aA3[I])
#define P1_M(I, J, C) do { acc[I][J] = mfma16((u32x4){al[C][I].x, al[C][I].y, ah[C][I].x, ah[C][I].y}, fb[C][J], acc[I][J]); SB0; } while (0)
#define P1_HALF(C, N, SL, H) do { \
    WL(8); P1_M(0, 0, C); G1_RDB(fb[N][0], 0, SL, H); SB0; \
    WL(8); P1_M(0, 1, C); al[N][0] = G1_RDA(0, SL, H, 0); SB0; \
    WL(8); P1_M(0, 2, C); ah[N][0] = G1_RDA(0, SL, H, 1); SB0; \
    WL(7); P1_M(1, 0, C); G1_RDB(fb[N][1], 1, SL, H); SB0; \
    P1_M(1, 1, C); G1_RDB(fb[N][2], 2, SL, H); SB0; \
    P1_M(1, 2, C); al[N][1] = G1_RDA(1, SL, H, 0); SB0; \
    WL(8); P1_M(2, 0, C); ah[N][1] = G1_RDA(1, SL, H, 1); SB0; \
    P1_M(2, 1, C); al[N][2] = G1_RDA(2, SL, H, 0); SB0; \
    P1_M(2, 2, C); ah[N][2] = G1_RDA(2, SL, H, 1); SB0; \
    WL(9); P1_M(3, 0, C); al[N][3] = G1_RDA(3, SL, H, 0); SB0; \
    P1_M(3, 1, C); ah[N][3] = G1_RDA(3, SL, H, 1); SB0; \
    P1_M(3, 2, C); } while (0)
#define P1_STEP(SL) do { BAR(); SB0; P1_HALF(0, 1, SL, 1); P1_HALF(1, 0, ((SL) + 1) % 5, 0); } while (0)
    asm volatile("s_waitcnt lgkmcnt(0)\n\ts_barrier" ::: "memory");
    G1_RDB(fb[0][0], 0, 0, 0); al[0][0] = G1_RDA(0, 0, 0, 0); ah[0][0] = G1_RDA(0, 0, 0, 1); G1_RDB(fb[0][1], 1, 0, 0); G1_RDB(fb[0][2], 2, 0, 0);
    al[0][1] = G1_RDA(1, 0, 0, 0); ah[0][1] = G1_RDA(1, 0, 0, 1); al[0][2] = G1_RDA(2, 0, 0, 0); ah[0][2] = G1_RDA(2, 0, 0, 1);
    al[0][3] = G1_RDA(3, 0, 0, 0); ah[0][3] = G1_RDA(3, 0, 0, 1);
    P1_STEP(0); P1_STEP(1);
#pragma unroll 1
    for (int t = 0; t < 6; ++t) { P1_STEP(2); P1_STEP(3); P1_STEP(4); P1_STEP(0); P1_STEP(1); }
    asm volatile("s_waitcnt lgkmcnt(0)\n\ts_barrier" ::: "memory");
    SB0;
    f32x4* ex = (f32x4*)(smem + L_EX);
#pragma unroll
    for (int ii = 0; ii < 2; ++ii)
#pragma unroll
      for (int j = 0; j < 3; ++j) ex[(wave * 6 + ii * 3 + j) * 64 + lane] = acc[2 + ii][j];
    asm volatile("s_waitcnt lgkmcnt(0)\n\ts_barrier" ::: "memory");
#pragma unroll
    for (int j = 0; j < 3; ++j) g1_store2(acc[0][j], acc[1][j], bb0, bb1, rw, goff + j * 256);
    WAITV(0);
    count_in(ws, WS_FG + (G.ft * 6 + G.dt) * 128, lane);
    BAR();
    G2_XLOAD();
    {
      const u32x4 srdW2 = make_srd(w2, (u32)((size_t)F * S * 2));
#pragma unroll 1
      for (int n = PFW2H * 3456 + ((bid >> 3) * 4 + wave) * 64 + lane; n < PFW2N * 1024; n += 27 * 256)
        touch(ldsb + L_PF, (u32)((1152 * G.kq + (n >> 4)) * (S * 2) + (bid & 1) * 2048 + (n & 15) * 128), srdW2, 0);
    }
  }
  if (!isG1) G2_XLOAD();
  BAR();
  {
    const u32 aW0 = trbase + (u32)(((wave * 4 + (lp >> 1)) ^ swz) * 16), aW1 = trbase + (u32)(((wave * 4 + 2 + (lp >> 1)) ^ swz) * 16);
    const u32 aG = ldsb + L_X + (u32)((lg * 144 + (lane & 15)) * 16);
    f32x4 acc[2][9];
#pragma unroll
    for (int i = 0; i < 2; ++i)
#pragma unroll
      for (int j = 0; j < 9; ++j) acc[i][j] = (f32x4){0.f, 0.f, 0.f, 0.f};
    u32x4 fg[2][9];
    u32x2 wl[2][2], wh[2][2];
    const u32 aG2 = aG + 2 * 18432;
#define G2_RDG(dst, J, SL, H) dst = rd128<18432 * ((SL) & 1) + 9216 * (H) + 256 * (J)>((SL) < 2 ? aG : aG2)
#define G2_RDW(a, SL, H, T) trd<16384 * (SL) + 8192 * (H) + 1024 * (T)>(a)
#define P2_M(I, J, C) do { acc[I][J] = mfma16(fg[C][J], (u32x4){wl[C][I].x, wl[C][I].y, wh[C][I].x, wh[C][I].y}, acc[I][J]); SB0; } while (0)
#define P2_HALF(C, N, SL, H) do { \
    WL(10); P2_M(0, 0, C); wl[N][0] = G2_RDW(aW0, SL, H, 0); SB0; \
    WL(9);  P2_M(1, 0, C); wh[N][0] = G2_RDW(aW0, SL, H, 1); SB0; \
    WL(9);  P2_M(0, 1, C); G2_RDG(fg[N][0], 0, SL, H); SB0; \
            P2_M(1, 1, C); wl[N][1] = G2_RDW(aW1, SL, H, 0); SB0; \
    WL(10); P2_M(0, 2, C); wh[N][1] = G2_RDW(aW1, SL, H, 1); SB0; \
            P2_M(1, 2, C); G2_RDG(fg[N][1], 1, SL, H); SB0; \
    WL(11); P2_M(0, 3, C); G2_RDG(fg[N][2], 2, SL, H); SB0; \
            P2_M(1, 3, C); G2_RDG(fg[N][3], 3, SL, H); SB0; \
    WL(12); P2_M(0, 4, C); G2_RDG(fg[N][4], 4, SL, H); SB0; \
            P2_M(1, 4, C); G2_RDG(fg[N][5], 5, SL, H); SB0; \
    WL(13); P2_M(0, 5, C); G2_RDG(fg[N][6], 6, SL, H); SB0; \
            P2_M(1, 5, C); G2_RDG(fg[N][7], 7, SL, H); SB0; \
    WL(14); P2_M(0, 6, C); G2_RDG(fg[N][8], 8, SL, H); SB0; \
            P2_M(1, 6, C); \
    WL(14); P2_M(0, 7, C); P2_M(1, 7, C); \
    WL(13); P2_M(0, 8, C); P2_M(1, 8, C); } while (0)
#define P2_BODY(SL) do { SB0; P2_HALF(0, 1, SL, 1); P2_HALF(1, 0, ((SL) + 1) & 3, 0); } while (0)
    wl[0][0] = G2_RDW(aW0, 0, 0, 0); wh[0][0] = G2_RDW(aW0, 0, 0, 1); G2_RDG(fg[0][0], 0, 0, 0); wl[0][1] = G2_RDW(aW1, 0, 0, 0); wh[0][1] = G2_RDW(aW1, 0, 0, 1);
    G2_RDG(fg[0][1], 1, 0, 0); G2_RDG(fg[0][2], 2, 0, 0); G2_RDG(fg[0][3], 3, 0, 0); G2_RDG(fg[0][4], 4, 0, 0);
    G2_RDG(fg[0][5], 5, 0, 0); G2_RDG(fg[0][6], 6, 0, 0); G2_RDG(fg[0][7], 7, 0, 0); G2_RDG(fg[0][8], 8, 0, 0);
    P2_BODY(0);
    if (G.nD == 2) BAR();
    P2_BODY(1);
#pragma unroll 1
    for (int it = 0; it < 4; ++it) { BAR(); P2_BODY(2); BAR(); P2_BODY(3); BAR(); P2_BODY(0); BAR(); P2_BODY(1); }
    BAR(); P2_BODY(2);
    if (G.nD == 3) BAR();
    P2_BODY(3);
    if (G.nD == 3) P2_BODY(0);
    asm volatile("s_waitcnt lgkmcnt(0)" ::: "memory");
    SB0;
    switch (G.kq) {
      case 0: g2_finish<0>(acc, xr, bs, ws, rw, out, G.tile, G.s0, G.d2, wave, lane); break;
      case 1: g2_finish<1>(acc, xr, bs, ws, rw, out, G.tile, G.s0, G.d2, wave, lane); break;
      case 2: g2_finish<2>(acc, xr, bs, ws, rw, out, G.tile, G.s0, G.d2, wave, lane); break;
      default: g2_finish<3>(acc, xr, bs, ws, rw, out, G.tile, G.s0, G.d2, wave, lane); break;
    }
  }
}

extern "C" void kernel_launch(void* const* d_in, const int* in_sizes, int n_in, void* d_out, int out_size, void* d_ws, size_t ws_size, hipStream_t stream) {
  (void)hipFuncSetAttribute((const void*)k2_fused, hipFuncAttributeMaxDynamicSharedMemorySize, MK_LDS);
  k1_rows<<<256, 512, 0, stream>>>((const u16*)d_in[0], (const u16*)d_in[1], (const u16*)d_in[2], (const u16*)d_in[4], (char*)d_ws);
  k2_fused<<<256, 512, MK_LDS, stream>>>((const u16*)d_in[0], (const u16*)d_in[4], (const u16*)d_in[5], (const u16*)d_in[6], (const u16*)d_in[7], (char*)d_ws, (u16*)d_out);
}
```
